```python
import torch

def _softmax(x, axis=-1):
    return torch.softmax(x, dim=axis)
def _gelu(x):
    return torch.nn.functional.gelu(x, approximate="tanh")
def _silu(x):
    return torch.nn.functional.silu(x)
def _sigmoid(x):
    return torch.sigmoid(x)
def _layernorm(x, axis=-1, eps=1e-5):
    m = x.mean(dim=axis, keepdim=True)
    v = x.var(dim=axis, keepdim=True, unbiased=False)
    return (x - m) / torch.sqrt(v + eps)
def _scatter_add(tgt, idx, src, axis=0):
    return tgt.clone().index_add_(axis, idx, src)
def _segment_sum(src, seg, n):
    out = torch.zeros((n,) + src.shape[1:], dtype=src.dtype)
    return out.index_add_(0, seg, src)
def _pad(x, pw):
    flat = tuple(v for pair in reversed(pw) for v in pair)
    return torch.nn.functional.pad(x, flat)

kernel_name = 'recurrent_scan__spectral_fnet__dense_activation_gelu__prompt'

def setup_inputs():

    _w = (lambda a: (lambda t: t.to(torch.bfloat16) if t.is_floating_point() else t)(torch.as_tensor(a)))
    def _A_setup():
        B, S, D = 1, 2048, 1024
        ns = torch.arange(S)[:, None] * torch.arange(S)[None, :]
        nd = torch.arange(D)[:, None] * torch.arange(D)[None, :]
        Ws_re = torch.cos(-2.0 * torch.pi * ns / S).to(float)
        Ws_im = torch.sin(-2.0 * torch.pi * ns / S).to(float)
        Wd_re = torch.cos(-2.0 * torch.pi * nd / D).to(float)
        Wd_im = torch.sin(-2.0 * torch.pi * nd / D).to(float)
        return {
            "x": _w((lambda *s: torch.randn(*s, dtype=torch.bfloat16) * 0.05)(B, S, D)),
            "dft_seq_re": _w(Ws_re),
            "dft_seq_im": _w(Ws_im),
            "dft_hid_re": _w(Wd_re),
            "dft_hid_im": _w(Wd_im),
        }
    _A_in = _A_setup()

    def _B_setup():
        B, S, D, F = 1, 2048, 1024, 3584
        return {
            "x": _w((lambda *s: torch.randn(*s, dtype=torch.bfloat16) * 0.05)(B, S, D)),
            "w1": _w((lambda *s: torch.randn(*s, dtype=torch.bfloat16) * 0.2)(D, F) * (D ** -0.5)),
            "b1": _w((lambda *s: torch.randn(*s, dtype=torch.bfloat16) * 3.2)(F) * 0.01),
            "w2": _w((lambda *s: torch.randn(*s, dtype=torch.bfloat16) * 0.05)(F, D) * (F ** -0.5)),
            "b2": _w((lambda *s: torch.randn(*s, dtype=torch.bfloat16) * 0.1)(D) * 0.01),
        }
    _B_in = _B_setup()

    return {
        "A_x": _A_in["x"],
        "A_dft_seq_re": _A_in["dft_seq_re"],
        "A_dft_seq_im": _A_in["dft_seq_im"],
        "A_dft_hid_re": _A_in["dft_hid_re"],
        "A_dft_hid_im": _A_in["dft_hid_im"],
        "B_x": _B_in["x"],
        "B_w1": _B_in["w1"],
        "B_b1": _B_in["b1"],
        "B_w2": _B_in["w2"],
        "B_b2": _B_in["b2"],
    }

def reference(A_x, A_dft_seq_re, A_dft_seq_im, A_dft_hid_re, A_dft_hid_im, B_x, B_w1, B_b1, B_w2, B_b2):
    def _A_ref(x, dft_seq_re, dft_seq_im, dft_hid_re, dft_hid_im):
        a = torch.einsum("bsd,ed->bse", x, dft_hid_re)
        b = torch.einsum("bsd,ed->bse", x, dft_hid_im)
        y = torch.einsum("ts,bse->bte", dft_seq_re, a) - torch.einsum("ts,bse->bte", dft_seq_im, b)
        return y

    def _B_ref(x, w1, b1, w2, b2):
        h = torch.einsum("bsd,df->bsf", x, w1) + b1
        h = _gelu(h)
        return torch.einsum("bsf,fd->bsd", h, w2) + b2

    x = A_x
    xn = _layernorm(x, axis=-1)
    s = _A_ref(xn, A_dft_seq_re, A_dft_seq_im, A_dft_hid_re, A_dft_hid_im)
    g = _silu(_B_ref(xn, B_w1, B_b1, B_w2, B_b2))
    return x + s * g
```

```cpp
#include <hip/hip_runtime.h>
#include <hip/hip_bf16.h>
typedef unsigned short u16;
__device__ __forceinline__ float bf2f(u16 v){ return __uint_as_float(((unsigned)v)<<16); }
__device__ __forceinline__ u16 f2bf(float f){ unsigned u=__float_as_uint(f); u += 0x7FFFu + ((u>>16)&1u); return (u16)(u>>16); }
constexpr int S=2048, D=1024, F=3584;

__global__ void ln_k(const u16* x, u16* xn){
  int row=blockIdx.x; int t=threadIdx.x;
  __shared__ float red[8];
  float v[4]; float s=0;
  for(int i=0;i<4;i++){ v[i]=bf2f(x[row*D+t+i*256]); s+=v[i]; }
  for(int o=32;o>=1;o>>=1) s+=__shfl_xor(s,o);
  if((t&63)==0) red[t>>6]=s; __syncthreads();
  float m=(red[0]+red[1]+red[2]+red[3])/D; __syncthreads();
  float q=0; for(int i=0;i<4;i++){ float d=v[i]-m; q+=d*d; }
  for(int o=32;o>=1;o>>=1) q+=__shfl_xor(q,o);
  if((t&63)==0) red[4+(t>>6)]=q; __syncthreads();
  float var=(red[4]+red[5]+red[6]+red[7])/D;
  float r=1.0f/sqrtf(var+1e-5f);
  for(int i=0;i<4;i++) xn[row*D+t+i*256]=f2bf((v[i]-m)*r);
}
template<int MODE>
__global__ void gemm_k(const u16* A,int lda,const u16* B,long sbk,long sbn,int M,int N,int K,float* Cf,u16* Cb,const u16* bias){
  __shared__ float As[32][33], Bs[32][33];
  int tx=threadIdx.x, ty=threadIdx.y; int n=blockIdx.x*32+tx, m=blockIdx.y*32+ty;
  float acc=0;
  for(int k0=0;k0<K;k0+=32){
    As[ty][tx]=bf2f(A[(long)m*lda+k0+tx]);
    if(sbn==1) Bs[ty][tx]=bf2f(B[(long)(k0+ty)*sbk+(long)n*sbn]);
    else Bs[tx][ty]=bf2f(B[(long)(k0+tx)*sbk+(long)(blockIdx.x*32+ty)*sbn]);
    __syncthreads();
    for(int k=0;k<32;k++) acc+=As[ty][k]*Bs[k][tx];
    __syncthreads();
  }
  if(MODE==0) Cb[(long)m*N+n]=f2bf(acc);
  if(MODE==1){ float h=acc+bf2f(bias[n]); float g=0.5f*h*(1.f+tanhf(0.7978845608028654f*(h+0.044715f*h*h*h))); Cb[(long)m*N+n]=f2bf(g);}
  if(MODE==2) Cf[(long)m*N+n]=acc;
}
__global__ void fin_k(const u16* x,const float* t1,const float* t2,const float* o,const u16* b2,u16* out){
  int i=blockIdx.x*256+threadIdx.x; int n=i%D;
  float y=t1[i]-t2[i]; float z=o[i]+bf2f(b2[n]); float g=z/(1.f+__expf(-z));
  out[i]=f2bf(bf2f(x[i])+y*g);
}
extern "C" void kernel_launch(void* const* d_in, const int* in_sizes, int n_in, void* d_out, int out_size, void* d_ws, size_t ws_size, hipStream_t stream){
  const u16* x=(const u16*)d_in[0]; const u16* Cr=(const u16*)d_in[1]; const u16* Ci=(const u16*)d_in[2];
  const u16* Dr=(const u16*)d_in[3]; const u16* Di=(const u16*)d_in[4];
  const u16* w1=(const u16*)d_in[6]; const u16* b1=(const u16*)d_in[7]; const u16* w2=(const u16*)d_in[8]; const u16* b2=(const u16*)d_in[9];
  char* ws=(char*)d_ws;
  u16* xn=(u16*)ws; u16* a=(u16*)(ws+(4<<20)); u16* b=(u16*)(ws+(8<<20)); u16* h=(u16*)(ws+(12<<20));
  float* t1=(float*)(ws+(32<<20)); float* t2=(float*)(ws+(40<<20)); float* o=(float*)(ws+(48<<20));
  ln_k<<<S,256,0,stream>>>(x,xn);
  dim3 bl(32,32);
  gemm_k<0><<<dim3(D/32,S/32),bl,0,stream>>>(xn,D,Dr,1,D,S,D,D,nullptr,a,nullptr);
  gemm_k<0><<<dim3(D/32,S/32),bl,0,stream>>>(xn,D,Di,1,D,S,D,D,nullptr,b,nullptr);
  gemm_k<1><<<dim3(F/32,S/32),bl,0,stream>>>(xn,D,w1,F,1,S,F,D,nullptr,h,b1);
  gemm_k<2><<<dim3(D/32,S/32),bl,0,stream>>>(Cr,S,a,D,1,S,D,S,t1,nullptr,nullptr);
  gemm_k<2><<<dim3(D/32,S/32),bl,0,stream>>>(Ci,S,b,D,1,S,D,S,t2,nullptr,nullptr);
  gemm_k<2><<<dim3(D/32,S/32),bl,0,stream>>>(h,F,w2,D,1,S,D,F,o,nullptr,nullptr);
  fin_k<<<S*D/256,256,0,stream>>>(x,t1,t2,o,b2,(u16*)d_out);
}
```

```cpp
#include <hip/hip_runtime.h>
#include <hip/hip_bf16.h>

typedef unsigned short u16;
typedef __bf16 bf16x8 __attribute__((ext_vector_type(8)));
typedef short s16x4 __attribute__((ext_vector_type(4)));
typedef short s16x8 __attribute__((ext_vector_type(8)));
typedef float f32x4 __attribute__((ext_vector_type(4)));
typedef float f32x2 __attribute__((ext_vector_type(2)));
typedef __bf16 bf16x2 __attribute__((ext_vector_type(2)));
typedef unsigned u32x4 __attribute__((ext_vector_type(4)));
typedef unsigned u32x2 __attribute__((ext_vector_type(2)));
#define LAS __attribute__((address_space(3)))
#define GAS __attribute__((address_space(1)))
#define DI __device__ __forceinline__

constexpr int S = 2048, D = 1024, F = 3584;

#define WAIT_V(nn) asm volatile("s_waitcnt vmcnt(%0)" :: "n"(nn) : "memory")
#define WAIT_L(n) asm volatile("s_waitcnt lgkmcnt(" #n ")" ::: "memory")
#define BAR() __builtin_amdgcn_s_barrier()
#define SCHED() __builtin_amdgcn_sched_barrier(0)

DI unsigned rfl(unsigned x) { return __builtin_amdgcn_readfirstlane(x); }
DI float bf2f(unsigned v) { return __uint_as_float(v << 16); }
DI unsigned cvtpk(float lo, float hi) { f32x2 v = {lo, hi}; bf16x2 b = __builtin_convertvector(v, bf16x2); return __builtin_bit_cast(unsigned, b); }
DI __amdgpu_buffer_rsrc_t mkrsrc(const void* p, unsigned n) { return __builtin_amdgcn_make_buffer_rsrc((void*)p, (short)0, (int)n, 0x00020000); }
#define ABL 0
DI void dma16(__amdgpu_buffer_rsrc_t r, unsigned voff, unsigned soff, unsigned m0v) {
  if (ABL & 1) return;
  asm volatile("s_mov_b32 m0, %0\n\ts_nop 0\n\tbuffer_load_dwordx4 %1, %2, %3 offen lds" ::"s"(m0v), "v"(voff), "s"(r), "s"(soff) : "memory");
}
#define PF 6
DI void touch(__amdgpu_buffer_rsrc_t r, unsigned voff, unsigned soff, unsigned& dummy) {
  if (ABL & 1) return;
  asm volatile("buffer_load_dword %0, %1, %2, %3 offen" : "+v"(dummy) : "v"(voff), "s"(r), "s"(soff) : "memory");
}
DI bf16x8 ldsd128(LAS unsigned char* p) { if (ABL & 4) { bf16x8 r; asm volatile("" : "=v"(r) : "v"(p)); return r; } return *(const LAS bf16x8*)p; }
DI bf16x8 ldstr(LAS unsigned char* p0, LAS unsigned char* p1) {
  if (ABL & 4) { bf16x8 r; asm volatile("" : "=v"(r) : "v"(p0), "v"(p1)); return r; }
  s16x4 a = __builtin_amdgcn_ds_read_tr16_b64_v4i16((LAS s16x4*)p0);
  s16x4 b = __builtin_amdgcn_ds_read_tr16_b64_v4i16((LAS s16x4*)p1);
  s16x8 c = __builtin_shufflevector(a, b, 0, 1, 2, 3, 4, 5, 6, 7);
  return __builtin_bit_cast(bf16x8, c);
}
DI f32x4 MFMA(bf16x8 a, bf16x8 b, f32x4 c) { if (ABL & 2) { asm volatile("" :: "v"(a), "v"(b)); return c; } return __builtin_amdgcn_mfma_f32_16x16x32_bf16(a, b, c, 0, 0, 0); }

#define MT(x)
#define MTNW(x)
#define ST_DECL
#define ST_ARG
#define ST_PASS
#define DBG_ARG
#define DBG_PASS
#define ST(i)
#define ST_OUT(dbg, kid, w)
__global__ __launch_bounds__(256) void k_ln(const u16* __restrict__ x, u16* __restrict__ xn, unsigned* __restrict__ flags DBG_ARG) {
  ST_DECL; ST(0);
  const int lane = threadIdx.x & 63, row = blockIdx.x * 4 + (threadIdx.x >> 6);
  if (threadIdx.x < 16) flags[blockIdx.x * 16 + threadIdx.x] = 0u;
  const u32x4* p = (const u32x4*)(x + (size_t)row * D);
  u32x4 v0 = p[lane], v1 = p[lane + 64];
  float f[16];
#pragma unroll
  for (int i = 0; i < 4; ++i) { f[2 * i] = bf2f(v0[i] & 0xffffu); f[2 * i + 1] = __uint_as_float(v0[i] & 0xffff0000u); f[8 + 2 * i] = bf2f(v1[i] & 0xffffu); f[9 + 2 * i] = __uint_as_float(v1[i] & 0xffff0000u); }
  float s = 0.f;
#pragma unroll
  for (int i = 0; i < 16; ++i) s += f[i];
#pragma unroll
  for (int o = 32; o >= 1; o >>= 1) s += __shfl_xor(s, o);
  const float m = s * (1.0f / D);
  float q = 0.f;
#pragma unroll
  for (int i = 0; i < 16; ++i) { f[i] -= m; q += f[i] * f[i]; }
#pragma unroll
  for (int o = 32; o >= 1; o >>= 1) q += __shfl_xor(q, o);
  const float r = 1.0f / sqrtf(q * (1.0f / D) + 1e-5f);
  u32x4 o0, o1;
#pragma unroll
  for (int i = 0; i < 4; ++i) { o0[i] = cvtpk(f[2 * i] * r, f[2 * i + 1] * r); o1[i] = cvtpk(f[8 + 2 * i] * r, f[9 + 2 * i] * r); }
  u32x4* po = (u32x4*)(xn + (size_t)row * D);
  po[lane] = o0; po[lane + 64] = o1;
  ST(1); ST_OUT(dbg, 0, row);
}

constexpr unsigned K1_A = 0, K1_B0 = 98304, K1_B1 = 122880, K1_SCR = 143360, K1_LDS = 144384;
constexpr int K1_NT = D / 64;

struct K1Issue {
  const unsigned char* abase; const unsigned char* bbase[3];
  unsigned voffA[2], voffB[3];
  unsigned sA[4], mA[4];
  unsigned mB[3], bvalid2;
  unsigned bstep, bslot;
  unsigned lds0;
};
DI void k1_issueA(const K1Issue& c, int tA, unsigned slotA) {
  const __amdgpu_buffer_rsrc_t r = mkrsrc(c.abase, tA < K1_NT ? 0x7fffffffu : 0u);
#pragma unroll
  for (int q = 0; q < 4; ++q) dma16(r, c.voffA[q & 1], c.sA[q] + (unsigned)tA * 128u, c.lds0 + K1_A + slotA + c.mA[q]);
}
DI void k1_issueB(const K1Issue& c, int tB) {
  const unsigned nr = tB < K1_NT ? 0x7fffffffu : 0u, so = (unsigned)tB * c.bstep, sl = (tB & 1) ? c.bslot : 0u;
  dma16(mkrsrc(c.bbase[0], nr), c.voffB[0], so, c.mB[0] + sl);
  dma16(mkrsrc(c.bbase[1], nr), c.voffB[1], so, c.mB[1] + sl);
  dma16(mkrsrc(c.bbase[2], c.bvalid2 ? nr : 0u), c.voffB[2], so, c.bvalid2 ? c.mB[2] + sl : c.lds0 + K1_SCR);
}

template <int TYPE  , int NB>
DI void k1_group(LAS unsigned char* lds, const K1Issue& c, int g, int wi, int lane, int srow0, int col0, u16* __restrict__ abT, u16* __restrict__ h, const u16* __restrict__ b1 ST_ARG) {
  const int fr = lane & 15, fq = lane >> 4;
  constexpr unsigned BRING = NB == 6 ? K1_B0 : K1_B1, BSLOT = NB * 2048u, PITCH = NB * 32u;
  const unsigned sw = (unsigned)((fr >> 1) & 7);
  const unsigned aoff0 = (unsigned)(wi * 64 + fr) * 128u + (((unsigned)fq ^ sw) << 4), aoff1 = aoff0 ^ 64u;
  unsigned boff0, boff1;
  if (TYPE == 0) { boff0 = BRING + (unsigned)fr * 128u + (((unsigned)fq ^ sw) << 4); boff1 = boff0 ^ 64u; }
  else {
    const unsigned q = (lane & 15) >> 2, p = lane & 3, g4 = lane >> 4;
    const unsigned base = BRING + (q + 4u * (g4 & 1u) + 16u * (g4 >> 1)) * PITCH + p * 8u;
    if (NB == 6) { boff0 = base + (g4 & 1u) * 32u; boff1 = base - (g4 & 1u) * 32u; } else { boff0 = base; boff1 = base; }
  }
  f32x4 acc[4][NB];
#pragma unroll
  for (int m = 0; m < 4; ++m)
#pragma unroll
    for (int n = 0; n < NB; ++n) acc[m][n] = (f32x4){0.f, 0.f, 0.f, 0.f};
  unsigned cur = 0;
  for (int t = 0; t < K1_NT; ++t) {
    bf16x8 af[4][2], bfr[NB][2];
    LAS unsigned char* pa0 = lds + aoff0 + cur; LAS unsigned char* pa1 = lds + aoff1 + cur;
    const unsigned bs = (t & 1) ? BSLOT : 0u;
    LAS unsigned char* pb0 = lds + boff0 + bs; LAS unsigned char* pb1 = lds + boff1 + bs;
#pragma unroll
    for (int m = 0; m < 4; ++m) { af[m][0] = ldsd128(pa0 + m * 2048); af[m][1] = ldsd128(pa1 + m * 2048); }
#pragma unroll
    for (int n = 0; n < NB; ++n) {
      if (TYPE == 0) { bfr[n][0] = ldsd128(pb0 + n * 2048); bfr[n][1] = ldsd128(pb1 + n * 2048); }
      else {
        LAS unsigned char* pb = (n & 1) ? pb1 : pb0;
#pragma unroll
        for (int ks = 0; ks < 2; ++ks) bfr[n][ks] = ldstr(pb + n * 32 + (32 * ks) * PITCH, pb + n * 32 + (32 * ks + 8) * PITCH);
      }
    }
    const unsigned prv = cur == 0u ? 65536u : cur - 32768u;
    k1_issueA(c, t + 2, prv);
    k1_issueB(c, g == 0 ? t + 1 : t + 2);
    WAIT_V(7); WAIT_L(0); BAR(); SCHED();
    __builtin_amdgcn_s_setprio(1);
#pragma unroll
    for (int ks = 0; ks < 2; ++ks)
#pragma unroll
      for (int n = 0; n < NB; ++n)
#pragma unroll
        for (int m = 0; m < 4; ++m) acc[m][n] = TYPE == 0 ? MFMA(af[m][ks], bfr[n][ks], acc[m][n]) : MFMA(bfr[n][ks], af[m][ks], acc[m][n]);
    __builtin_amdgcn_s_setprio(0);
    SCHED();
    if (!(g == 1 && t == K1_NT - 1)) BAR();
    SCHED();
    cur = cur == 65536u ? 0u : cur + 32768u;
  }
  ST(2);
  if (TYPE == 0) {
#pragma unroll
    for (int n = 0; n < NB; ++n)
#pragma unroll
      for (int j = 0; j < 2; ++j) {
        u32x4 w; const f32x4 a = acc[2 * j][n], b = acc[2 * j + 1][n];
        w[0] = cvtpk(a[0], a[1]); w[1] = cvtpk(a[2], a[3]); w[2] = cvtpk(b[0], b[1]); w[3] = cvtpk(b[2], b[3]);
        *(u32x4*)(abT + (size_t)(col0 + 16 * n + fr) * S + srow0 + 64 * wi + 32 * j + 8 * fq) = w;
      }
  } else {
#pragma unroll
    for (int n = 0; n < NB; ++n) {
      const u32x2 bb = *(const u32x2*)(b1 + col0 + 16 * n + 4 * fq);
      const float bv[4] = {bf2f(bb[0] & 0xffffu), __uint_as_float(bb[0] & 0xffff0000u), bf2f(bb[1] & 0xffffu), __uint_as_float(bb[1] & 0xffff0000u)};
#pragma unroll
      for (int m = 0; m < 4; ++m) {
        float gl[4];
#pragma unroll
        for (int r = 0; r < 4; ++r) {
          const float v = acc[m][n][r] + bv[r];
          const float u = v * (1.0f + 0.044715f * v * v) * (-2.0f * 0.7978845608028654f * 1.4426950408889634f);
          gl[r] = v * __builtin_amdgcn_rcpf(1.0f + __builtin_amdgcn_exp2f(u));
        }
        u32x2 w; w[0] = cvtpk(gl[0], gl[1]); w[1] = cvtpk(gl[2], gl[3]);
        const int srow = srow0 + 64 * wi + 32 * (m >> 1) + 8 * (fr >> 2) + 4 * (m & 1) + (fr & 3);
        *(u32x2*)(h + (size_t)srow * F + col0 + 16 * n + 4 * fq) = w;
      }
    }
  }
}

__global__ __launch_bounds__(512, 2) void k_stage1(const u16* __restrict__ xn, const u16* __restrict__ Dr, const u16* __restrict__ Di, const u16* __restrict__ w1,
                                                   const u16* __restrict__ b1, u16* __restrict__ abT, u16* __restrict__ h DBG_ARG) {
  ST_DECL; ST(0);
  extern __shared__ __attribute__((aligned(16))) unsigned char smem[];
  LAS unsigned char* lds = (LAS unsigned char*)smem;
  const int bid = blockIdx.x, xcd = bid & 7, j = bid >> 3;
  const int pm = (xcd & 1) * 4 + (j & 3), ct = (xcd >> 1) * 8 + (j >> 2);
  const int tid = threadIdx.x, wid = (int)rfl(tid >> 6), lane = tid & 63, g = wid >> 2, wi = wid & 3;
  const int type0 = ct < 13 ? 0 : 1, col00 = ct < 13 ? 96 * ct : 96 * (ct - 13);
  const int type1 = ct < 10 ? 0 : 1, col01 = ct < 10 ? 1248 + 80 * ct : 1824 + 80 * (ct - 10);
  K1Issue c;
  c.lds0 = (unsigned)(size_t)lds;
  c.abase = (const unsigned char*)(xn + (size_t)pm * 256 * D);
  const unsigned i = (unsigned)lane;
#pragma unroll
  for (int par = 0; par < 2; ++par) c.voffA[par] = (8u * (i >> 5) + ((i >> 3) & 3u)) * (D * 2u) + (((i & 7u) ^ (i >> 4) ^ ((unsigned)par << 2)) << 4);
#pragma unroll
  for (int q = 0; q < 4; ++q) { const unsigned p = (unsigned)(g * 16 + wi * 4 + q); c.sA[q] = (16u * (p & 1u) + 4u * ((p >> 1) & 1u) + 32u * (p >> 2)) * (D * 2u); c.mA[q] = p * 1024u; }
  const int otype = g == 0 ? type1 : type0, ocol = g == 0 ? col01 : col00;
  const unsigned cpr = g == 0 ? 10u : 12u, oring = g == 0 ? K1_B1 : K1_B0;
  c.bslot = cpr * 1024u; c.bstep = otype == 0 ? 128u : 64u * F * 2u; c.bvalid2 = (unsigned)(wi + 8) < cpr;
#pragma unroll
  for (int q = 0; q < 3; ++q) {
    const unsigned p = (unsigned)(wi + 4 * q);
    c.mB[q] = c.lds0 + oring + p * 1024u;
    if (otype == 0) {
      int e0 = ocol + 8 * (int)p; if (e0 > 2040) e0 = 2040;
      c.bbase[q] = (const unsigned char*)(e0 < 1024 ? Dr + (size_t)e0 * D : Di + (size_t)(e0 - 1024) * D);
      c.voffB[q] = (i >> 3) * (D * 2u) + (((i & 7u) ^ (i >> 4) ^ ((p & 1u) << 2)) << 4);
    } else {
      const unsigned ci = 64u * p + i, rho = ci / cpr, pos = ci - rho * cpr, sp = pos >> 1;
      const unsigned seg = cpr == 12u ? sp ^ ((rho >> 2) & 1u) : sp;
      const unsigned k = (rho & 3u) + 8u * ((rho >> 2) & 1u) + 4u * ((rho >> 3) & 1u) + (rho & 48u);
      c.bbase[q] = (const unsigned char*)(w1 + ocol);
      c.voffB[q] = k * (F * 2u) + seg * 32u + (pos & 1u) * 16u;
    }
  }
  k1_issueA(c, 0, 0u);
  if (g == 1) k1_issueB(c, 0);
  k1_issueA(c, 1, 32768u);
  k1_issueB(c, g == 0 ? 0 : 1);
  WAIT_V(7); BAR();
  if (g == 1) BAR();
  SCHED();
  const int srow0 = pm * 256;
  ST(1);
  if (g == 0) {
    if (type0 == 0) k1_group<0, 6>(lds, c, 0, wi, lane, srow0, col00, abT, h, b1 ST_PASS);
    else k1_group<1, 6>(lds, c, 0, wi, lane, srow0, col00, abT, h, b1 ST_PASS);
  } else {
    if (type1 == 0) k1_group<0, 5>(lds, c, 1, wi, lane, srow0, col01, abT, h, b1 ST_PASS);
    else k1_group<1, 5>(lds, c, 1, wi, lane, srow0, col01, abT, h, b1 ST_PASS);
  }
}

constexpr unsigned K2_A0 = 0, K2_A1 = 49152, K2_B = 98304, K2_LDS = 147456;

template <bool TR>
DI void k2_main(LAS unsigned char* lds, const unsigned char* Abase, unsigned lda_b, const unsigned char* Bbase, int NT, int g, int wi, int lane, int pm4, int pn8, f32x4 (&acc)[4][4], unsigned (&fst)[6] ST_ARG) {
  const unsigned lds0 = (unsigned)(size_t)lds, i = (unsigned)lane;
  const int fr = lane & 15, fq = lane >> 4, wmi = wi >> 1, wn = wi & 1;
  const unsigned sw = (unsigned)((fr >> 1) & 7);
  unsigned voffA[2], voffBig[2], sBig[4], sSm[2];
#pragma unroll
  for (int par = 0; par < 2; ++par) voffA[par] = (i >> 3) * lda_b + (((i & 7u) ^ (i >> 4) ^ ((unsigned)par << 2)) << 4);
  const unsigned char* bigbase; unsigned bigstep, bigring;
  if (g == 0) {
    bigbase = Bbase; bigring = lds0 + K2_B;
    if (TR) {
      bigstep = 64u * D * 2u;
#pragma unroll
      for (int par = 0; par < 2; ++par) voffBig[par] = (i >> 4) * (D * 2u) + ((((i & 15u) >> 1) ^ ((i >> 4) + 4u * par)) << 5) + (i & 1u) * 16u;
#pragma unroll
      for (int q = 0; q < 4; ++q) { const unsigned p = (unsigned)(4 * wi + q); sBig[q] = (8u * (p & 1u) + 4u * ((p >> 1) & 1u) + 16u * (p >> 2)) * (D * 2u); }
    } else {
      bigstep = 128u;
#pragma unroll
      for (int par = 0; par < 2; ++par) voffBig[par] = (i >> 3) * (S * 2u) + (((i & 7u) ^ (i >> 4) ^ ((unsigned)par << 2)) << 4);
#pragma unroll
      for (int q = 0; q < 4; ++q) sBig[q] = (unsigned)(4 * wi + q) * 8u * (S * 2u);
    }
  } else {
    bigbase = Abase; bigring = lds0 + K2_A1; bigstep = 128u;
#pragma unroll
    for (int par = 0; par < 2; ++par) voffBig[par] = voffA[par];
#pragma unroll
    for (int q = 0; q < 4; ++q) sBig[q] = (128u + (unsigned)(4 * wi + q) * 8u) * lda_b;
  }
  const unsigned psm = (unsigned)((g == 0 ? 8 : 0) + 2 * wi);
#pragma unroll
  for (int q = 0; q < 2; ++q) sSm[q] = (psm + q) * 8u * lda_b;
  const unsigned mBig = bigring + (unsigned)wi * 4096u, mSm = lds0 + K2_A0 + psm * 1024u;
  auto issue_big = [&](int tile, unsigned slot) {
    const __amdgpu_buffer_rsrc_t r = mkrsrc(bigbase, tile < NT ? 0x7fffffffu : 0u);
#pragma unroll
    for (int q = 0; q < 4; ++q) dma16(r, voffBig[q & 1], sBig[q] + (unsigned)tile * bigstep, mBig + slot + q * 1024u);
  };
  auto issue_sm = [&](int tile, unsigned slot) {
    const __amdgpu_buffer_rsrc_t r = mkrsrc(Abase, tile < NT ? 0x7fffffffu : 0u);
#pragma unroll
    for (int q = 0; q < 2; ++q) dma16(r, voffA[q], sSm[q] + (unsigned)tile * 128u, mSm + slot + q * 1024u);
  };
  const unsigned aoff0 = (g == 0 ? K2_A0 : K2_A1) + (unsigned)(wmi * 64 + fr) * 128u + (((unsigned)fq ^ sw) << 4), aoff1 = aoff0 ^ 64u;
  unsigned boff[4];
  if (TR) {
    const unsigned q = (lane & 15) >> 2, p = lane & 3, g4 = lane >> 4, s8 = q + 4u * (g4 & 1u);
#pragma unroll
    for (int n = 0; n < 4; ++n) boff[n] = K2_B + (s8 + 16u * (g4 >> 1)) * 256u + ((((unsigned)(wn * 4 + n)) ^ s8) << 5) + p * 8u;
  } else {
    boff[0] = K2_B + (unsigned)(wn * 64 + fr) * 128u + (((unsigned)fq ^ sw) << 4); boff[1] = boff[0] ^ 64u; boff[2] = boff[3] = 0;
  }
  unsigned dummy = 0;
  const unsigned char* tchbase; unsigned tchvoff, tchstep;
  if (g == 0) { tchbase = Abase; tchvoff = (unsigned)(pn8 * 32 + wi * 8 + (lane & 7)) * lda_b; tchstep = 128u; }
  else if (!TR) { tchbase = Bbase; tchvoff = (unsigned)(pm4 * 32 + wi * 8 + (lane & 7)) * (S * 2u); tchstep = 128u; }
  else { tchbase = Bbase; tchvoff = (unsigned)(pm4 * 16 + wi * 4 + ((lane & 7) >> 1)) * (D * 2u) + (lane & 1) * 128u; tchstep = 64u * D * 2u; }
  constexpr int NPT = PF > 2 ? PF - 2 : 0;
  auto ptouch = [&]() {
#pragma unroll
    for (int t = 2; t < PF; ++t) touch(mkrsrc(tchbase, 0x7fffffffu), tchvoff, (unsigned)t * tchstep, dummy);
  };
  if (g == 0) {
    issue_big(0, 0u); issue_sm(0, 0u); issue_big(1, 16384u); issue_sm(1, 16384u); ptouch();
    WAIT_V(6 + NPT); BAR();
  } else {
    issue_sm(0, 0u); issue_big(0, 0u); issue_sm(1, 16384u); issue_big(1, 16384u); issue_sm(2, 32768u); ptouch();
    WAIT_V(12 + NPT); BAR();
    WAIT_V(6 + NPT); BAR();
  }
  SCHED();
  ST(1);
  unsigned cur = 0;
  for (int t = 0; t < NT; ++t) {
    bf16x8 af[4][2], bfr[4][2];
    MT(q0);
    LAS unsigned char* pa0 = lds + aoff0 + cur; LAS unsigned char* pa1 = lds + aoff1 + cur;
    const unsigned prv = cur == 0u ? 32768u : cur - 16384u;
    const __amdgpu_buffer_rsrc_t rb = mkrsrc(bigbase, t + 2 < NT ? 0x7fffffffu : 0u);
    const int tsm = g == 0 ? t + 2 : t + 3; const unsigned ssm = g == 0 ? prv : cur;
    const __amdgpu_buffer_rsrc_t rs = mkrsrc(Abase, tsm < NT ? 0x7fffffffu : 0u);
    LAS unsigned char* pb0 = lds + boff[0] + cur; LAS unsigned char* pb1 = lds + boff[1] + cur;
#pragma unroll
    for (int q = 0; q < 4; ++q) {
      dma16(rb, voffBig[q & 1], sBig[q] + (unsigned)(t + 2) * bigstep, mBig + prv + q * 1024u);
      af[q][0] = ldsd128(pa0 + q * 2048); af[q][1] = ldsd128(pa1 + q * 2048);
      if (q == 3) break;
      if (TR) { LAS unsigned char* pb = lds + boff[q] + cur; bfr[q][0] = ldstr(pb, pb + 2048); }
      else bfr[q][0] = ldsd128(pb0 + q * 2048);
    }
#pragma unroll
    for (int q = 0; q < 2; ++q) {
      dma16(rs, voffA[q], sSm[q] + (unsigned)tsm * 128u, mSm + ssm + q * 1024u);
      if (q == 0) {
        if (TR) { LAS unsigned char* pb = lds + boff[3] + cur; bfr[3][0] = ldstr(pb, pb + 2048); }
        else bfr[3][0] = ldsd128(pb0 + 3 * 2048);
      }
#pragma unroll
      for (int n = 2 * q; n < 2 * q + 2; ++n) {
        if (TR) { LAS unsigned char* pb = lds + boff[n] + cur; bfr[n][1] = ldstr(pb + 8192, pb + 8192 + 2048); }
        else bfr[n][1] = ldsd128(pb1 + n * 2048);
      }
    }
    touch(mkrsrc(tchbase, t + PF < NT ? 0x7fffffffu : 0u), tchvoff, (unsigned)(t + PF) * tchstep, dummy);
    MTNW(q1);
    WAIT_L(0);
    MT(q2);
    BAR(); SCHED();
    MT(q3);
    __builtin_amdgcn_s_setprio(1);
#pragma unroll
    for (int ks = 0; ks < 2; ++ks)
#pragma unroll
      for (int n = 0; n < 4; ++n)
#pragma unroll
        for (int m = 0; m < 4; ++m) acc[m][n] = MFMA(bfr[n][ks], af[m][ks], acc[m][n]);
    __builtin_amdgcn_s_setprio(0);
    SCHED();
    MT(q4);
    WAIT_V(8);
    MT(q5);
    if (!(g == 1 && t == NT - 1)) BAR();
    SCHED();
    cur = cur == 32768u ? 0u : cur + 16384u;
  }
  WAIT_V(0);
  asm volatile("" :: "v"(dummy));
}

__global__ __launch_bounds__(512, 2) void k_stage2(const u16* __restrict__ Cr, const u16* __restrict__ Ci, const u16* __restrict__ abT, const u16* __restrict__ h,
                                                   const u16* __restrict__ w2, const u16* __restrict__ x, const u16* __restrict__ b2, u16* __restrict__ out,
                                                   float* xbuf, unsigned* flags DBG_ARG) {
  ST_DECL; ST(0);
  extern __shared__ __attribute__((aligned(16))) unsigned char smem[];
  LAS unsigned char* lds = (LAS unsigned char*)smem;
  const int bid = blockIdx.x, xcd = bid & 7, j = bid >> 3;
  const int role = xcd >> 1, pm = (xcd & 1) * 4 + (j & 3), pn = j >> 2;
  const int tid = threadIdx.x, wid = (int)rfl(tid >> 6), lane = tid & 63, g = wid >> 2, wi = wid & 3;
  f32x4 acc[4][4]; unsigned fst[6] = {0, 0, 0, 0, 0, 0};
#pragma unroll
  for (int m = 0; m < 4; ++m)
#pragma unroll
    for (int n = 0; n < 4; ++n) acc[m][n] = (f32x4){0.f, 0.f, 0.f, 0.f};
  if (role < 2) {
    const u16* A = (role ? Ci : Cr) + (size_t)pm * 256 * S;
    const u16* B = abT + (size_t)(role * 1024 + pn * 128) * S;
    k2_main<false>(lds, (const unsigned char*)A, S * 2u, (const unsigned char*)B, S / 64, g, wi, lane, j & 3, pn, acc, fst ST_PASS);
  } else {
    const int k0 = (role - 2) * (F / 2);
    const u16* A = h + (size_t)pm * 256 * F + k0;
    const u16* B = w2 + (size_t)k0 * D + pn * 128;
    k2_main<true>(lds, (const unsigned char*)A, F * 2u, (const unsigned char*)B, F / 128, g, wi, lane, j & 3, pn, acc, fst ST_PASS);
  }
  const int tile = pm * 8 + pn;
  ST(2);
  const __amdgpu_buffer_rsrc_t xr = mkrsrc(xbuf + (size_t)tile * (4 * 8 * 16 * 256), 4u * 8u * 16u * 1024u);
#pragma unroll
  for (int m = 0; m < 4; ++m)
    if (m != role) {
#pragma unroll
      for (int n = 0; n < 4; ++n)
        __builtin_amdgcn_raw_buffer_store_b128(__builtin_bit_cast(u32x4, acc[m][n]), xr, lane * 16, ((role * 8 + wid) * 16 + m * 4 + n) * 1024, 16);
    }
  ST(3);
  WAIT_V(0);
  ST(4);
  GAS unsigned* fl = (GAS unsigned*)flags + (tile * 8 + wid) * 16;
  if (lane == 0) __hip_atomic_store(fl + role, 1u, __ATOMIC_RELAXED, __HIP_MEMORY_SCOPE_AGENT);
  f32x4 own[4];
#pragma unroll
  for (int n = 0; n < 4; ++n) own[n] = role == 0 ? acc[0][n] : role == 1 ? acc[1][n] : role == 2 ? acc[2][n] : acc[3][n];
  const int fr = lane & 15, fq = lane >> 4, wmi = wi >> 1, wn = wi & 1;
  const int trow = pm * 256 + g * 128 + wmi * 64 + role * 16 + fr, ecol = pn * 128 + wn * 64 + 4 * fq;
  u32x2 xv[4], bv[4];
#pragma unroll
  for (int n = 0; n < 4; ++n) { xv[n] = *(const u32x2*)(x + (size_t)trow * D + ecol + 16 * n); bv[n] = *(const u32x2*)(b2 + ecol + 16 * n); }
  {
    const __amdgpu_buffer_rsrc_t fr_ = mkrsrc((const void*)fl, 16u);
    unsigned spins = 0;
    for (;;) {
      const u32x4 f = __builtin_bit_cast(u32x4, __builtin_amdgcn_raw_buffer_load_b128(fr_, 0, 0, 16));
      const unsigned all = rfl(f[0] & f[1] & f[2] & f[3]);
      if (all == 1u || ++spins > (1u << 22)) break;
      __builtin_amdgcn_s_sleep(1);
    }
  }
  asm volatile("" ::: "memory");
  ST(5);
  f32x4 part[4][4];
#pragma unroll
  for (int r = 0; r < 4; ++r)
#pragma unroll
    for (int n = 0; n < 4; ++n)
      part[r][n] = r == role ? own[n] : __builtin_bit_cast(f32x4, __builtin_amdgcn_raw_buffer_load_b128(xr, lane * 16, ((r * 8 + wid) * 16 + role * 4 + n) * 1024, 16));
#pragma unroll
  for (int n = 0; n < 4; ++n) {
    const float xb[4] = {bf2f(xv[n][0] & 0xffffu), __uint_as_float(xv[n][0] & 0xffff0000u), bf2f(xv[n][1] & 0xffffu), __uint_as_float(xv[n][1] & 0xffff0000u)};
    const float bb[4] = {bf2f(bv[n][0] & 0xffffu), __uint_as_float(bv[n][0] & 0xffff0000u), bf2f(bv[n][1] & 0xffffu), __uint_as_float(bv[n][1] & 0xffff0000u)};
    float o[4];
#pragma unroll
    for (int r = 0; r < 4; ++r) {
      const float y = part[0][n][r] - part[1][n][r], z = part[2][n][r] + part[3][n][r] + bb[r];
      const float gz = z * __builtin_amdgcn_rcpf(1.0f + __builtin_amdgcn_exp2f(-1.4426950408889634f * z));
      o[r] = xb[r] + y * gz;
    }
    u32x2 w; w[0] = cvtpk(o[0], o[1]); w[1] = cvtpk(o[2], o[3]);
    *(u32x2*)(out + (size_t)trow * D + ecol + 16 * n) = w;
  }
}

extern "C" void kernel_launch(void* const* d_in, const int* in_sizes, int n_in, void* d_out, int out_size, void* d_ws, size_t ws_size, hipStream_t stream) {
  const u16* x = (const u16*)d_in[0]; const u16* Cr = (const u16*)d_in[1]; const u16* Ci = (const u16*)d_in[2];
  const u16* Dr = (const u16*)d_in[3]; const u16* Di = (const u16*)d_in[4];
  const u16* w1 = (const u16*)d_in[6]; const u16* b1 = (const u16*)d_in[7]; const u16* w2 = (const u16*)d_in[8]; const u16* b2 = (const u16*)d_in[9];
  char* ws = (char*)d_ws;
  unsigned* flags = (unsigned*)ws;
  u16* xn = (u16*)(ws + (1 << 20));
  u16* abT = (u16*)(ws + (8 << 20));
  u16* h = (u16*)(ws + (16 << 20));
  float* xbuf = (float*)(ws + (32 << 20));
  (void)hipFuncSetAttribute((const void*)k_stage1, hipFuncAttributeMaxDynamicSharedMemorySize, K1_LDS);
  (void)hipFuncSetAttribute((const void*)k_stage2, hipFuncAttributeMaxDynamicSharedMemorySize, K2_LDS);
  k_ln<<<S / 4, 256, 0, stream>>>(x, xn, flags DBG_PASS);
  k_stage1<<<256, 512, K1_LDS, stream>>>(xn, Dr, Di, w1, b1, abT, h DBG_PASS);
  k_stage2<<<256, 512, K2_LDS, stream>>>(Cr, Ci, abT, h, w2, x, b2, (u16*)d_out, xbuf, flags DBG_PASS);
}
```

```cpp
#include <hip/hip_runtime.h>
#include <hip/hip_bf16.h>

typedef unsigned short u16;
typedef __bf16 bf16x8 __attribute__((ext_vector_type(8)));
typedef short s16x4 __attribute__((ext_vector_type(4)));
typedef short s16x8 __attribute__((ext_vector_type(8)));
typedef float f32x4 __attribute__((ext_vector_type(4)));
typedef float f32x2 __attribute__((ext_vector_type(2)));
typedef __bf16 bf16x2 __attribute__((ext_vector_type(2)));
typedef unsigned u32x4 __attribute__((ext_vector_type(4)));
typedef unsigned u32x2 __attribute__((ext_vector_type(2)));
#define LAS __attribute__((address_space(3)))
#define GAS __attribute__((address_space(1)))
#define DI __device__ __forceinline__

constexpr int S = 2048, D = 1024, F = 3584;

#define LEAD1 4
#define LEAD2 4
#define LNKT 6
#define WARM2 0
#define STAUX 0

#define WAIT_V(nn) asm volatile("s_waitcnt vmcnt(%0)" ::"n"(nn) : "memory")
#define WAIT_L(n) asm volatile("s_waitcnt lgkmcnt(" #n ")" ::: "memory")
#define BAR() __builtin_amdgcn_s_barrier()
#define SCHED() __builtin_amdgcn_sched_barrier(0)

DI unsigned rfl(unsigned x) { return __builtin_amdgcn_readfirstlane(x); }
DI float bflo(unsigned v) { return __uint_as_float(v << 16); }
DI float bfhi(unsigned v) { return __uint_as_float(v & 0xffff0000u); }
DI unsigned cvtpk(float lo, float hi) { f32x2 v = {lo, hi}; bf16x2 b = __builtin_convertvector(v, bf16x2); return __builtin_bit_cast(unsigned, b); }
DI __amdgpu_buffer_rsrc_t mkrsrc(const void* p, unsigned n) { return __builtin_amdgcn_make_buffer_rsrc((void*)p, (short)0, (int)n, 0x00020000); }
DI void dma16(__amdgpu_buffer_rsrc_t r, unsigned voff, unsigned soff, unsigned m0v) {
  asm volatile("s_mov_b32 m0, %0\n\ts_nop 0\n\tbuffer_load_dwordx4 %1, %2, %3 offen lds" ::"s"(m0v), "v"(voff), "s"(r), "s"(soff) : "memory");
}
DI void touch(__amdgpu_buffer_rsrc_t r, unsigned voff, unsigned soff, unsigned& dummy) {
  asm volatile("buffer_load_dword %0, %1, %2, %3 offen" : "+v"(dummy) : "v"(voff), "s"(r), "s"(soff) : "memory");
}
DI bf16x8 ldsd128(LAS unsigned char* p) { return *(const LAS bf16x8*)p; }
DI bf16x8 ldstr(LAS unsigned char* p0, LAS unsigned char* p1) {
  s16x4 a = __builtin_amdgcn_ds_read_tr16_b64_v4i16((LAS s16x4*)p0);
  s16x4 b = __builtin_amdgcn_ds_read_tr16_b64_v4i16((LAS s16x4*)p1);
  s16x8 c = __builtin_shufflevector(a, b, 0, 1, 2, 3, 4, 5, 6, 7);
  return __builtin_bit_cast(bf16x8, c);
}
#define MFMA(a, b, c) __builtin_amdgcn_mfma_f32_16x16x32_bf16(a, b, c, 0, 0, 0)

#define ST_DECL
#define ST_ARG
#define ST_PASS
#define DBG_ARG
#define DBG_PASS
#define ST(i)
#define STC(i)
#define ST_OUT(dbg, kid, w)

__global__ __launch_bounds__(256) void k_ln(const u16* __restrict__ x, u16* __restrict__ xn, unsigned* __restrict__ flags,
                                            const u16* __restrict__ Dr, const u16* __restrict__ Di, const u16* __restrict__ w1 DBG_ARG) {
  ST_DECL; ST(0);
  const int lane = threadIdx.x & 63, row = blockIdx.x * 4 + (threadIdx.x >> 6);
  const u32x4* p = (const u32x4*)(x + (size_t)row * D);
  u32x4 v0 = p[lane], v1 = p[lane + 64];
  unsigned tv = 0;
  if (LNKT > 0) {
    constexpr int NL = (11 * LNKT + 3) / 4, ND = 2048 * LNKT, NTOT = 5632 * LNKT;
    int L = row * NL + lane;
    if (lane < NL && L < NTOT) {
      const unsigned* q;
      if (L < ND) { const int r = L / LNKT, kt = L - r * LNKT; q = (const unsigned*)((r < 1024 ? Dr + (size_t)r * D : Di + (size_t)(r - 1024) * D) + kt * 64); }
      else q = (const unsigned*)(w1 + (size_t)(L - ND) * 64);
      tv = *q;
    }
  }
  if (threadIdx.x < 16) flags[blockIdx.x * 16 + threadIdx.x] = 0u;
  float f[16];
#pragma unroll
  for (int i = 0; i < 4; ++i) { f[2 * i] = bflo(v0[i]); f[2 * i + 1] = bfhi(v0[i]); f[8 + 2 * i] = bflo(v1[i]); f[9 + 2 * i] = bfhi(v1[i]); }
  float s = 0.f;
#pragma unroll
  for (int i = 0; i < 16; ++i) s += f[i];
#pragma unroll
  for (int o = 32; o >= 1; o >>= 1) s += __shfl_xor(s, o);
  const float m = s * (1.0f / D);
  float q = 0.f;
#pragma unroll
  for (int i = 0; i < 16; ++i) { f[i] -= m; q += f[i] * f[i]; }
#pragma unroll
  for (int o = 32; o >= 1; o >>= 1) q += __shfl_xor(q, o);
  const float r = 1.0f / sqrtf(q * (1.0f / D) + 1e-5f);
  u32x4 o0, o1;
#pragma unroll
  for (int i = 0; i < 4; ++i) { o0[i] = cvtpk(f[2 * i] * r, f[2 * i + 1] * r); o1[i] = cvtpk(f[8 + 2 * i] * r, f[9 + 2 * i] * r); }
  u32x4* po = (u32x4*)(xn + (size_t)row * D);
  po[lane] = o0; po[lane + 64] = o1;
  asm volatile("" ::"v"(tv));
  ST(1); ST_OUT(dbg, 0, row);
}

constexpr unsigned K1_A = 0, K1_B0 = 98304, K1_B1 = 122880, K1_SCR = 143360, K1_LDS = 144384;
constexpr int K1_NT = D / 64;

struct K1Issue {
  const unsigned char* abase; const unsigned char* bbase[3];
  unsigned voffA[2], voffB[3];
  unsigned sA[4], mA;
  unsigned mB, m2, bvalid2;
  unsigned bstep, bslot;
  int leadmask;
  const unsigned char* wc; const unsigned char* ww; int wstep;
};
DI unsigned k1_phys(const K1Issue& c, int t) { return (unsigned)(t + (((c.leadmask >> (t & 3)) & 1) * LEAD1)) & (unsigned)(K1_NT - 1); }
struct K1Step { __amdgpu_buffer_rsrc_t ra, rb0, rb1, rb2; unsigned ka, kb, sa, sb, m2; };
DI K1Step k1_prep(const K1Issue& c, int tA, unsigned slotA, int tB) {
  K1Step s; const unsigned nb = tB < K1_NT ? 0x7fffffffu : 0u;
  s.ra = mkrsrc(c.abase, tA < K1_NT ? 0x7fffffffu : 0u);
  s.rb0 = mkrsrc(c.bbase[0], nb); s.rb1 = mkrsrc(c.bbase[1], nb); s.rb2 = mkrsrc(c.bbase[2], c.bvalid2 ? nb : 0u);
  s.ka = k1_phys(c, tA) * 128u; s.kb = k1_phys(c, tB) * c.bstep; s.sa = c.mA + slotA; s.sb = c.mB + ((tB & 1) ? c.bslot : 0u);
  s.m2 = c.bvalid2 ? s.sb + 8192u : c.m2;
  return s;
}
template <int Q> DI void k1_dma(const K1Issue& c, const K1Step& s) {
  if (Q < 4) dma16(s.ra, c.voffA[Q & 1], c.sA[Q & 3] + s.ka, s.sa + (Q & 3) * 1024u);
  else if (Q == 4) dma16(s.rb0, c.voffB[0], s.kb, s.sb);
  else if (Q == 5) dma16(s.rb1, c.voffB[1], s.kb, s.sb + 4096u);
  else dma16(s.rb2, c.voffB[2], s.kb, s.m2);
}

template <int TYPE  , int NB>
DI void k1_group(LAS unsigned char* lds, const K1Issue& c, int g, int wi, int lane, int srow0, int col0, u16* __restrict__ abT, u16* __restrict__ h, const u16* __restrict__ b1 ST_ARG) {
  const int fr = lane & 15, fq = lane >> 4;
  constexpr unsigned BRING = NB == 6 ? K1_B0 : K1_B1, BSLOT = NB * 2048u, PITCH = NB * 32u;
  const unsigned sw = (unsigned)((fr >> 1) & 7);
  const unsigned aoff0 = (unsigned)(wi * 64 + fr) * 128u + (((unsigned)fq ^ sw) << 4), aoff1 = aoff0 ^ 64u;
  unsigned boff0, boff1;
  if (TYPE == 0) { boff0 = BRING + (unsigned)fr * 128u + (((unsigned)fq ^ sw) << 4); boff1 = boff0 ^ 64u; }
  else {
    const unsigned q = (lane & 15) >> 2, p = lane & 3, g4 = lane >> 4;
    const unsigned base = BRING + (q + 4u * (g4 & 1u) + 16u * (g4 >> 1)) * PITCH + p * 8u;
    if (NB == 6) { boff0 = base + (g4 & 1u) * 32u; boff1 = base - (g4 & 1u) * 32u; } else { boff0 = base; boff1 = base; }
  }
  f32x4 acc[4][NB];
#pragma unroll
  for (int m = 0; m < 4; ++m)
#pragma unroll
    for (int n = 0; n < NB; ++n) acc[m][n] = (f32x4){0.f, 0.f, 0.f, 0.f};
  unsigned dummy = 0;
  unsigned cur = 0;
  for (int t = 0; t < K1_NT; ++t) {
    bf16x8 af[4][2], bfr[NB][2];
    LAS unsigned char* pa0 = lds + aoff0 + cur; LAS unsigned char* pa1 = lds + aoff1 + cur;
    const unsigned bs = (t & 1) ? BSLOT : 0u;
    LAS unsigned char* pb0 = lds + boff0 + bs; LAS unsigned char* pb1 = lds + boff1 + bs;
    const unsigned prv = cur == 0u ? 65536u : cur - 32768u;
    const K1Step st = k1_prep(c, t + 2, prv, t + 1 + g);
    auto rdB = [&](int n) {
      if (TYPE == 0) { bfr[n][0] = ldsd128(pb0 + n * 2048); bfr[n][1] = ldsd128(pb1 + n * 2048); }
      else {
        LAS unsigned char* pb = (n & 1) ? pb1 : pb0;
#pragma unroll
        for (int ks = 0; ks < 2; ++ks) bfr[n][ks] = ldstr(pb + n * 32 + (32 * ks) * PITCH, pb + n * 32 + (32 * ks + 8) * PITCH);
      }
    };
    auto rdA = [&](int m) { af[m][0] = ldsd128(pa0 + m * 2048); af[m][1] = ldsd128(pa1 + m * 2048); };
    k1_dma<4>(c, st); rdA(0); rdA(1);
    k1_dma<5>(c, st); rdA(2); rdA(3);
    k1_dma<6>(c, st); rdB(0);
    k1_dma<0>(c, st); rdB(1);
    k1_dma<1>(c, st); rdB(2);
    k1_dma<2>(c, st); rdB(3);
    k1_dma<3>(c, st); rdB(4);
    if (NB == 6) rdB(5);
    if (WARM2) {
      if (t == c.wstep) {
        touch(mkrsrc(c.wc, 8192u), (unsigned)lane * 128u, 0u, dummy);
        touch(mkrsrc(c.ww, 3584u), (unsigned)lane * 128u, 0u, dummy);
      }
      if (t == c.wstep || t == c.wstep + 1) WAIT_V(9); else WAIT_V(7);
    } else WAIT_V(7);
    WAIT_L(0); BAR(); SCHED();
    __builtin_amdgcn_s_setprio(1);
#pragma unroll
    for (int ks = 0; ks < 2; ++ks)
#pragma unroll
      for (int n = 0; n < NB; ++n)
#pragma unroll
        for (int m = 0; m < 4; ++m) acc[m][n] = TYPE == 0 ? MFMA(af[m][ks], bfr[n][ks], acc[m][n]) : MFMA(bfr[n][ks], af[m][ks], acc[m][n]);
    __builtin_amdgcn_s_setprio(0);
    SCHED();
    if (!(g == 1 && t == K1_NT - 1)) BAR();
    SCHED();
    cur = cur == 65536u ? 0u : cur + 32768u;
  }
  ST(2); STC(6);
  if (TYPE == 0) {
    const __amdgpu_buffer_rsrc_t ro = mkrsrc(abT + (size_t)col0 * S + srow0 + 64 * wi, 0x7fffffffu);
    const unsigned vo = (unsigned)fr * (S * 2u) + (unsigned)fq * 16u;
#pragma unroll
    for (int n = 0; n < NB; ++n)
#pragma unroll
      for (int j = 0; j < 2; ++j) {
        u32x4 w; const f32x4 a = acc[2 * j][n], b = acc[2 * j + 1][n];
        w[0] = cvtpk(a[0], a[1]); w[1] = cvtpk(a[2], a[3]); w[2] = cvtpk(b[0], b[1]); w[3] = cvtpk(b[2], b[3]);
        __builtin_amdgcn_raw_buffer_store_b128(w, ro, vo + n * 16 * (S * 2) + j * 64, 0, STAUX);
      }
  } else {
    u32x2 bb[NB];
#pragma unroll
    for (int n = 0; n < NB; ++n) bb[n] = *(const u32x2*)(b1 + col0 + 16 * n + 4 * fq);
    const __amdgpu_buffer_rsrc_t ro = mkrsrc(h + (size_t)(srow0 + 64 * wi) * F + col0, 0x7fffffffu);
    const unsigned vrow = (unsigned)(8 * (fr >> 2) + (fr & 3)) * (F * 2u);
    const unsigned vo16 = vrow + (unsigned)(fq & 1) * 32u + (unsigned)(fq >> 1) * 16u, vo8 = vrow + (unsigned)fq * 8u;
    auto gelu4 = [&](const f32x4& a, const u32x2& b) -> u32x2 {
      const float bv[4] = {bflo(b[0]), bfhi(b[0]), bflo(b[1]), bfhi(b[1])};
      float gl[4];
#pragma unroll
      for (int r = 0; r < 4; ++r) {
        const float v = a[r] + bv[r];
        const float u = v * __builtin_fmaf(v * v, -0.044715f * 2.0f * 0.7978845608028654f * 1.4426950408889634f, -2.0f * 0.7978845608028654f * 1.4426950408889634f);
        gl[r] = v * __builtin_amdgcn_rcpf(1.0f + __builtin_amdgcn_exp2f(u));
      }
      u32x2 w; w[0] = cvtpk(gl[0], gl[1]); w[1] = cvtpk(gl[2], gl[3]); return w;
    };
#pragma unroll
    for (int m = 0; m < 4; ++m) {
      const int so = (32 * (m >> 1) + 4 * (m & 1)) * (F * 2);
#pragma unroll
      for (int n = 0; n + 1 < NB; n += 2) {
        u32x2 a = gelu4(acc[m][n], bb[n]), b = gelu4(acc[m][n + 1], bb[n + 1]);
        auto r0 = __builtin_amdgcn_permlane16_swap(a[0], b[0], false, false);
        auto r1 = __builtin_amdgcn_permlane16_swap(a[1], b[1], false, false);
        u32x4 w; w[0] = r0[0]; w[1] = r1[0]; w[2] = r0[1]; w[3] = r1[1];
        __builtin_amdgcn_raw_buffer_store_b128(w, ro, vo16 + so + n * 32, 0, STAUX);
      }
      if (NB & 1) {
        u32x2 a = gelu4(acc[m][NB - 1], bb[NB - 1]);
        __builtin_amdgcn_raw_buffer_store_b64(a, ro, vo8 + so + (NB - 1) * 32, 0, STAUX);
      }
    }
  }
  WAIT_V(0);
  asm volatile("" ::"v"(dummy));
}

__global__ __launch_bounds__(512, 2) void k_stage1(const u16* __restrict__ xn, const u16* __restrict__ Dr, const u16* __restrict__ Di, const u16* __restrict__ w1,
                                                   const u16* __restrict__ b1, u16* __restrict__ abT, u16* __restrict__ h,
                                                   const u16* __restrict__ Cr, const u16* __restrict__ Ci, const u16* __restrict__ w2 DBG_ARG) {
  ST_DECL; ST(0);
  extern __shared__ __attribute__((aligned(16))) unsigned char smem[];
  LAS unsigned char* lds = (LAS unsigned char*)smem;
  const int bid = blockIdx.x, xcd = bid & 7, j = bid >> 3;
  const int pm = (xcd & 1) * 4 + (j & 3), ct = (xcd >> 1) * 8 + (j >> 2);
  const int tid = threadIdx.x, wid = (int)rfl(tid >> 6), lane = tid & 63, g = wid >> 2, wi = wid & 3;
  const int type0 = ct < 13 ? 0 : 1, col00 = ct < 13 ? 96 * ct : 96 * (ct - 13);
  const int type1 = ct < 10 ? 0 : 1, col01 = ct < 10 ? 1248 + 80 * ct : 1824 + 80 * (ct - 10);
  K1Issue c;
  const unsigned lds0 = (unsigned)(size_t)lds;
  c.abase = (const unsigned char*)(xn + (size_t)pm * 256 * D);
  c.leadmask = 1 << (((j & 3) + (j >> 2)) & 3);
  const unsigned i = (unsigned)lane;
#pragma unroll
  for (int par = 0; par < 2; ++par) c.voffA[par] = (8u * (i >> 5) + ((i >> 3) & 3u)) * (D * 2u) + (((i & 7u) ^ (i >> 4) ^ ((unsigned)par << 2)) << 4);
#pragma unroll
  for (int q = 0; q < 4; ++q) { const unsigned p = (unsigned)(g * 16 + wi * 4 + q); c.sA[q] = (16u * (p & 1u) + 4u * ((p >> 1) & 1u) + 32u * (p >> 2)) * (D * 2u); }
  c.mA = lds0 + K1_A + (unsigned)(g * 16 + wi * 4) * 1024u;
  const int otype = g == 0 ? type1 : type0, ocol = g == 0 ? col01 : col00;
  const unsigned cpr = g == 0 ? 10u : 12u, oring = g == 0 ? K1_B1 : K1_B0;
  c.bslot = cpr * 1024u; c.bstep = otype == 0 ? 128u : 64u * F * 2u; c.bvalid2 = (unsigned)(wi + 8) < cpr;
  c.mB = lds0 + oring + (unsigned)wi * 1024u; c.m2 = lds0 + K1_SCR;
#pragma unroll
  for (int q = 0; q < 3; ++q) {
    const unsigned p = (unsigned)(wi + 4 * q);
    if (otype == 0) {
      int e0 = ocol + 8 * (int)p; if (e0 > 2040) e0 = 2040;
      c.bbase[q] = (const unsigned char*)(e0 < 1024 ? Dr + (size_t)e0 * D : Di + (size_t)(e0 - 1024) * D);
      c.voffB[q] = (i >> 3) * (D * 2u) + (((i & 7u) ^ (i >> 4) ^ ((p & 1u) << 2)) << 4);
    } else {
      const unsigned ci = 64u * p + i, rho = ci / cpr, pos = ci - rho * cpr, sp = pos >> 1;
      const unsigned seg = cpr == 12u ? sp ^ ((rho >> 2) & 1u) : sp;
      const unsigned k = (rho & 3u) + 8u * ((rho >> 2) & 1u) + 4u * ((rho >> 3) & 1u) + (rho & 48u);
      c.bbase[q] = (const unsigned char*)(w1 + ocol);
      c.voffB[q] = k * (F * 2u) + seg * 32u + (pos & 1u) * 16u;
    }
  }
  {
    const int gw = bid * 8 + wid;
    c.wc = (const unsigned char*)(gw < 1024 ? Cr : Ci) + (size_t)(gw & 1023) * 8192;
    c.ww = (const unsigned char*)w2 + (size_t)gw * 3584;
    c.wstep = 1 + (bid >> 3) % 12;
  }
  {
    const K1Step s0 = k1_prep(c, 0, 0u, 0);
    k1_dma<0>(c, s0); k1_dma<1>(c, s0); k1_dma<2>(c, s0); k1_dma<3>(c, s0);
    if (g == 1) { k1_dma<4>(c, s0); k1_dma<5>(c, s0); k1_dma<6>(c, s0); }
    const K1Step s1 = k1_prep(c, 1, 32768u, g);
    k1_dma<4>(c, s1); k1_dma<5>(c, s1); k1_dma<6>(c, s1);
    k1_dma<0>(c, s1); k1_dma<1>(c, s1); k1_dma<2>(c, s1); k1_dma<3>(c, s1);
  }
  WAIT_V(7); BAR();
  if (g == 1) BAR();
  SCHED();
  const int srow0 = pm * 256;
  ST(1); STC(5);
  if (g == 0) {
    if (type0 == 0) k1_group<0, 6>(lds, c, 0, wi, lane, srow0, col00, abT, h, b1 ST_PASS);
    else k1_group<1, 6>(lds, c, 0, wi, lane, srow0, col00, abT, h, b1 ST_PASS);
  } else {
    if (type1 == 0) k1_group<0, 5>(lds, c, 1, wi, lane, srow0, col01, abT, h, b1 ST_PASS);
    else k1_group<1, 5>(lds, c, 1, wi, lane, srow0, col01, abT, h, b1 ST_PASS);
  }
}

constexpr unsigned K2_A0 = 0, K2_A1 = 49152, K2_B = 98304, K2_LDS = 147456;

template <bool TR>
DI void k2_main(LAS unsigned char* lds, const unsigned char* Abase, unsigned lda_b, const unsigned char* Bbase, int NT, int g, int wi, int lane, int pm4, int pn8, f32x4 (&acc)[4][4] ST_ARG) {
  const unsigned lds0 = (unsigned)(size_t)lds, i = (unsigned)lane;
  const int fr = lane & 15, fq = lane >> 4, wmi = wi >> 1, wn = wi & 1;
  const unsigned sw = (unsigned)((fr >> 1) & 7);
  unsigned voffA[2], voffBig[2], sBig[4], sSm[2];
#pragma unroll
  for (int par = 0; par < 2; ++par) voffA[par] = (i >> 3) * lda_b + (((i & 7u) ^ (i >> 4) ^ ((unsigned)par << 2)) << 4);
  const unsigned char* bigbase; unsigned bigstep, bigring;
  if (g == 0) {
    bigbase = Bbase; bigring = lds0 + K2_B;
    if (TR) {
      bigstep = 64u * D * 2u;
#pragma unroll
      for (int par = 0; par < 2; ++par) voffBig[par] = (i >> 4) * (D * 2u) + ((((i & 15u) >> 1) ^ ((i >> 4) + 4u * par)) << 5) + (i & 1u) * 16u;
#pragma unroll
      for (int q = 0; q < 4; ++q) { const unsigned p = (unsigned)(4 * wi + q); sBig[q] = (8u * (p & 1u) + 4u * ((p >> 1) & 1u) + 16u * (p >> 2)) * (D * 2u); }
    } else {
      bigstep = 128u;
#pragma unroll
      for (int par = 0; par < 2; ++par) voffBig[par] = (i >> 3) * (S * 2u) + (((i & 7u) ^ (i >> 4) ^ ((unsigned)par << 2)) << 4);
#pragma unroll
      for (int q = 0; q < 4; ++q) sBig[q] = (unsigned)(4 * wi + q) * 8u * (S * 2u);
    }
  } else {
    bigbase = Abase; bigring = lds0 + K2_A1; bigstep = 128u;
#pragma unroll
    for (int par = 0; par < 2; ++par) voffBig[par] = voffA[par];
#pragma unroll
    for (int q = 0; q < 4; ++q) sBig[q] = (128u + (unsigned)(4 * wi + q) * 8u) * lda_b;
  }
  const unsigned psm = (unsigned)((g == 0 ? 8 : 0) + 2 * wi);
#pragma unroll
  for (int q = 0; q < 2; ++q) sSm[q] = (psm + q) * 8u * lda_b;
  const unsigned mBig = bigring + (unsigned)wi * 4096u, mSm = lds0 + K2_A0 + psm * 1024u;
  const int phase = (pm4 + pn8) & 3;
  auto phys = [&](int tile) -> unsigned { int p = tile; if (LEAD2 > 0 && (tile & 3) == phase) { p += LEAD2; if (p >= NT) p -= NT; } return (unsigned)p; };
  auto issue_big = [&](int tile, unsigned slot) {
    const __amdgpu_buffer_rsrc_t r = mkrsrc(bigbase, 0x7fffffffu);
#pragma unroll
    for (int q = 0; q < 4; ++q) dma16(r, voffBig[q & 1], sBig[q] + phys(tile) * bigstep, mBig + slot + q * 1024u);
  };
  auto issue_sm = [&](int tile, unsigned slot) {
    const __amdgpu_buffer_rsrc_t r = mkrsrc(Abase, 0x7fffffffu);
#pragma unroll
    for (int q = 0; q < 2; ++q) dma16(r, voffA[q], sSm[q] + phys(tile) * 128u, mSm + slot + q * 1024u);
  };
  if (g == 0) { issue_big(0, 0u); issue_sm(0, 0u); issue_big(1, 16384u); issue_sm(1, 16384u); }
  else { issue_sm(0, 0u); issue_big(0, 0u); issue_sm(1, 16384u); issue_big(1, 16384u); issue_sm(2, 32768u); }
  unsigned dummy = 0;
  constexpr int NPT = LEAD2 > 2 ? LEAD2 - 2 : 0;
  {
    const unsigned char* tchbase; unsigned tchvoff, tchstep;
    if (g == 0) { tchbase = Abase; tchvoff = (unsigned)(pn8 * 32 + wi * 8 + (lane & 7)) * lda_b; tchstep = 128u; }
    else if (!TR) { tchbase = Bbase; tchvoff = (unsigned)(pm4 * 32 + wi * 8 + (lane & 7)) * (S * 2u); tchstep = 128u; }
    else { tchbase = Bbase; tchvoff = (unsigned)(pm4 * 16 + wi * 4 + ((lane & 7) >> 1)) * (D * 2u) + (lane & 1) * 128u; tchstep = 64u * D * 2u; }
#pragma unroll
    for (int t = 2; t < LEAD2; ++t) touch(mkrsrc(tchbase, 0x7fffffffu), tchvoff, (unsigned)t * tchstep, dummy);
  }
  const unsigned aoff0 = (g == 0 ? K2_A0 : K2_A1) + (unsigned)(wmi * 64 + fr) * 128u + (((unsigned)fq ^ sw) << 4), aoff1 = aoff0 ^ 64u;
  unsigned boff[4];
  if (TR) {
    const unsigned q = (lane & 15) >> 2, p = lane & 3, g4 = lane >> 4, s8 = q + 4u * (g4 & 1u);
#pragma unroll
    for (int n = 0; n < 4; ++n) boff[n] = K2_B + (s8 + 16u * (g4 >> 1)) * 256u + ((((unsigned)(wn * 4 + n)) ^ s8) << 5) + p * 8u;
  } else {
    boff[0] = K2_B + (unsigned)(wn * 64 + fr) * 128u + (((unsigned)fq ^ sw) << 4); boff[1] = boff[0] ^ 64u; boff[2] = boff[3] = 0;
  }
  if (g == 0) { WAIT_V(6 + NPT); BAR(); }
  else { WAIT_V(12 + NPT); BAR(); WAIT_V(6 + NPT); BAR(); }
  SCHED();
  ST(1);
  unsigned cur = 0;
  for (int t = 0; t < NT; ++t) {
    bf16x8 af[4][2], bfr[4][2];
    LAS unsigned char* pa0 = lds + aoff0 + cur; LAS unsigned char* pa1 = lds + aoff1 + cur;
    const unsigned prv = cur == 0u ? 32768u : cur - 16384u;
    const __amdgpu_buffer_rsrc_t rb = mkrsrc(bigbase, t + 2 < NT ? 0x7fffffffu : 0u);
    const int tsm = g == 0 ? t + 2 : t + 3; const unsigned ssm = g == 0 ? prv : cur;
    const __amdgpu_buffer_rsrc_t rs = mkrsrc(Abase, tsm < NT ? 0x7fffffffu : 0u);
    const unsigned kb = phys(t + 2) * bigstep, ks_ = phys(tsm) * 128u;
    LAS unsigned char* pb0 = lds + boff[0] + cur; LAS unsigned char* pb1 = lds + boff[1] + cur;
#pragma unroll
    for (int q = 0; q < 4; ++q) {
      dma16(rb, voffBig[q & 1], sBig[q] + kb, mBig + prv + q * 1024u);
      af[q][0] = ldsd128(pa0 + q * 2048); af[q][1] = ldsd128(pa1 + q * 2048);
      if (q == 3) break;
      if (TR) { LAS unsigned char* pb = lds + boff[q] + cur; bfr[q][0] = ldstr(pb, pb + 2048); }
      else bfr[q][0] = ldsd128(pb0 + q * 2048);
    }
#pragma unroll
    for (int q = 0; q < 2; ++q) {
      dma16(rs, voffA[q], sSm[q] + ks_, mSm + ssm + q * 1024u);
      if (q == 0) {
        if (TR) { LAS unsigned char* pb = lds + boff[3] + cur; bfr[3][0] = ldstr(pb, pb + 2048); }
        else bfr[3][0] = ldsd128(pb0 + 3 * 2048);
      }
#pragma unroll
      for (int n = 2 * q; n < 2 * q + 2; ++n) {
        if (TR) { LAS unsigned char* pb = lds + boff[n] + cur; bfr[n][1] = ldstr(pb + 8192, pb + 8192 + 2048); }
        else bfr[n][1] = ldsd128(pb1 + n * 2048);
      }
    }
    WAIT_L(0); BAR(); SCHED();
    __builtin_amdgcn_s_setprio(1);
#pragma unroll
    for (int ks = 0; ks < 2; ++ks)
#pragma unroll
      for (int n = 0; n < 4; ++n)
#pragma unroll
        for (int m = 0; m < 4; ++m) acc[m][n] = MFMA(bfr[n][ks], af[m][ks], acc[m][n]);
    __builtin_amdgcn_s_setprio(0);
    SCHED();
    WAIT_V(6);
    if (!(g == 1 && t == NT - 1)) BAR();
    SCHED();
    cur = cur == 32768u ? 0u : cur + 16384u;
  }
  WAIT_V(0);
  asm volatile("" ::"v"(dummy));
}

__global__ __launch_bounds__(512, 2) void k_stage2(const u16* __restrict__ Cr, const u16* __restrict__ Ci, const u16* __restrict__ abT, const u16* __restrict__ h,
                                                   const u16* __restrict__ w2, const u16* __restrict__ x, const u16* __restrict__ b2, u16* __restrict__ out,
                                                   unsigned char* xbuf, unsigned* flags DBG_ARG) {
  ST_DECL; ST(0);
  extern __shared__ __attribute__((aligned(16))) unsigned char smem[];
  LAS unsigned char* lds = (LAS unsigned char*)smem;
  const int bid = blockIdx.x, xcd = bid & 7, j = bid >> 3;
  const int role = xcd >> 1, pm = (xcd & 1) * 4 + (j & 3), pn = j >> 2;
  const int tid = threadIdx.x, wid = (int)rfl(tid >> 6), lane = tid & 63, g = wid >> 2, wi = wid & 3;
  f32x4 acc[4][4];
#pragma unroll
  for (int m = 0; m < 4; ++m)
#pragma unroll
    for (int n = 0; n < 4; ++n) acc[m][n] = (f32x4){0.f, 0.f, 0.f, 0.f};
  if (role < 2) {
    const u16* A = (role ? Ci : Cr) + (size_t)pm * 256 * S;
    const u16* B = abT + (size_t)(role * 1024 + pn * 128) * S;
    k2_main<false>(lds, (const unsigned char*)A, S * 2u, (const unsigned char*)B, S / 64, g, wi, lane, j & 3, pn, acc ST_PASS);
  } else {
    const int k0 = (role - 2) * (F / 2);
    const u16* A = h + (size_t)pm * 256 * F + k0;
    const u16* B = w2 + (size_t)k0 * D + pn * 128;
    k2_main<true>(lds, (const unsigned char*)A, F * 2u, (const unsigned char*)B, F / 128, g, wi, lane, j & 3, pn, acc ST_PASS);
  }
  const int tile = pm * 8 + pn;
  ST(2);
  const __amdgpu_buffer_rsrc_t xr = mkrsrc(xbuf + (size_t)tile * (4 * 8 * 8 * 1024), 4u * 8u * 8u * 1024u);
  const __amdgpu_buffer_rsrc_t xw = mkrsrc(xbuf + (size_t)tile * (4 * 8 * 8 * 1024) + (role * 8 + wid) * 8192, 8192u);
  GAS unsigned* fl = (GAS unsigned*)flags + (tile * 8 + wid) * 16;
  const __amdgpu_buffer_rsrc_t frs = mkrsrc((const void*)fl, 16u);
  const int fr = lane & 15, fq = lane >> 4, wmi = wi >> 1, wn = wi & 1;
  const int trow = pm * 256 + g * 128 + wmi * 64 + role * 16 + fr, ecol = pn * 128 + wn * 64 + 4 * fq;
#pragma unroll
  for (int m = 0; m < 4; ++m)
    if (m != role) {
#pragma unroll
      for (int pr = 0; pr < 2; ++pr) {
        const f32x4 a = acc[m][2 * pr], b = acc[m][2 * pr + 1];
        u32x4 w; w[0] = cvtpk(a[0], a[1]); w[1] = cvtpk(a[2], a[3]); w[2] = cvtpk(b[0], b[1]); w[3] = cvtpk(b[2], b[3]);
        __builtin_amdgcn_raw_buffer_store_b128(w, xw, lane * 16 + (m * 2 + pr) * 1024, 0, 16);
      }
    }
  u32x4 f0 = __builtin_bit_cast(u32x4, __builtin_amdgcn_raw_buffer_load_b128(frs, 0, 0, 16));
  u32x2 xv[4], bv[4];
#pragma unroll
  for (int n = 0; n < 4; ++n) { xv[n] = *(const u32x2*)(x + (size_t)trow * D + ecol + 16 * n); bv[n] = *(const u32x2*)(b2 + ecol + 16 * n); }
  f32x4 own[4];
#pragma unroll
  for (int n = 0; n < 4; ++n) own[n] = role == 0 ? acc[0][n] : role == 1 ? acc[1][n] : role == 2 ? acc[2][n] : acc[3][n];
  ST(3);
  WAIT_V(0);
  ST(4);
  if (lane == 0) __hip_atomic_store(fl + role, 1u, __ATOMIC_RELAXED, __HIP_MEMORY_SCOPE_AGENT);
  u32x4 pk[4][2];
#pragma unroll
  for (int r = 0; r < 4; ++r) { pk[r][0] = (u32x4){0u, 0u, 0u, 0u}; pk[r][1] = (u32x4){0u, 0u, 0u, 0u}; }
  unsigned have = 1u << role;
  for (unsigned spins = 0;;) {
    const unsigned rdy = (rfl(f0[0]) == 1u ? 1u : 0u) | (rfl(f0[1]) == 1u ? 2u : 0u) | (rfl(f0[2]) == 1u ? 4u : 0u) | (rfl(f0[3]) == 1u ? 8u : 0u);
    const unsigned nw = rdy & ~have;
    asm volatile("" ::: "memory");
#pragma unroll
    for (int r = 0; r < 4; ++r)
      if (nw & (1u << r)) {
#pragma unroll
        for (int pr = 0; pr < 2; ++pr) pk[r][pr] = __builtin_bit_cast(u32x4, __builtin_amdgcn_raw_buffer_load_b128(xr, lane * 16, ((r * 8 + wid) * 8 + role * 2 + pr) * 1024, 16));
      }
    have |= nw;
    if (have == 15u || ++spins > (1u << 22)) break;
    if (!nw) __builtin_amdgcn_s_sleep(1);
    f0 = __builtin_bit_cast(u32x4, __builtin_amdgcn_raw_buffer_load_b128(frs, 0, 0, 16));
  }
  ST(5);
#pragma unroll
  for (int n = 0; n < 4; ++n) {
    const float xb[4] = {bflo(xv[n][0]), bfhi(xv[n][0]), bflo(xv[n][1]), bfhi(xv[n][1])};
    const float bb[4] = {bflo(bv[n][0]), bfhi(bv[n][0]), bflo(bv[n][1]), bfhi(bv[n][1])};
    float pv[4][4];
#pragma unroll
    for (int r = 0; r < 4; ++r) {
      const unsigned w0 = pk[r][n >> 1][(n & 1) * 2], w1 = pk[r][n >> 1][(n & 1) * 2 + 1];
      pv[r][0] = r == role ? own[n][0] : bflo(w0); pv[r][1] = r == role ? own[n][1] : bfhi(w0);
      pv[r][2] = r == role ? own[n][2] : bflo(w1); pv[r][3] = r == role ? own[n][3] : bfhi(w1);
    }
    float o[4];
#pragma unroll
    for (int e = 0; e < 4; ++e) {
      const float y = pv[0][e] - pv[1][e], z = pv[2][e] + pv[3][e] + bb[e];
      const float gz = z * __builtin_amdgcn_rcpf(1.0f + __builtin_amdgcn_exp2f(-1.4426950408889634f * z));
      o[e] = xb[e] + y * gz;
    }
    u32x2 w; w[0] = cvtpk(o[0], o[1]); w[1] = cvtpk(o[2], o[3]);
    *(u32x2*)(out + (size_t)trow * D + ecol + 16 * n) = w;
  }
}

extern "C" void kernel_launch(void* const* d_in, const int* in_sizes, int n_in, void* d_out, int out_size, void* d_ws, size_t ws_size, hipStream_t stream) {
  const u16* x = (const u16*)d_in[0]; const u16* Cr = (const u16*)d_in[1]; const u16* Ci = (const u16*)d_in[2];
  const u16* Dr = (const u16*)d_in[3]; const u16* Di = (const u16*)d_in[4];
  const u16* w1 = (const u16*)d_in[6]; const u16* b1 = (const u16*)d_in[7]; const u16* w2 = (const u16*)d_in[8]; const u16* b2 = (const u16*)d_in[9];
#define WSEND 0
  char* ws = (char*)d_ws + (WSEND ? ((ws_size - (48u << 20)) & ~(size_t)0xfffff) : 0);
  unsigned* flags = (unsigned*)ws;
  u16* xn = (u16*)(ws + (1 << 20));
  u16* abT = (u16*)(ws + (8 << 20));
  u16* h = (u16*)(ws + (16 << 20));
  unsigned char* xbuf = (unsigned char*)(ws + (32 << 20));
  (void)hipFuncSetAttribute((const void*)k_stage1, hipFuncAttributeMaxDynamicSharedMemorySize, K1_LDS);
  (void)hipFuncSetAttribute((const void*)k_stage2, hipFuncAttributeMaxDynamicSharedMemorySize, K2_LDS);
  k_ln<<<S / 4, 256, 0, stream>>>(x, xn, flags, Dr, Di, w1 DBG_PASS);
  k_stage1<<<256, 512, K1_LDS, stream>>>(xn, Dr, Di, w1, b1, abT, h, Cr, Ci, w2 DBG_PASS);
  k_stage2<<<256, 512, K2_LDS, stream>>>(Cr, Ci, abT, h, w2, x, b2, (u16*)d_out, xbuf, flags DBG_PASS);
}
```

```cpp
#include <hip/hip_runtime.h>
#include <hip/hip_bf16.h>

typedef unsigned short u16;
typedef __bf16 bf16x8 __attribute__((ext_vector_type(8)));
typedef short s16x4 __attribute__((ext_vector_type(4)));
typedef short s16x8 __attribute__((ext_vector_type(8)));
typedef float f32x4 __attribute__((ext_vector_type(4)));
typedef float f32x2 __attribute__((ext_vector_type(2)));
typedef __bf16 bf16x2 __attribute__((ext_vector_type(2)));
typedef unsigned u32x4 __attribute__((ext_vector_type(4)));
typedef unsigned u32x2 __attribute__((ext_vector_type(2)));
#define LAS __attribute__((address_space(3)))
#define GAS __attribute__((address_space(1)))
#define DI __device__ __forceinline__

constexpr int S = 2048, D = 1024, F = 3584;

#define LEAD1 4
#define LEAD2 4
#define LEADT 4
#define LNKT 6
#define PREIN 0
#define XSLEEP 4
#define WARMC 0
#define SWARM 0
#define PREBAR 1
#define DBAL 2

#define CTMAP(xcd, j) (DBAL == 2 ? ((xcd) >> 1) + 4 * ((j) >> 2) : (DBAL && (xcd) < 4) ? 2 * ((j) >> 2) + ((xcd) >> 1) : ((xcd) >> 1) * 8 + ((j) >> 2))
#define WAIT_V(nn) asm volatile("s_waitcnt vmcnt(%0)" ::"n"(nn) : "memory")
#define WAIT_L(n) do { asm volatile("" ::: "memory"); __builtin_amdgcn_s_waitcnt(0xC07F); asm volatile("" ::: "memory"); } while (0)
#define BAR() __builtin_amdgcn_s_barrier()
#define SCHED() __builtin_amdgcn_sched_barrier(0)

DI unsigned rfl(unsigned x) { return __builtin_amdgcn_readfirstlane(x); }
DI float bflo(unsigned v) { return __uint_as_float(v << 16); }
DI float bfhi(unsigned v) { return __uint_as_float(v & 0xffff0000u); }
DI unsigned cvtpk(float lo, float hi) { f32x2 v = {lo, hi}; bf16x2 b = __builtin_convertvector(v, bf16x2); return __builtin_bit_cast(unsigned, b); }
DI unsigned cvtpk2(f32x2 v) { bf16x2 b = __builtin_convertvector(v, bf16x2); return __builtin_bit_cast(unsigned, b); }
DI __amdgpu_buffer_rsrc_t mkrsrc(const void* p, unsigned n) { return __builtin_amdgcn_make_buffer_rsrc((void*)p, (short)0, (int)n, 0x00020000); }
DI u32x4 mksrd(const void* p, unsigned n) { const unsigned long long a = (unsigned long long)p; u32x4 r; r[0] = (unsigned)a; r[1] = (unsigned)(a >> 32) & 0xffffu; r[2] = n; r[3] = 0x00020000u; return r; }
DI void dma_m0(const u32x4& r, unsigned voff, unsigned soff, unsigned m0v) {
  asm volatile("s_mov_b32 m0, %0\n\ts_nop 0\n\tbuffer_load_dwordx4 %1, %2, %3 offen lds" ::"s"(m0v), "v"(voff), "s"(r), "s"(soff) : "memory");
}
template <int OFF> DI void dma_off(const u32x4& r, unsigned voff, unsigned soff) {
  asm volatile("buffer_load_dwordx4 %0, %1, %2 offen offset:%3 lds" ::"v"(voff), "s"(r), "s"(soff), "n"(OFF) : "memory");
}
DI void touch(const u32x4& r, unsigned voff, unsigned soff, unsigned& dummy) {
  asm volatile("buffer_load_dword %0, %1, %2, %3 offen" : "+v"(dummy) : "v"(voff), "s"(r), "s"(soff) : "memory");
}
DI bf16x8 ldsd128(unsigned a) { return *(const LAS bf16x8*)(size_t)a; }
DI bf16x8 ldstr(unsigned a0, unsigned a1) {
  s16x4 a = __builtin_amdgcn_ds_read_tr16_b64_v4i16((LAS s16x4*)(size_t)a0);
  s16x4 b = __builtin_amdgcn_ds_read_tr16_b64_v4i16((LAS s16x4*)(size_t)a1);
  s16x8 c = __builtin_shufflevector(a, b, 0, 1, 2, 3, 4, 5, 6, 7);
  return __builtin_bit_cast(bf16x8, c);
}
#define MFMA(a, b, c) __builtin_amdgcn_mfma_f32_16x16x32_bf16(a, b, c, 0, 0, 0)
template <int V> struct IC { static constexpr int value = V; };
template <int N> DI void mfma_shadow() {
#pragma unroll
  for (int i = 0; i < N; ++i) { __builtin_amdgcn_sched_group_barrier(0x8, 1, 0); __builtin_amdgcn_sched_group_barrier(0x6, 2, 0); }
}

#define ST_DECL
#define ST_ARG
#define ST_PASS
#define DBG_ARG
#define DBG_PASS
#define ST(i)
#define STC(i)
#define ST_OUT(dbg, kid, w)

__global__ __launch_bounds__(256) void k_ln(const u16* __restrict__ x, u16* __restrict__ xn, unsigned* __restrict__ flags,
                                            const u16* __restrict__ Dr, const u16* __restrict__ Di, const u16* __restrict__ w1 DBG_ARG) {
  ST_DECL; ST(0);
  const int lane = threadIdx.x & 63, row = blockIdx.x * 4 + (threadIdx.x >> 6);
  const u32x4* p = (const u32x4*)(x + (size_t)row * D);
  u32x4 v0 = p[lane], v1 = p[lane + 64];
  unsigned tv = 0;
  if (LNKT > 0) {
    constexpr int NL = (11 * LNKT + 3) / 4, ND = 2048 * LNKT, NTOT = 5632 * LNKT;
    int L = row * NL + lane;
    if (lane < NL && L < NTOT) {
      const unsigned* q;
      if (L < ND) { const int r = L / (LNKT > 0 ? LNKT : 1), kt = L - r * LNKT; q = (const unsigned*)((r < 1024 ? Dr + (size_t)r * D : Di + (size_t)(r - 1024) * D) + kt * 64); }
      else q = (const unsigned*)(w1 + (size_t)(L - ND) * 64);
      tv = *q;
    }
  }
  if (threadIdx.x < 32) flags[blockIdx.x * 32 + threadIdx.x] = 0u;
  float f[16];
#pragma unroll
  for (int i = 0; i < 4; ++i) { f[2 * i] = bflo(v0[i]); f[2 * i + 1] = bfhi(v0[i]); f[8 + 2 * i] = bflo(v1[i]); f[9 + 2 * i] = bfhi(v1[i]); }
  float s = 0.f;
#pragma unroll
  for (int i = 0; i < 16; ++i) s += f[i];
#pragma unroll
  for (int o = 32; o >= 1; o >>= 1) s += __shfl_xor(s, o);
  const float m = s * (1.0f / D);
  float q = 0.f;
#pragma unroll
  for (int i = 0; i < 16; ++i) { f[i] -= m; q += f[i] * f[i]; }
#pragma unroll
  for (int o = 32; o >= 1; o >>= 1) q += __shfl_xor(q, o);
  const float r = 1.0f / sqrtf(q * (1.0f / D) + 1e-5f);
  u32x4 o0, o1;
#pragma unroll
  for (int i = 0; i < 4; ++i) { o0[i] = cvtpk(f[2 * i] * r, f[2 * i + 1] * r); o1[i] = cvtpk(f[8 + 2 * i] * r, f[9 + 2 * i] * r); }
  u32x4* po = (u32x4*)(xn + (size_t)row * D);
  po[lane] = o0; po[lane + 64] = o1;
  asm volatile("" ::"v"(tv));
  ST(1); ST_OUT(dbg, 0, row);
}

constexpr unsigned K1_B0 = 98304, K1_B1 = 122880, K1_BSLOT = 12288, KM_LDS = 147456;
constexpr unsigned K2_R0 = 32768, K2_R1 = 65536, K2_R2 = 0, K2_Q0 = 98304, K2_Q1 = 114688, K2_Q2 = 131072;
#define KBSV 32768
constexpr unsigned KBS = KBSV;
constexpr int K1_NT = D / 64;
constexpr int NPT = LEAD2 > 2 ? LEAD2 - 2 : 0;
constexpr int CNT_D = 512 * 16, CNT_M = CNT_D + 16 * 16;

struct K1Issue {
  u32x4 ra, rb[3];
  unsigned voffA[2], voffB[3];
  unsigned sA[4];
  unsigned mA, mB, bstep;
  int leadmask;
  u32x4 wc, ww;
};
struct K1Step { unsigned sa[4], kb, ma, mb; };
DI unsigned k1_phys(const K1Issue& c, int t) { return (unsigned)(t + (((c.leadmask >> (t & 3)) & 1) * LEAD1)) & (unsigned)(K1_NT - 1); }
DI K1Step k1_prep(const K1Issue& c, int tA, unsigned slotA, int tB) {
  K1Step s; const unsigned ka = k1_phys(c, tA) * 128u;
#pragma unroll
  for (int q = 0; q < 4; ++q) s.sa[q] = c.sA[q] + ka;
  s.kb = k1_phys(c, tB) * c.bstep; s.ma = c.mA + slotA; s.mb = c.mB + ((tB & 1) ? K1_BSLOT : 0u);
  return s;
}
template <int Q> DI void k1_dma(const K1Issue& c, const K1Step& s) {
  if (Q == 0) dma_m0(c.ra, c.voffA[0], s.sa[0], s.ma);
  else if (Q < 4) dma_off<(Q & 3) * 1024>(c.ra, c.voffA[Q & 1], s.sa[Q & 3]);
  else if (Q == 4) dma_m0(c.rb[0], c.voffB[0], s.kb, s.mb);
  else if (Q == 5) dma_off<1024>(c.rb[1], c.voffB[1], s.kb);
  else dma_off<2048>(c.rb[2], c.voffB[2], s.kb);
}

struct K2Issue {
  const unsigned char* Abase; const unsigned char* bigbase; const unsigned char* tchbase;
  unsigned voffA[2], voffBig[2], sBig[4], sSm[2], bigstep, astep, mBig, mSm, tchvoff, tchstep;
  int NT, leadmask, lead; bool bigIsQ;
};
DI unsigned k2_phys(const K2Issue& c, int t) { int p = t + (((c.leadmask >> (t & 7)) & 1) * c.lead); p = p >= c.NT ? p - c.NT : p; return (unsigned)p; }
DI K2Issue k2_setup(unsigned lds0, bool TR, const unsigned char* Abase, const unsigned char* Bbase, int NT, int g, int wi, int lane, int pm4, int pn8) {
  K2Issue c; const unsigned i = (unsigned)lane;
  c.Abase = Abase; c.NT = NT;
  if (!TR && LEADT == 8) { c.leadmask = 1 << ((2 * pm4 + pn8) & 7); c.lead = 8; }
  else { c.leadmask = 0x11 << ((pm4 + pn8) & 3); c.lead = 4; }
  c.astep = TR ? 8u * KBS : 128u;
#pragma unroll
  for (int par = 0; par < 2; ++par) c.voffA[par] = TR ? i * 16u : (i >> 3) * (S * 2u) + (((i & 7u) ^ (i >> 4) ^ ((unsigned)par << 2)) << 4);
  auto sApiece = [&](unsigned p, unsigned half) -> unsigned { return TR ? (p >> 1) * KBS + (p & 1u) * 1024u + half * 2048u : (half * 128u + p * 8u) * (S * 2u); };
  const unsigned idx = (unsigned)(wi * 8 + (lane & 7));
  if (g == 0) {
    c.bigbase = Bbase;
    if (TR) {
      c.bigstep = 64u * D * 2u;
#pragma unroll
      for (int par = 0; par < 2; ++par) c.voffBig[par] = (i >> 4) * (D * 2u) + ((((i & 15u) >> 1) ^ ((i >> 4) + 4u * par)) << 5) + (i & 1u) * 16u;
#pragma unroll
      for (int q = 0; q < 4; ++q) { const unsigned p = (unsigned)(4 * wi + q); c.sBig[q] = (8u * (p & 1u) + 4u * ((p >> 1) & 1u) + 16u * (p >> 2)) * (D * 2u); }
    } else {
      c.bigstep = 8u * KBS;
#pragma unroll
      for (int par = 0; par < 2; ++par) c.voffBig[par] = i * 16u;
#pragma unroll
      for (int q = 0; q < 4; ++q) { const unsigned p = (unsigned)(4 * wi + q); c.sBig[q] = (p >> 1) * KBS + (p & 1u) * 1024u; }
    }
    c.tchbase = Abase; c.tchstep = c.astep;
    c.tchvoff = TR ? (unsigned)pn8 * KBS + idx * 128u : ((unsigned)pn8 * 32u + idx) * (S * 2u);
  } else {
    c.bigbase = Abase; c.bigstep = c.astep;
#pragma unroll
    for (int par = 0; par < 2; ++par) c.voffBig[par] = c.voffA[par];
#pragma unroll
    for (int q = 0; q < 4; ++q) c.sBig[q] = sApiece((unsigned)(4 * wi + q), 1u);
    c.tchbase = Bbase;
    if (!TR) { c.tchvoff = (2u * pm4 + (idx >> 4)) * KBS + (idx & 15u) * 128u; c.tchstep = 8u * KBS; }
    else { c.tchvoff = (unsigned)(pm4 * 16 + wi * 4 + ((lane & 7) >> 1)) * (D * 2u) + (lane & 1) * 128u; c.tchstep = 64u * D * 2u; }
  }
  const unsigned psm = (unsigned)((g == 0 ? 8 : 0) + 2 * wi);
#pragma unroll
  for (int q = 0; q < 2; ++q) c.sSm[q] = sApiece(psm + q, 0u);
#pragma unroll
  for (int q = 0; q < 4; ++q) c.sBig[q] -= q * 1024u;
  c.sSm[1] -= 1024u;
  c.bigIsQ = (g == 0) != TR;
  c.mBig = lds0 + ((g == 1 && !TR) ? 16384u : 0u) + (unsigned)wi * 4096u; c.mSm = lds0 + (TR ? 16384u : 0u) + psm * 1024u;
  return c;
}
DI void k2_big(const K2Issue& c, int tile, unsigned sR, unsigned sQ) {
  const u32x4 r = mksrd(c.bigbase + k2_phys(c, tile) * c.bigstep, 0x7fffffffu);
  dma_m0(r, c.voffBig[0], c.sBig[0], c.mBig + (c.bigIsQ ? sQ : sR));
  dma_off<1024>(r, c.voffBig[1], c.sBig[1]); dma_off<2048>(r, c.voffBig[0], c.sBig[2]); dma_off<3072>(r, c.voffBig[1], c.sBig[3]);
}
DI void k2_sm(const K2Issue& c, int tile, unsigned sR) {
  const u32x4 r = mksrd(c.Abase + k2_phys(c, tile) * c.astep, 0x7fffffffu);
  dma_m0(r, c.voffA[0], c.sSm[0], c.mSm + sR);
  dma_off<1024>(r, c.voffA[1], c.sSm[1]);
}
template <int TEND = LEAD2> DI void k2_touch(const K2Issue& c, unsigned& dummy) {
#pragma unroll
  for (int t = 2; t < TEND; ++t) touch(mksrd(c.tchbase, 0x7fffffffu), c.tchvoff, (unsigned)t * c.tchstep, dummy);
}
DI void k2_pre(const K2Issue& c, int g, bool tRole, int tile) {
  const unsigned sR = tile ? K2_R1 : K2_R0, sQ = tile ? K2_Q1 : K2_Q0;
  if (tRole) { k2_sm(c, tile, sR); if (g == 1) k2_big(c, tile, sR, sQ); }
  else if (g == 0) k2_big(c, tile, sR, sQ);
}
struct K2Args { const u16* Cr; const u16* Ci; const u16* abT; const u16* h; const u16* w2; };
DI K2Issue k2_make(const K2Args& a, unsigned lds0) {
  int bid = blockIdx.x, tid = threadIdx.x; asm volatile("" : "+s"(bid), "+v"(tid));
  const int xcd = bid & 7, j = bid >> 3, pm = (xcd & 1) * 4 + (j & 3), role = xcd >> 1, pn = j >> 2;
  const int wid = (int)rfl(tid >> 6), lane = tid & 63, g = wid >> 2, wi = wid & 3;
  const bool tRole = role < 2; const int k0 = (role & 1) * (F / 2);
  const u16* A2 = tRole ? (role ? a.Ci : a.Cr) + (size_t)pm * 256 * S : a.h + (size_t)(k0 / 8) * (KBS / 2) + pm * 256 * 8;
  const u16* B2 = tRole ? a.abT + (size_t)(role * 1024 + pn * 128) * 8 : a.w2 + (size_t)k0 * D + pn * 128;
  K2Issue c = k2_setup(lds0, !tRole, (const unsigned char*)A2, (const unsigned char*)B2, tRole ? S / 64 : F / 128, g, wi, lane, j & 3, pn);
#pragma unroll
  for (int q = 0; q < 4; ++q) c.sBig[q] = rfl(c.sBig[q]);
#pragma unroll
  for (int q = 0; q < 2; ++q) c.sSm[q] = rfl(c.sSm[q]);
  c.bigstep = rfl(c.bigstep); c.astep = rfl(c.astep); c.mBig = rfl(c.mBig); c.mSm = rfl(c.mSm); c.tchstep = rfl(c.tchstep);
  asm volatile("s_nop 4" : "+s"(c.sBig[0]), "+s"(c.sBig[1]), "+s"(c.sBig[2]), "+s"(c.sBig[3]), "+s"(c.sSm[0]), "+s"(c.sSm[1]), "+s"(c.tchstep) :: "memory");
  return c;
}

template <int TYPE  , int NB>
DI void k1_group(unsigned lds0, const K1Issue& c, const K2Args& a2, bool tRole, bool perm, int wi, int lane, int pm, int col0, u16* abT, u16* h, const u16* __restrict__ b1,
                 unsigned* sync ST_ARG) {
  constexpr int g = NB == 6 ? 0 : 1;
  const int fr = lane & 15, fq = lane >> 4, srow0 = pm * 256;
  constexpr unsigned BRING = NB == 6 ? K1_B0 : K1_B1, PITCH = NB * 32u;
  const unsigned sw = (unsigned)((fr >> 1) & 7);
  const unsigned aoff0 = lds0 + (unsigned)(wi * 64 + fr) * 128u + (((unsigned)fq ^ sw) << 4), aoff1 = aoff0 ^ 64u;
  unsigned boff0, boff1;
  if (TYPE == 0) { boff0 = lds0 + BRING + (unsigned)fr * 128u + (((unsigned)fq ^ sw) << 4); boff1 = boff0 ^ 64u; }
  else {
    const unsigned q = (lane & 15) >> 2, p = lane & 3, g4 = lane >> 4;
    const unsigned base = lds0 + BRING + (q + 4u * (g4 & 1u) + 16u * (g4 >> 1)) * PITCH + p * 8u;
    if (NB == 6) { boff0 = base + (g4 & 1u) * 32u; boff1 = base - (g4 & 1u) * 32u; } else { boff0 = base; boff1 = base; }
  }
  f32x4 acc[4][NB];
  u32x2 bb[NB];
  if (TYPE == 1) {
    const u32x4 rbias = mksrd(b1 + col0, NB * 32u);
#pragma unroll
    for (int n = 0; n < NB; ++n) asm volatile("buffer_load_dwordx2 %0, %1, %2, 0 offen offset:%3" : "=v"(bb[n]) : "v"((unsigned)fq * 8u), "s"(rbias), "n"(n * 32) : "memory");
  }
  {
    const K1Step s0 = k1_prep(c, 0, 0u, 0);
    k1_dma<0>(c, s0); k1_dma<1>(c, s0); k1_dma<2>(c, s0); k1_dma<3>(c, s0);
    if (g == 1) { k1_dma<4>(c, s0); k1_dma<5>(c, s0); k1_dma<6>(c, s0); }
    const K1Step s1 = k1_prep(c, 1, 32768u, g);
    k1_dma<4>(c, s1); k1_dma<5>(c, s1); k1_dma<6>(c, s1);
    k1_dma<0>(c, s1); k1_dma<1>(c, s1); k1_dma<2>(c, s1); k1_dma<3>(c, s1);
  }
  unsigned cur = 0;
  K1Step st = k1_prep(c, 2, 65536u, 1 + g);
  unsigned pa0 = aoff0, pa1 = aoff1, pb0 = boff0, pb1 = boff1;
  WAIT_V(7);
  if (TYPE == 1) {
#pragma unroll
    for (int n = 0; n < NB; ++n) asm volatile("" : "+v"(bb[n]));
  }
#pragma unroll
  for (int m = 0; m < 4; ++m)
#pragma unroll
    for (int n = 0; n < NB; ++n) acc[m][n] = TYPE == 1 ? (f32x4){bflo(bb[n][0]), bfhi(bb[n][0]), bflo(bb[n][1]), bfhi(bb[n][1])} : (f32x4){0.f, 0.f, 0.f, 0.f};
  BAR();
  if (g == 1) BAR();
  SCHED();
  ST(1); STC(0);
  K2Issue c2;
  unsigned woc = 0, wow = 0, sdum1 = 0; u32x2 sdum = {0u, 0u};
  auto iter = [&](auto modec, int t) {
    constexpr int MODE = decltype(modec)::value;
    bf16x8 af[4][2], bfr[NB][2];
    constexpr bool va = MODE == 0, vb = MODE == 0 || (MODE == 1 && g == 0);
    auto rdB = [&](int n) {
      if (TYPE == 0) { bfr[n][0] = ldsd128(pb0 + n * 2048); bfr[n][1] = ldsd128(pb1 + n * 2048); }
      else {
        const unsigned pb = (n & 1) ? pb1 : pb0;
#pragma unroll
        for (int ks = 0; ks < 2; ++ks) bfr[n][ks] = ldstr(pb + n * 32 + (32 * ks) * PITCH, pb + n * 32 + (32 * ks + 8) * PITCH);
      }
    };
    auto rdA = [&](int m) { af[m][0] = ldsd128(pa0 + m * 2048); af[m][1] = ldsd128(pa1 + m * 2048); };
    if (vb) k1_dma<4>(c, st);
    rdA(0); rdA(1);
    if (vb) k1_dma<5>(c, st);
    rdA(2); rdA(3);
    if (vb) k1_dma<6>(c, st);
    rdB(0);
    if (va) k1_dma<0>(c, st);
    rdB(1);
    if (va) k1_dma<1>(c, st);
    rdB(2);
    if (va) k1_dma<2>(c, st);
    rdB(3);
    if (va) k1_dma<3>(c, st);
    rdB(4);
    if (NB == 6) rdB(5);
    if (MODE == 0) WAIT_V(7);
    else if (PREIN) {
      k2_pre(c2, g, tRole, MODE - 1);
      if (MODE == 1) { if (g == 0) { if (tRole) WAIT_V(5); else WAIT_V(7); } else { if (tRole) WAIT_V(6); else WAIT_V(0); } }
      else if (g == 0) { if (tRole) WAIT_V(4); else WAIT_V(8); }
    } else { if (vb) WAIT_V(3); else WAIT_V(0); }
    WAIT_L(0); BAR(); SCHED();
    __builtin_amdgcn_s_setprio(1);
#pragma unroll
    for (int ks = 0; ks < 2; ++ks)
#pragma unroll
      for (int n = 0; n < NB; ++n)
#pragma unroll
        for (int m = 0; m < 4; ++m) {
          acc[m][n] = TYPE == 0 ? MFMA(af[m][ks], bfr[n][ks], acc[m][n]) : MFMA(bfr[n][ks], af[m][ks], acc[m][n]);
          if (SWARM && MODE == 0 && ks == 0 && n * 4 + m < 7) {
            constexpr int SOFF = SWARM == 2 ? 60 : 0;
            const int u = n * 4 + m;
            SCHED();
            if (SWARM == 2) {
              if (u < 5) asm volatile("s_buffer_load_dwordx2 %0, %1, %2 offset:%3" : "+s"(sdum) : "s"(c.wc), "s"(woc), "n"((u % 5) * 128 + SOFF));
              else asm volatile("s_buffer_load_dwordx2 %0, %1, %2 offset:%3" : "+s"(sdum) : "s"(c.ww), "s"(wow), "n"(((u - 5) & 1) * 128 + SOFF));
            } else {
              if (u < 5) asm volatile("s_buffer_load_dword %0, %1, %2 offset:%3" : "+s"(sdum1) : "s"(c.wc), "s"(woc), "n"((u % 5) * 128));
              else asm volatile("s_buffer_load_dword %0, %1, %2 offset:%3" : "+s"(sdum1) : "s"(c.ww), "s"(wow), "n"(((u - 5) & 1) * 128));
            }
            SCHED();
          }
        }
    if (MODE != 2) {
      if (SWARM) { woc += 640u; wow += 256u; asm volatile("" : "+s"(woc), "+s"(wow)); }
      st = k1_prep(c, t + 3, cur, t + 2 + g);
      cur = cur == 65536u ? 0u : cur + 32768u;
      const unsigned bs = (t & 1) ? 0u : K1_BSLOT;
      pa0 = aoff0 + cur; pa1 = aoff1 + cur; pb0 = boff0 + bs; pb1 = boff1 + bs;
      mfma_shadow<(SWARM && MODE == 0) ? 8 * NB - 7 : 8 * NB>();
      asm volatile("" : "+s"(st.sa[0]), "+s"(st.sa[1]), "+s"(st.sa[2]), "+s"(st.sa[3]), "+s"(st.kb), "+s"(st.ma), "+s"(st.mb), "+s"(cur), "+v"(pa0), "+v"(pa1), "+v"(pb0), "+v"(pb1));
    }
    __builtin_amdgcn_s_setprio(0);
    SCHED();
    if (!(g == 1 && MODE == 2)) BAR();
    SCHED();
  };
  for (int t = 0; t < K1_NT - 2; ++t) iter(IC<0>{}, t);
  if (PREIN) c2 = k2_make(a2, lds0);
  iter(IC<1>{}, K1_NT - 2);
  iter(IC<2>{}, K1_NT - 1);
  asm volatile("" ::"s"(sdum), "s"(sdum1));
  ST(2); STC(1);
  if (TYPE == 0) {
    const __amdgpu_buffer_rsrc_t ro = mkrsrc(abT + (size_t)(srow0 / 8 + 8 * wi) * (KBS / 2) + col0 * 8, 0x7fffffffu);
    const unsigned vo = (unsigned)fq * KBS + (unsigned)fr * 16u;
#pragma unroll
    for (int j = 0; j < 2; ++j)
#pragma unroll
      for (int n = 0; n < NB; ++n) {
        u32x4 w; const f32x4 a = acc[2 * j][n], b = acc[2 * j + 1][n];
        w[0] = cvtpk(a[0], a[1]); w[1] = cvtpk(a[2], a[3]); w[2] = cvtpk(b[0], b[1]); w[3] = cvtpk(b[2], b[3]);
        __builtin_amdgcn_raw_buffer_store_b128(w, ro, vo + j * 4 * KBS + n * 256, 0, 16);
      }
  } else {
    const __amdgpu_buffer_rsrc_t ro = mkrsrc(h + (size_t)(col0 / 8) * (KBS / 2) + (srow0 + 64 * wi) * 8, 0x7fffffffu);
    const unsigned rowl = perm ? (unsigned)(8 * (fr >> 2) + 4 * (fq & 1) + (fr & 3)) : (unsigned)(16 * (fq & 1) + fr);
    const unsigned vo = (unsigned)(fq >> 1) * KBS + rowl * 16u;
    auto gelu2 = [&](f32x2 v) -> unsigned {
      constexpr float c1 = -2.0f * 0.7978845608028654f * 1.4426950408889634f, c3 = c1 * 0.044715f;
      const f32x2 u = v * __builtin_elementwise_fma(v * v, (f32x2){c3, c3}, (f32x2){c1, c1});
      f32x2 e; e[0] = __builtin_amdgcn_exp2f(u[0]); e[1] = __builtin_amdgcn_exp2f(u[1]);
      const f32x2 d = e + 1.0f;
      f32x2 r; r[0] = __builtin_amdgcn_rcpf(d[0]); r[1] = __builtin_amdgcn_rcpf(d[1]);
      return cvtpk2(v * r);
    };
#pragma unroll
    for (int n = 0; n < NB; ++n)
#pragma unroll
      for (int j = 0; j < 2; ++j) {
        const f32x4 a = acc[2 * j][n], b = acc[2 * j + 1][n];
        const unsigned a0 = gelu2((f32x2){a[0], a[1]}), a1 = gelu2((f32x2){a[2], a[3]}), b0 = gelu2((f32x2){b[0], b[1]}), b1_ = gelu2((f32x2){b[2], b[3]});
        auto r0 = __builtin_amdgcn_permlane16_swap(a0, b0, false, false);
        auto r1 = __builtin_amdgcn_permlane16_swap(a1, b1_, false, false);
        u32x4 w; w[0] = r0[0]; w[1] = r1[0]; w[2] = r0[1]; w[3] = r1[1];
        __builtin_amdgcn_raw_buffer_store_b128(w, ro, vo + n * 2 * KBS + j * 512, 0, 16);
      }
  }
  WAIT_V(0);
  if (lane == 0) {
    GAS unsigned* sy = (GAS unsigned*)sync;
    const int lo = TYPE == 0 ? col0 >> 7 : col0 / (F / 2), hi = TYPE == 0 ? (col0 + NB * 16 - 1) >> 7 : (col0 + NB * 16 - 1) / (F / 2);
    GAS unsigned* cb = TYPE == 0 ? sy + CNT_D : sy + CNT_M + pm * 32;
    __hip_atomic_fetch_add(cb + lo * 16, 1u, __ATOMIC_RELAXED, __HIP_MEMORY_SCOPE_AGENT);
    if (hi != lo) __hip_atomic_fetch_add(cb + hi * 16, 1u, __ATOMIC_RELAXED, __HIP_MEMORY_SCOPE_AGENT);
  }
  ST(3);
}

template <bool TR>
DI void k2_loop(unsigned lds0, const K2Issue& c, int g, int wi, int lane, f32x4 (&acc)[4][4] ST_ARG) {
  const int fr = lane & 15, fq = lane >> 4, wmi = wi >> 1, wn = wi & 1;
  constexpr int NT = TR ? F / 128 : S / 64;
  const unsigned sw = (unsigned)((fr >> 1) & 7);
  unsigned dummy = 0;
  if (TR) {
    if (g == 0) { k2_sm(c, 0, K2_R0); k2_sm(c, 1, K2_R1); k2_touch(c, dummy); }
    else { k2_sm(c, 0, K2_R0); k2_big(c, 0, K2_R0, K2_Q0); k2_sm(c, 1, K2_R1); k2_big(c, 1, K2_R1, K2_Q1); k2_sm(c, 2, K2_R2); }
  } else {
    if (g == 0) { k2_big(c, 0, K2_R0, K2_Q0); k2_big(c, 1, K2_R1, K2_Q1); }
    else k2_touch(c, dummy);
  }
  const unsigned abase = lds0 + (TR ? (g == 0 ? 16384u : 0u) : (g == 0 ? 0u : 16384u));
  const bool aIsQ = TR && g == 1;
  const unsigned aoff0 = TR ? abase + (unsigned)fq * 2048u + (unsigned)(wmi * 64 + fr) * 16u : abase + (unsigned)(wmi * 64 + fr) * 128u + (((unsigned)fq ^ sw) << 4);
  const unsigned aoff1 = aoff0 ^ 64u;
  constexpr int AM = TR ? 256 : 2048;
  unsigned boff[4];
  if (TR) {
    const unsigned q = (lane & 15) >> 2, p = lane & 3, g4 = lane >> 4, s8 = q + 4u * (g4 & 1u);
#pragma unroll
    for (int n = 0; n < 4; ++n) boff[n] = lds0 + (s8 + 16u * (g4 >> 1)) * 256u + ((((unsigned)(wn * 4 + n)) ^ s8) << 5) + p * 8u;
  } else {
    boff[0] = lds0 + (unsigned)fq * 2048u + (unsigned)(wn * 64 + fr) * 16u; boff[1] = boff[2] = boff[3] = 0;
  }
  unsigned curR = K2_R0, curQ = K2_Q0;
  u32x4 rb, rs; unsigned mb, ms, pa0, pa1, pb[4];
  auto prep = [&](int t) {
    const unsigned prvR = curR == 0u ? 65536u : curR - 32768u, prvQ = curQ == K2_Q0 ? K2_Q2 : curQ - 16384u;
    const int tsm = t + 2 + g;
    rb = mksrd(c.bigbase + k2_phys(c, t + 2) * c.bigstep, t + 2 < NT ? 0x7fffffffu : 0u);
    rs = mksrd(c.Abase + k2_phys(c, tsm) * c.astep, tsm < NT ? 0x7fffffffu : 0u);
    mb = c.mBig + (c.bigIsQ ? prvQ : prvR); ms = c.mSm + (g == 0 ? prvR : curR);
    const unsigned sa = aIsQ ? curQ : curR, sb = TR ? curR : curQ;
    pa0 = aoff0 + sa; pa1 = aoff1 + sa;
#pragma unroll
    for (int n = 0; n < (TR ? 4 : 1); ++n) pb[n] = boff[n] + sb;
  };
  prep(0);
  if (TR) {
    if (g == 0) { WAIT_V(2 + NPT); BAR(); } else { WAIT_V(12); BAR(); WAIT_V(6); BAR(); }
  } else {
    if (g == 0) { WAIT_V(4); BAR(); } else { WAIT_V(PREIN ? 63 : 12 + NPT); BAR(); WAIT_V(PREIN ? 63 : 6 + NPT); BAR(); }
  }
  SCHED();
  ST(5); STC(2);
  auto rdA = [&](int m, int ks) -> bf16x8 { return TR ? ldsd128(pa0 + m * AM + ks * 8192) : ldsd128((ks ? pa1 : pa0) + m * AM); };
  auto rdB = [&](int n, int ks) -> bf16x8 { return TR ? ldstr(pb[n] + ks * 8192, pb[n] + ks * 8192 + 2048) : ldsd128(pb[0] + n * 256 + ks * 8192); };
  for (int t = 0; t < NT; ++t) {
    bf16x8 af[4][2], bfr[4][2];
    dma_m0(rb, c.voffBig[0], c.sBig[0], mb);
    af[0][0] = rdA(0, 0); af[0][1] = rdA(0, 1); bfr[0][0] = rdB(0, 0);
    dma_off<1024>(rb, c.voffBig[1], c.sBig[1]);
    af[1][0] = rdA(1, 0); af[1][1] = rdA(1, 1); bfr[1][0] = rdB(1, 0);
    dma_off<2048>(rb, c.voffBig[0], c.sBig[2]);
    af[2][0] = rdA(2, 0); af[2][1] = rdA(2, 1); bfr[2][0] = rdB(2, 0);
    dma_off<3072>(rb, c.voffBig[1], c.sBig[3]);
    af[3][0] = rdA(3, 0); af[3][1] = rdA(3, 1);
    dma_m0(rs, c.voffA[0], c.sSm[0], ms);
    bfr[3][0] = rdB(3, 0); bfr[0][1] = rdB(0, 1); bfr[1][1] = rdB(1, 1);
    dma_off<1024>(rs, c.voffA[1], c.sSm[1]);
    bfr[2][1] = rdB(2, 1); bfr[3][1] = rdB(3, 1);
    WAIT_L(0); BAR(); SCHED();
    __builtin_amdgcn_s_setprio(1);
#pragma unroll
    for (int ks = 0; ks < 2; ++ks)
#pragma unroll
      for (int n = 0; n < 4; ++n)
#pragma unroll
        for (int m = 0; m < 4; ++m) acc[m][n] = MFMA(bfr[n][ks], af[m][ks], acc[m][n]);
    curR = curR == 65536u ? 0u : curR + 32768u; curQ = curQ == K2_Q2 ? K2_Q0 : curQ + 16384u;
    prep(t + 1);
    mfma_shadow<32>();
    if (TR) asm volatile("" : "+s"(rb), "+s"(rs), "+s"(mb), "+s"(ms), "+s"(curR), "+s"(curQ), "+v"(pa0), "+v"(pb[0]), "+v"(pb[1]), "+v"(pb[2]), "+v"(pb[3]));
    else asm volatile("" : "+s"(rb), "+s"(rs), "+s"(mb), "+s"(ms), "+s"(curR), "+s"(curQ), "+v"(pa0), "+v"(pa1), "+v"(pb[0]));
    __builtin_amdgcn_s_setprio(0);
    SCHED();
    WAIT_V(6);
    if (!(g == 1 && t == NT - 1)) BAR();
    SCHED();
  }
  WAIT_V(0);
  asm volatile("" ::"v"(dummy));
}

__global__ __launch_bounds__(512, 2) void k_main(const u16* __restrict__ xn, const u16* __restrict__ Dr, const u16* __restrict__ Di, const u16* __restrict__ w1,
                                                 const u16* __restrict__ b1, u16* abT, u16* h, const u16* __restrict__ Cr, const u16* __restrict__ Ci,
                                                 const u16* __restrict__ w2, const u16* __restrict__ x, const u16* __restrict__ b2, u16* __restrict__ out,
                                                 unsigned char* xbuf, unsigned* sync DBG_ARG) {
  ST_DECL; ST(0);
  extern __shared__ __attribute__((aligned(16))) unsigned char smem[];
  const unsigned lds0 = (unsigned)(size_t)(LAS unsigned char*)smem;
  const int bid = blockIdx.x, xcd = bid & 7, j = bid >> 3;
  const int pm = (xcd & 1) * 4 + (j & 3), ct = CTMAP(xcd, j);
  const int role = xcd >> 1, pn = j >> 2, tile = pm * 8 + pn;
  const int tid = threadIdx.x, wid = (int)rfl(tid >> 6), lane = tid & 63, g = wid >> 2, wi = wid & 3;
  const int type0 = ct < 13 ? 0 : 1, col00 = ct < 13 ? 96 * ct : 96 * (ct - 13);
  const int type1 = ct < 10 ? 0 : 1, col01 = ct < 10 ? 1248 + 80 * ct : 1824 + 80 * (ct - 10);
  const bool perm = type0 == 0;
  K1Issue c;
  c.ra = mksrd(xn + (size_t)pm * 256 * D, 0x7fffffffu);
  c.leadmask = 1 << (((j & 3) + (j >> 2)) & 3);
  const unsigned i = (unsigned)lane;
#pragma unroll
  for (int par = 0; par < 2; ++par) c.voffA[par] = (perm ? 8u * (i >> 5) + ((i >> 3) & 3u) : (i >> 3)) * (D * 2u) + (((i & 7u) ^ (i >> 4) ^ ((unsigned)par << 2)) << 4);
#pragma unroll
  for (int q = 0; q < 4; ++q) { const unsigned p = (unsigned)(g * 16 + wi * 4 + q); c.sA[q] = (perm ? 16u * (p & 1u) + 4u * ((p >> 1) & 1u) + 32u * (p >> 2) : 8u * p) * (D * 2u) - q * 1024u; }
  c.mA = lds0 + (unsigned)(g * 16 + wi * 4) * 1024u;
  const int otype = g == 0 ? type1 : type0, ocol = g == 0 ? col01 : col00;
  const unsigned cpr = g == 0 ? 10u : 12u, oring = g == 0 ? K1_B1 : K1_B0;
  c.bstep = otype == 0 ? 128u : 64u * F * 2u;
  c.mB = lds0 + oring + (unsigned)wi * 3072u;
#pragma unroll
  for (int q = 0; q < 3; ++q) {
    const unsigned p = (unsigned)(3 * wi + q), nr = p < cpr ? 0x7fffffffu : 0u;
    if (otype == 0) {
      int e0 = ocol + 8 * (int)p; if (e0 > 2040) e0 = 2040;
      c.rb[q] = mksrd((const unsigned char*)(e0 < 1024 ? Dr + (size_t)e0 * D : Di + (size_t)(e0 - 1024) * D) - q * 1024, nr);
      c.voffB[q] = (i >> 3) * (D * 2u) + (((i & 7u) ^ (i >> 4) ^ ((p & 1u) << 2)) << 4);
    } else {
      const unsigned ci = 64u * p + i, rho = ci / cpr, pos = ci - rho * cpr, sp = pos >> 1;
      const unsigned seg = cpr == 12u ? sp ^ ((rho >> 2) & 1u) : sp;
      const unsigned k = (rho & 3u) + 8u * ((rho >> 2) & 1u) + 4u * ((rho >> 3) & 1u) + (rho & 48u);
      c.rb[q] = mksrd((const unsigned char*)(w1 + ocol) - q * 1024, nr);
      c.voffB[q] = k * (F * 2u) + seg * 32u + (pos & 1u) * 16u;
    }
  }
  {
    const int gw = bid * 8 + wid;
    c.wc = mksrd((const unsigned char*)(gw < 1024 ? Cr : Ci) + (size_t)(gw & 1023) * 8192, 8192u);
    c.ww = mksrd((const unsigned char*)w2 + (size_t)gw * 3584, 3584u);
  }
  const bool tRole = role < 2;
  const K2Args a2 = {Cr, Ci, abT, h, w2};
  GAS unsigned* fl = (GAS unsigned*)sync + (tile * 8 + wid) * 16;
  if (g == 0) {
    if (type0 == 0) k1_group<0, 6>(lds0, c, a2, tRole, perm, wi, lane, pm, col00, abT, h, b1, sync ST_PASS);
    else k1_group<1, 6>(lds0, c, a2, tRole, perm, wi, lane, pm, col00, abT, h, b1, sync ST_PASS);
  } else {
    if (type1 == 0) k1_group<0, 5>(lds0, c, a2, tRole, perm, wi, lane, pm, col01, abT, h, b1, sync ST_PASS);
    else k1_group<1, 5>(lds0, c, a2, tRole, perm, wi, lane, pm, col01, abT, h, b1, sync ST_PASS);
  }
  const K2Issue c2 = k2_make(a2, lds0);
  unsigned dummy0 = 0;
  if (PREBAR) BAR();
  if (!PREIN) { k2_pre(c2, g, tRole, 0); k2_pre(c2, g, tRole, 1); }
  if (tRole) { if (g == 0) k2_touch<LEADT>(c2, dummy0); else k2_sm(c2, 2, K2_R2); }
  else if (g == 1) k2_touch(c2, dummy0);
  if (WARMC && ct < 10) {
    const int idx = ct * 8 + pm, sl = idx % 40;
    constexpr unsigned SLB = 1639u * 128u;
    const unsigned off = (unsigned)sl * SLB, rem = (unsigned)(S * S * 2) - off;
    const u32x4 rc = mksrd((const unsigned char*)(idx < 40 ? Cr : Ci) + off, rem < SLB ? rem : SLB);
#pragma unroll
    for (int q = 0; q < 4; ++q) touch(rc, (unsigned)(tid * 128), (unsigned)(q * 65536), dummy0);
    WAIT_V(0);
  }
  if (wid == 0) {
    unsigned need; GAS unsigned* cw;
    if (tRole) {
      const int eb = role * 8 + pn, lo = eb * 128, hi = lo + 127;
      const int hlo = lo < 1248 ? lo / 96 : 13 + (lo - 1248) / 80, hhi = hi < 1248 ? hi / 96 : 13 + (hi - 1248) / 80;
      need = (unsigned)(hhi - hlo + 1) * 32u; cw = (GAS unsigned*)sync + CNT_D + eb * 16;
    } else {
      need = (role == 2 ? 19u : 23u) * 4u;
      cw = (GAS unsigned*)sync + CNT_M + pm * 32 + (role & 1) * 16;
    }
    for (unsigned spins = 0; rfl(__hip_atomic_load(cw, __ATOMIC_RELAXED, __HIP_MEMORY_SCOPE_AGENT)) < need && spins < (1u << 22); ++spins) __builtin_amdgcn_s_sleep(2);
    __builtin_amdgcn_fence(__ATOMIC_ACQUIRE, "agent");
    WAIT_V(0);
  }
  BAR();
  asm volatile("" ::: "memory");
  ST(4);
  f32x4 acc[4][4];
#pragma unroll
  for (int m = 0; m < 4; ++m)
#pragma unroll
    for (int n = 0; n < 4; ++n) acc[m][n] = (f32x4){0.f, 0.f, 0.f, 0.f};
  if (tRole) k2_loop<false>(lds0, c2, g, wi, lane, acc ST_PASS);
  else k2_loop<true>(lds0, c2, g, wi, lane, acc ST_PASS);
  asm volatile("" ::"v"(dummy0));
  ST(6); STC(3);
  const __amdgpu_buffer_rsrc_t xr = mkrsrc(xbuf + (size_t)tile * (4 * 8 * 8 * 1024), 4u * 8u * 8u * 1024u);
  const __amdgpu_buffer_rsrc_t xw = mkrsrc(xbuf + (size_t)tile * (4 * 8 * 8 * 1024) + (role * 8 + wid) * 8192, 8192u);
  const int fr = lane & 15, fq = lane >> 4, wmi = wi >> 1, wn = wi & 1;
  const int trow = pm * 256 + g * 128 + wmi * 64 + role * 16 + fr, ecol = pn * 128 + wn * 64 + 4 * fq;
#pragma unroll
  for (int m = 0; m < 4; ++m)
    if (m != role) {
#pragma unroll
      for (int pr = 0; pr < 2; ++pr) {
        const f32x4 a = acc[m][2 * pr], b = acc[m][2 * pr + 1];
        u32x4 w; w[0] = cvtpk(a[0], a[1]); w[1] = cvtpk(a[2], a[3]); w[2] = cvtpk(b[0], b[1]); w[3] = cvtpk(b[2], b[3]);
        __builtin_amdgcn_raw_buffer_store_b128(w, xw, lane * 16 + (m * 2 + pr) * 1024, 0, 16);
      }
    }
  const __amdgpu_buffer_rsrc_t frs = mkrsrc((const void*)fl, 16u);
  u32x4 f0 = __builtin_bit_cast(u32x4, __builtin_amdgcn_raw_buffer_load_b128(frs, 0, 0, 16));
  u32x2 xv[4], bv[4];
#pragma unroll
  for (int n = 0; n < 4; ++n) { xv[n] = *(const u32x2*)(x + (size_t)trow * D + ecol + 16 * n); bv[n] = *(const u32x2*)(b2 + ecol + 16 * n); }
  f32x4 own[4];
#pragma unroll
  for (int n = 0; n < 4; ++n) own[n] = role == 0 ? acc[0][n] : role == 1 ? acc[1][n] : role == 2 ? acc[2][n] : acc[3][n];
  WAIT_V(0);
  if (lane == 0) __hip_atomic_store(fl + role, 1u, __ATOMIC_RELAXED, __HIP_MEMORY_SCOPE_AGENT);
  u32x4 pk[4][2];
#pragma unroll
  for (int r = 0; r < 4; ++r) { pk[r][0] = (u32x4){0u, 0u, 0u, 0u}; pk[r][1] = (u32x4){0u, 0u, 0u, 0u}; }
  unsigned have = 1u << role;
  for (unsigned spins = 0;;) {
    const unsigned rdy = (rfl(f0[0]) == 1u ? 1u : 0u) | (rfl(f0[1]) == 1u ? 2u : 0u) | (rfl(f0[2]) == 1u ? 4u : 0u) | (rfl(f0[3]) == 1u ? 8u : 0u);
    const unsigned nw = rdy & ~have;
    asm volatile("" ::: "memory");
    have |= nw;
    if (have != 15u) f0 = __builtin_bit_cast(u32x4, __builtin_amdgcn_raw_buffer_load_b128(frs, 0, 0, 16));
#pragma unroll
    for (int r = 0; r < 4; ++r)
      if (nw & (1u << r)) {
#pragma unroll
        for (int pr = 0; pr < 2; ++pr) pk[r][pr] = __builtin_bit_cast(u32x4, __builtin_amdgcn_raw_buffer_load_b128(xr, lane * 16, ((r * 8 + wid) * 8 + role * 2 + pr) * 1024, 16));
      }
    if (have == 15u || ++spins > (1u << 22)) break;
    if (!nw) __builtin_amdgcn_s_sleep(XSLEEP);
  }
#pragma unroll
  for (int n = 0; n < 4; ++n) {
    const float xb[4] = {bflo(xv[n][0]), bfhi(xv[n][0]), bflo(xv[n][1]), bfhi(xv[n][1])};
    const float bb[4] = {bflo(bv[n][0]), bfhi(bv[n][0]), bflo(bv[n][1]), bfhi(bv[n][1])};
    float pv[4][4];
#pragma unroll
    for (int r = 0; r < 4; ++r) {
      const unsigned w0 = pk[r][n >> 1][(n & 1) * 2], w1_ = pk[r][n >> 1][(n & 1) * 2 + 1];
      pv[r][0] = r == role ? own[n][0] : bflo(w0); pv[r][1] = r == role ? own[n][1] : bfhi(w0);
      pv[r][2] = r == role ? own[n][2] : bflo(w1_); pv[r][3] = r == role ? own[n][3] : bfhi(w1_);
    }
    float o[4];
#pragma unroll
    for (int e = 0; e < 4; ++e) {
      const float y = pv[0][e] - pv[1][e], z = pv[2][e] + pv[3][e] + bb[e];
      const float gz = z * __builtin_amdgcn_rcpf(1.0f + __builtin_amdgcn_exp2f(-1.4426950408889634f * z));
      o[e] = xb[e] + y * gz;
    }
    u32x2 w; w[0] = cvtpk(o[0], o[1]); w[1] = cvtpk(o[2], o[3]);
    *(u32x2*)(out + (size_t)trow * D + ecol + 16 * n) = w;
  }
}

extern "C" void kernel_launch(void* const* d_in, const int* in_sizes, int n_in, void* d_out, int out_size, void* d_ws, size_t ws_size, hipStream_t stream) {
  const u16* x = (const u16*)d_in[0]; const u16* Cr = (const u16*)d_in[1]; const u16* Ci = (const u16*)d_in[2];
  const u16* Dr = (const u16*)d_in[3]; const u16* Di = (const u16*)d_in[4];
  const u16* w1 = (const u16*)d_in[6]; const u16* b1 = (const u16*)d_in[7]; const u16* w2 = (const u16*)d_in[8]; const u16* b2 = (const u16*)d_in[9];
  char* ws = (char*)d_ws;
  unsigned* sync = (unsigned*)ws;
  u16* xn = (u16*)(ws + (1 << 20));
  u16* abT = (u16*)(ws + (6 << 20));
  u16* h = (u16*)(ws + (18 << 20));
  unsigned char* xbuf = (unsigned char*)(ws + (40 << 20));
  (void)hipFuncSetAttribute((const void*)k_main, hipFuncAttributeMaxDynamicSharedMemorySize, KM_LDS);
  k_ln<<<S / 4, 256, 0, stream>>>(x, xn, sync, Dr, Di, w1 DBG_PASS);
  k_main<<<256, 512, KM_LDS, stream>>>(xn, Dr, Di, w1, b1, abT, h, Cr, Ci, w2, x, b2, (u16*)d_out, xbuf, sync DBG_PASS);
}
```

```cpp
#include <hip/hip_runtime.h>
#include <hip/hip_bf16.h>

typedef unsigned short u16;
typedef __bf16 bf16x8 __attribute__((ext_vector_type(8)));
typedef short s16x4 __attribute__((ext_vector_type(4)));
typedef short s16x8 __attribute__((ext_vector_type(8)));
typedef float f32x4 __attribute__((ext_vector_type(4)));
typedef float f32x2 __attribute__((ext_vector_type(2)));
typedef __bf16 bf16x2 __attribute__((ext_vector_type(2)));
typedef unsigned u32x4 __attribute__((ext_vector_type(4)));
typedef unsigned u32x2 __attribute__((ext_vector_type(2)));
#define LAS __attribute__((address_space(3)))
#define GAS __attribute__((address_space(1)))
#define DI __device__ __forceinline__

constexpr int S = 2048, D = 1024, F = 3584;

#define LEAD1 4
#define LEAD2 4
#define LEADT 8
#define LNKT 6
#define PREIN 0
#define XSLEEP 4
#define WARMC 0
#define SWARM 0
#define OUTAUX 0
#define XNAUX 0
#define EARLYT 1
#define PREBAR 1
#define DBAL 2

#define CTMAP(xcd, j) (DBAL == 2 ? ((xcd) >> 1) + 4 * ((j) >> 2) : (DBAL && (xcd) < 4) ? 2 * ((j) >> 2) + ((xcd) >> 1) : ((xcd) >> 1) * 8 + ((j) >> 2))
#define NOACQ 1
#define SC1S " sc1"
#define WAIT_V(nn) asm volatile("s_waitcnt vmcnt(%0)" ::"n"(nn) : "memory")
#define WAIT_L(n) do { asm volatile("" ::: "memory"); __builtin_amdgcn_s_waitcnt(0xC07F); asm volatile("" ::: "memory"); } while (0)
#define BAR() __builtin_amdgcn_s_barrier()
#define SCHED() __builtin_amdgcn_sched_barrier(0)

DI unsigned rfl(unsigned x) { return __builtin_amdgcn_readfirstlane(x); }
DI float bflo(unsigned v) { return __uint_as_float(v << 16); }
DI float bfhi(unsigned v) { return __uint_as_float(v & 0xffff0000u); }
DI unsigned cvtpk(float lo, float hi) { f32x2 v = {lo, hi}; bf16x2 b = __builtin_convertvector(v, bf16x2); return __builtin_bit_cast(unsigned, b); }
DI unsigned cvtpk2(f32x2 v) { bf16x2 b = __builtin_convertvector(v, bf16x2); return __builtin_bit_cast(unsigned, b); }
DI __amdgpu_buffer_rsrc_t mkrsrc(const void* p, unsigned n) { return __builtin_amdgcn_make_buffer_rsrc((void*)p, (short)0, (int)n, 0x00020000); }
DI u32x4 mksrd(const void* p, unsigned n) { const unsigned long long a = (unsigned long long)p; u32x4 r; r[0] = (unsigned)a; r[1] = (unsigned)(a >> 32) & 0xffffu; r[2] = n; r[3] = 0x00020000u; return r; }
DI void dma_m0(const u32x4& r, unsigned voff, unsigned soff, unsigned m0v) {
  asm volatile("s_mov_b32 m0, %0\n\ts_nop 0\n\tbuffer_load_dwordx4 %1, %2, %3 offen" SC1S " lds" ::"s"(m0v), "v"(voff), "s"(r), "s"(soff) : "memory");
}
template <int OFF> DI void dma_off(const u32x4& r, unsigned voff, unsigned soff) {
  asm volatile("buffer_load_dwordx4 %0, %1, %2 offen offset:%3" SC1S " lds" ::"v"(voff), "s"(r), "s"(soff), "n"(OFF) : "memory");
}
DI void touch(const u32x4& r, unsigned voff, unsigned soff, unsigned m0v) {
  asm volatile("s_mov_b32 m0, %0\n\ts_nop 0\n\tbuffer_load_dword %1, %2, %3 offen" SC1S " lds" ::"s"(m0v), "v"(voff), "s"(r), "s"(soff) : "memory");
}
DI bf16x8 ldsd128(unsigned a) { return *(const LAS bf16x8*)(size_t)a; }
DI bf16x8 ldstr(unsigned a0, unsigned a1) {
  s16x4 a = __builtin_amdgcn_ds_read_tr16_b64_v4i16((LAS s16x4*)(size_t)a0);
  s16x4 b = __builtin_amdgcn_ds_read_tr16_b64_v4i16((LAS s16x4*)(size_t)a1);
  s16x8 c = __builtin_shufflevector(a, b, 0, 1, 2, 3, 4, 5, 6, 7);
  return __builtin_bit_cast(bf16x8, c);
}
DI float wave_sum(float v) {
#define DPPADD(ctrl, rm) v += __int_as_float(__builtin_amdgcn_update_dpp(0, __float_as_int(v), ctrl, rm, 0xf, false))
  DPPADD(0x111, 0xf); DPPADD(0x112, 0xf); DPPADD(0x114, 0xf); DPPADD(0x118, 0xf); DPPADD(0x142, 0xa); DPPADD(0x143, 0xc);
#undef DPPADD
  return __int_as_float(__builtin_amdgcn_readlane(__float_as_int(v), 63));
}
#define MFMA(a, b, c) __builtin_amdgcn_mfma_f32_16x16x32_bf16(a, b, c, 0, 0, 0)
template <int V> struct IC { static constexpr int value = V; };
template <int N> DI void mfma_shadow() {
#pragma unroll
  for (int i = 0; i < N; ++i) { __builtin_amdgcn_sched_group_barrier(0x8, 1, 0); __builtin_amdgcn_sched_group_barrier(0x6, 2, 0); }
}

#define ST_DECL
#define ST_ARG
#define ST_PASS
#define DBG_ARG
#define DBG_PASS
#define ST(i)
#define STC(i)
#define STL(i)
#define ST_OUT(dbg, kid, w)

constexpr unsigned K1_B0 = 98304, K1_B1 = 122880, K1_BSLOT = 12288, KM_SCR = 147456  , KM_CNT = KM_SCR + 2048  , KM_LDS = KM_CNT + 64;
constexpr unsigned K2_R0 = 32768, K2_R1 = 65536, K2_R2 = 0, K2_Q0 = 98304, K2_Q1 = 114688, K2_Q2 = 131072;
#define KBSV 32768
constexpr unsigned KBS = KBSV;
constexpr int K1_NT = D / 64;
constexpr int NPT = LEAD2 > 2 ? LEAD2 - 2 : 0;
typedef unsigned long long u64;
typedef GAS u64 gu64;
constexpr int SY_EPOCH = 0, SY_LN = 16  , SY_D = SY_LN + 256  , SY_M = SY_D + 192  , SY_X = SY_M + 384  ;
DI u64 ld_sc1(gu64* p) { return __hip_atomic_load(p, __ATOMIC_RELAXED, __HIP_MEMORY_SCOPE_AGENT); }
DI void st_sc1(gu64* p, u64 v) { __hip_atomic_store(p, v, __ATOMIC_RELAXED, __HIP_MEMORY_SCOPE_AGENT); }
DI void wait_words(gu64* p, int n, u64 tag, int lane) {
  gu64* q = p + (lane < n ? lane : 0);
  for (unsigned spins = 0; __builtin_amdgcn_ballot_w64(ld_sc1(q) != tag) != 0ull && spins < (1u << 20); ++spins) __builtin_amdgcn_s_sleep(2);
  if (!NOACQ) { __builtin_amdgcn_fence(__ATOMIC_ACQUIRE, "agent"); WAIT_V(0); }
  asm volatile("" ::: "memory");
}

struct K1Issue {
  u32x4 ra, rb[3];
  unsigned voffA[2], voffB[3];
  unsigned sA[4];
  unsigned mA, mB, bstep;
  int leadmask;
};
struct K1Step { unsigned sa[4], kb, ma, mb; };
DI unsigned k1_phys(const K1Issue& c, int t) { return (unsigned)(t + (((c.leadmask >> (t & 3)) & 1) * LEAD1)) & (unsigned)(K1_NT - 1); }
DI K1Step k1_prep(const K1Issue& c, int tA, unsigned slotA, int tB) {
  K1Step s; const unsigned ka = k1_phys(c, tA) * 128u;
#pragma unroll
  for (int q = 0; q < 4; ++q) s.sa[q] = c.sA[q] + ka;
  s.kb = k1_phys(c, tB) * c.bstep; s.ma = c.mA + slotA; s.mb = c.mB + ((tB & 1) ? K1_BSLOT : 0u);
  return s;
}
template <int Q> DI void k1_dma(const K1Issue& c, const K1Step& s) {
  if (Q == 0) dma_m0(c.ra, c.voffA[0], s.sa[0], s.ma);
  else if (Q < 4) dma_off<(Q & 3) * 1024>(c.ra, c.voffA[Q & 1], s.sa[Q & 3]);
  else if (Q == 4) dma_m0(c.rb[0], c.voffB[0], s.kb, s.mb);
  else if (Q == 5) dma_off<1024>(c.rb[1], c.voffB[1], s.kb);
  else dma_off<2048>(c.rb[2], c.voffB[2], s.kb);
}

struct K2Issue {
  const unsigned char* Abase; const unsigned char* bigbase; const unsigned char* tchbase;
  unsigned voffA[2], voffBig[2], sBig[4], sSm[2], bigstep, astep, mBig, mSm, mScr, tchvoff, tchstep;
  int NT, leadmask, lead; bool bigIsQ;
};
DI unsigned k2_phys(const K2Issue& c, int t) { int p = t + (((c.leadmask >> (t & 7)) & 1) * c.lead); p = p >= c.NT ? p - c.NT : p; return (unsigned)p; }
DI K2Issue k2_setup(unsigned lds0, bool TR, const unsigned char* Abase, const unsigned char* Bbase, int NT, int g, int wi, int lane, int pm4, int pn8) {
  K2Issue c; const unsigned i = (unsigned)lane;
  c.Abase = Abase; c.NT = NT;
  if (!TR && LEADT == 8) { c.leadmask = 1 << ((2 * pm4 + pn8) & 7); c.lead = 8; }
  else { c.leadmask = 0x11 << ((pm4 + pn8) & 3); c.lead = 4; }
  c.astep = TR ? 8u * KBS : 128u;
#pragma unroll
  for (int par = 0; par < 2; ++par) c.voffA[par] = TR ? i * 16u : (i >> 3) * (S * 2u) + (((i & 7u) ^ (i >> 4) ^ ((unsigned)par << 2)) << 4);
  auto sApiece = [&](unsigned p, unsigned half) -> unsigned { return TR ? (p >> 1) * KBS + (p & 1u) * 1024u + half * 2048u : (half * 128u + p * 8u) * (S * 2u); };
  const unsigned idx = (unsigned)(wi * 8 + (lane & 7));
  if (g == 0) {
    c.bigbase = Bbase;
    if (TR) {
      c.bigstep = 64u * D * 2u;
#pragma unroll
      for (int par = 0; par < 2; ++par) c.voffBig[par] = (i >> 4) * (D * 2u) + ((((i & 15u) >> 1) ^ ((i >> 4) + 4u * par)) << 5) + (i & 1u) * 16u;
#pragma unroll
      for (int q = 0; q < 4; ++q) { const unsigned p = (unsigned)(4 * wi + q); c.sBig[q] = (8u * (p & 1u) + 4u * ((p >> 1) & 1u) + 16u * (p >> 2)) * (D * 2u); }
    } else {
      c.bigstep = 8u * KBS;
#pragma unroll
      for (int par = 0; par < 2; ++par) c.voffBig[par] = i * 16u;
#pragma unroll
      for (int q = 0; q < 4; ++q) { const unsigned p = (unsigned)(4 * wi + q); c.sBig[q] = (p >> 1) * KBS + (p & 1u) * 1024u; }
    }
    c.tchbase = Abase; c.tchstep = c.astep;
    c.tchvoff = TR ? (unsigned)pn8 * KBS + idx * 128u : ((unsigned)pn8 * 32u + idx) * (S * 2u);
  } else {
    c.bigbase = Abase; c.bigstep = c.astep;
#pragma unroll
    for (int par = 0; par < 2; ++par) c.voffBig[par] = c.voffA[par];
#pragma unroll
    for (int q = 0; q < 4; ++q) c.sBig[q] = sApiece((unsigned)(4 * wi + q), 1u);
    c.tchbase = Bbase;
    if (!TR) { c.tchvoff = (2u * pm4 + (idx >> 4)) * KBS + (idx & 15u) * 128u; c.tchstep = 8u * KBS; }
    else { c.tchvoff = (unsigned)(pm4 * 16 + wi * 4 + ((lane & 7) >> 1)) * (D * 2u) + (lane & 1) * 128u; c.tchstep = 64u * D * 2u; }
  }
  const unsigned psm = (unsigned)((g == 0 ? 8 : 0) + 2 * wi);
#pragma unroll
  for (int q = 0; q < 2; ++q) c.sSm[q] = sApiece(psm + q, 0u);
#pragma unroll
  for (int q = 0; q < 4; ++q) c.sBig[q] -= q * 1024u;
  c.sSm[1] -= 1024u;
  c.bigIsQ = (g == 0) != TR; c.mScr = lds0 + KM_SCR + (unsigned)(g * 4 + wi) * 256u;
  c.mBig = lds0 + ((g == 1 && !TR) ? 16384u : 0u) + (unsigned)wi * 4096u; c.mSm = lds0 + (TR ? 16384u : 0u) + psm * 1024u;
  return c;
}
DI void k2_big(const K2Issue& c, int tile, unsigned sR, unsigned sQ) {
  const u32x4 r = mksrd(c.bigbase + k2_phys(c, tile) * c.bigstep, 0x7fffffffu);
  dma_m0(r, c.voffBig[0], c.sBig[0], c.mBig + (c.bigIsQ ? sQ : sR));
  dma_off<1024>(r, c.voffBig[1], c.sBig[1]); dma_off<2048>(r, c.voffBig[0], c.sBig[2]); dma_off<3072>(r, c.voffBig[1], c.sBig[3]);
}
DI void k2_sm(const K2Issue& c, int tile, unsigned sR) {
  const u32x4 r = mksrd(c.Abase + k2_phys(c, tile) * c.astep, 0x7fffffffu);
  dma_m0(r, c.voffA[0], c.sSm[0], c.mSm + sR);
  dma_off<1024>(r, c.voffA[1], c.sSm[1]);
}
template <int TEND = LEAD2> DI void k2_touch(const K2Issue& c) {
#pragma unroll
  for (int t = 2; t < TEND; ++t) touch(mksrd(c.tchbase, 0x7fffffffu), c.tchvoff, (unsigned)t * c.tchstep, c.mScr);
}
DI void k2_pre(const K2Issue& c, int g, bool tRole, int tile) {
  const unsigned sR = tile ? K2_R1 : K2_R0, sQ = tile ? K2_Q1 : K2_Q0;
  if (tRole) { k2_sm(c, tile, sR); if (g == 1) k2_big(c, tile, sR, sQ); }
  else if (g == 0) k2_big(c, tile, sR, sQ);
}
struct K2Args { const u16* Cr; const u16* Ci; const u16* abT; const u16* h; const u16* w2; };
DI K2Issue k2_make(const K2Args& a, unsigned lds0) {
  int bid = blockIdx.x, tid = threadIdx.x; asm volatile("" : "+s"(bid), "+v"(tid));
  const int xcd = bid & 7, j = bid >> 3, pm = (xcd & 1) * 4 + (j & 3), role = xcd >> 1, pn = j >> 2;
  const int wid = (int)rfl(tid >> 6), lane = tid & 63, g = wid >> 2, wi = wid & 3;
  const bool tRole = role < 2; const int k0 = (role & 1) * (F / 2);
  const u16* A2 = tRole ? (role ? a.Ci : a.Cr) + (size_t)pm * 256 * S : a.h + (size_t)(k0 / 8) * (KBS / 2) + pm * 256 * 8;
  const u16* B2 = tRole ? a.abT + (size_t)(role * 1024 + pn * 128) * 8 : a.w2 + (size_t)k0 * D + pn * 128;
  K2Issue c = k2_setup(lds0, !tRole, (const unsigned char*)A2, (const unsigned char*)B2, tRole ? S / 64 : F / 128, g, wi, lane, j & 3, pn);
#pragma unroll
  for (int q = 0; q < 4; ++q) c.sBig[q] = rfl(c.sBig[q]);
#pragma unroll
  for (int q = 0; q < 2; ++q) c.sSm[q] = rfl(c.sSm[q]);
  c.bigstep = rfl(c.bigstep); c.astep = rfl(c.astep); c.mBig = rfl(c.mBig); c.mSm = rfl(c.mSm); c.mScr = rfl(c.mScr); c.tchstep = rfl(c.tchstep);
  asm volatile("s_nop 4" : "+s"(c.sBig[0]), "+s"(c.sBig[1]), "+s"(c.sBig[2]), "+s"(c.sBig[3]), "+s"(c.sSm[0]), "+s"(c.sSm[1]), "+s"(c.tchstep) :: "memory");
  return c;
}

template <int TYPE  , int NB>
DI void k1_group(unsigned lds0, const K1Issue& c, const K2Args& a2, bool tRole, bool perm, int wi, int lane, int pm, int col0, u16* abT, u16* h, const u16* __restrict__ b1,
                 gu64* sy, u64 tag, int ct, const u16* __restrict__ x, u16* xn ST_ARG) {
  constexpr int g = NB == 6 ? 0 : 1;
  const int fr = lane & 15, fq = lane >> 4, srow0 = pm * 256;
  constexpr unsigned BRING = NB == 6 ? K1_B0 : K1_B1, PITCH = NB * 32u;
  const unsigned sw = (unsigned)((fr >> 1) & 7);
  const unsigned aoff0 = lds0 + (unsigned)(wi * 64 + fr) * 128u + (((unsigned)fq ^ sw) << 4), aoff1 = aoff0 ^ 64u;
  unsigned boff0, boff1;
  if (TYPE == 0) { boff0 = lds0 + BRING + (unsigned)fr * 128u + (((unsigned)fq ^ sw) << 4); boff1 = boff0 ^ 64u; }
  else {
    const unsigned q = (lane & 15) >> 2, p = lane & 3, g4 = lane >> 4;
    const unsigned base = lds0 + BRING + (q + 4u * (g4 & 1u) + 16u * (g4 >> 1)) * PITCH + p * 8u;
    if (NB == 6) { boff0 = base + (g4 & 1u) * 32u; boff1 = base - (g4 & 1u) * 32u; } else { boff0 = base; boff1 = base; }
  }
  f32x4 acc[4][NB];
  const unsigned scr = lds0 + KM_SCR + (unsigned)(g * 4 + wi) * 256u;
  const K1Step s0 = k1_prep(c, 0, 0u, 0), s1 = k1_prep(c, 1, 32768u, g);
  if (TYPE == 1) touch(mksrd(b1 + col0, NB * 32u), (unsigned)lane * 4u, 0u, scr);
  WAIT_V(TYPE);
  STL(2);
  BAR();
  if (g == 0 && wi == 0) {
    if (lane == 0) st_sc1(sy + SY_LN + pm * 32 + ct, tag);
    wait_words(sy + SY_LN + pm * 32, 32, tag, lane);
  }
  BAR();
  asm volatile("" ::: "memory");
  STL(3);
  k1_dma<0>(c, s0); k1_dma<1>(c, s0); k1_dma<2>(c, s0); k1_dma<3>(c, s0);
  k1_dma<0>(c, s1); k1_dma<1>(c, s1); k1_dma<2>(c, s1); k1_dma<3>(c, s1);
  unsigned cur = 0;
  K1Step st = k1_prep(c, 2, 65536u, 1 + g);
  unsigned pa0 = aoff0, pa1 = aoff1, pb0 = boff0, pb1 = boff1;
  WAIT_V(4);
  u32x2 bb[NB];
  if (TYPE == 1) {
#pragma unroll
    for (int n = 0; n < NB; ++n) bb[n] = *(const LAS u32x2*)(size_t)(scr + (unsigned)fq * 8u + n * 32);
  }
#pragma unroll
  for (int m = 0; m < 4; ++m)
#pragma unroll
    for (int n = 0; n < NB; ++n) acc[m][n] = TYPE == 1 ? (f32x4){bflo(bb[n][0]), bfhi(bb[n][0]), bflo(bb[n][1]), bfhi(bb[n][1])} : (f32x4){0.f, 0.f, 0.f, 0.f};
  BAR();
  if (g == 1) BAR();
  SCHED();
  ST(1); STC(0);
  auto iter = [&](auto modec, int t) {
    constexpr int MODE = decltype(modec)::value;
    bf16x8 af[4][2], bfr[NB][2];
    constexpr bool va = MODE == 0, vb = MODE == 0 || (MODE == 1 && g == 0);
    auto rdB = [&](int n) {
      if (TYPE == 0) { bfr[n][0] = ldsd128(pb0 + n * 2048); bfr[n][1] = ldsd128(pb1 + n * 2048); }
      else {
        const unsigned pb = (n & 1) ? pb1 : pb0;
#pragma unroll
        for (int ks = 0; ks < 2; ++ks) bfr[n][ks] = ldstr(pb + n * 32 + (32 * ks) * PITCH, pb + n * 32 + (32 * ks + 8) * PITCH);
      }
    };
    auto rdA = [&](int m) { af[m][0] = ldsd128(pa0 + m * 2048); af[m][1] = ldsd128(pa1 + m * 2048); };
    if (vb) k1_dma<4>(c, st);
    rdA(0); rdA(1);
    if (vb) k1_dma<5>(c, st);
    rdA(2); rdA(3);
    if (vb) k1_dma<6>(c, st);
    rdB(0);
    if (va) k1_dma<0>(c, st);
    rdB(1);
    if (va) k1_dma<1>(c, st);
    rdB(2);
    if (va) k1_dma<2>(c, st);
    rdB(3);
    if (va) k1_dma<3>(c, st);
    rdB(4);
    if (NB == 6) rdB(5);
    if (MODE == 0) WAIT_V(7);
    else { if (vb) WAIT_V(3); else WAIT_V(0); }
    WAIT_L(0); BAR(); SCHED();
    __builtin_amdgcn_s_setprio(1);
#pragma unroll
    for (int ks = 0; ks < 2; ++ks)
#pragma unroll
      for (int n = 0; n < NB; ++n)
#pragma unroll
        for (int m = 0; m < 4; ++m) {
          acc[m][n] = TYPE == 0 ? MFMA(af[m][ks], bfr[n][ks], acc[m][n]) : MFMA(bfr[n][ks], af[m][ks], acc[m][n]);
        }
    if (MODE != 2) {
      st = k1_prep(c, t + 3, cur, t + 2 + g);
      cur = cur == 65536u ? 0u : cur + 32768u;
      const unsigned bs = (t & 1) ? 0u : K1_BSLOT;
      pa0 = aoff0 + cur; pa1 = aoff1 + cur; pb0 = boff0 + bs; pb1 = boff1 + bs;
      mfma_shadow<8 * NB>();
      asm volatile("" : "+s"(st.sa[0]), "+s"(st.sa[1]), "+s"(st.sa[2]), "+s"(st.sa[3]), "+s"(st.kb), "+s"(st.ma), "+s"(st.mb), "+s"(cur), "+v"(pa0), "+v"(pa1), "+v"(pb0), "+v"(pb1));
    }
    __builtin_amdgcn_s_setprio(0);
    SCHED();
    if (!(g == 1 && MODE == 2)) BAR();
    SCHED();
  };
  for (int t = 0; t < K1_NT - 2; ++t) iter(IC<0>{}, t);
  iter(IC<1>{}, K1_NT - 2);
  iter(IC<2>{}, K1_NT - 1);
  ST(2); STC(1);
  if (TYPE == 0) {
    const __amdgpu_buffer_rsrc_t ro = mkrsrc(abT + (size_t)(srow0 / 8 + 8 * wi) * (KBS / 2) + col0 * 8, 0x7fffffffu);
    const unsigned vo = (unsigned)fq * KBS + (unsigned)fr * 16u;
#pragma unroll
    for (int j = 0; j < 2; ++j)
#pragma unroll
      for (int n = 0; n < NB; ++n) {
        u32x4 w; const f32x4 a = acc[2 * j][n], b = acc[2 * j + 1][n];
        w[0] = cvtpk(a[0], a[1]); w[1] = cvtpk(a[2], a[3]); w[2] = cvtpk(b[0], b[1]); w[3] = cvtpk(b[2], b[3]);
        __builtin_amdgcn_raw_buffer_store_b128(w, ro, vo + j * 4 * KBS + n * 256, 0, 16);
      }
  } else {
    const __amdgpu_buffer_rsrc_t ro = mkrsrc(h + (size_t)(col0 / 8) * (KBS / 2) + (srow0 + 64 * wi) * 8, 0x7fffffffu);
    const unsigned rowl = perm ? (unsigned)(8 * (fr >> 2) + 4 * (fq & 1) + (fr & 3)) : (unsigned)(16 * (fq & 1) + fr);
    const unsigned vo = (unsigned)(fq >> 1) * KBS + rowl * 16u;
    auto gelu2 = [&](f32x2 v) -> unsigned {
      constexpr float c1 = -2.0f * 0.7978845608028654f * 1.4426950408889634f, c3 = c1 * 0.044715f;
      const f32x2 u = v * __builtin_elementwise_fma(v * v, (f32x2){c3, c3}, (f32x2){c1, c1});
      f32x2 e; e[0] = __builtin_amdgcn_exp2f(u[0]); e[1] = __builtin_amdgcn_exp2f(u[1]);
      const f32x2 d = e + 1.0f;
      f32x2 r; r[0] = __builtin_amdgcn_rcpf(d[0]); r[1] = __builtin_amdgcn_rcpf(d[1]);
      return cvtpk2(v * r);
    };
#pragma unroll
    for (int n = 0; n < NB; ++n)
#pragma unroll
      for (int j = 0; j < 2; ++j) {
        const f32x4 a = acc[2 * j][n], b = acc[2 * j + 1][n];
        const unsigned a0 = gelu2((f32x2){a[0], a[1]}), a1 = gelu2((f32x2){a[2], a[3]}), b0 = gelu2((f32x2){b[0], b[1]}), b1_ = gelu2((f32x2){b[2], b[3]});
        auto r0 = __builtin_amdgcn_permlane16_swap(a0, b0, false, false);
        auto r1 = __builtin_amdgcn_permlane16_swap(a1, b1_, false, false);
        u32x4 w; w[0] = r0[0]; w[1] = r1[0]; w[2] = r0[1]; w[3] = r1[1];
        __builtin_amdgcn_raw_buffer_store_b128(w, ro, vo + n * 2 * KBS + j * 512, 0, 16);
      }
  }
  WAIT_V(0);
  if (lane == 0) {
    const unsigned old = __hip_atomic_fetch_add((LAS unsigned*)(size_t)(lds0 + KM_CNT + g * 4), 1u, __ATOMIC_RELAXED, __HIP_MEMORY_SCOPE_WORKGROUP);
    if (old == 3u) {
      const int hid = TYPE == 0 ? (g == 0 ? ct : 13 + ct) : (g == 0 ? ct - 13 : 19 + ct - 10);
      st_sc1(TYPE == 0 ? sy + SY_D + hid * 8 + pm : sy + SY_M + pm * 48 + hid, tag);
    }
  }
  ST(3);
}

template <bool TR>
DI void k2_loop(unsigned lds0, const K2Issue& c, int g, int wi, int lane, f32x4 (&acc)[4][4], bool early ST_ARG) {
  const int fr = lane & 15, fq = lane >> 4, wmi = wi >> 1, wn = wi & 1;
  constexpr int NT = TR ? F / 128 : S / 64;
  const unsigned sw = (unsigned)((fr >> 1) & 7);
  if (TR) {
    if (g == 0) { k2_sm(c, 0, K2_R0); k2_sm(c, 1, K2_R1); k2_touch(c); }
    else { k2_sm(c, 0, K2_R0); k2_big(c, 0, K2_R0, K2_Q0); k2_sm(c, 1, K2_R1); k2_big(c, 1, K2_R1, K2_Q1); k2_sm(c, 2, K2_R2); }
  } else {
    if (early) {
      if (g == 0) { k2_big(c, 0, K2_R0, K2_Q0); k2_sm(c, 0, K2_R0); k2_big(c, 1, K2_R1, K2_Q1); k2_sm(c, 1, K2_R1); k2_touch<LEADT>(c); }
      else { k2_sm(c, 0, K2_R0); k2_big(c, 0, K2_R0, K2_Q0); k2_sm(c, 1, K2_R1); k2_big(c, 1, K2_R1, K2_Q1); k2_sm(c, 2, K2_R2); k2_touch(c); }
    } else {
      if (g == 0) { k2_big(c, 0, K2_R0, K2_Q0); k2_big(c, 1, K2_R1, K2_Q1); }
      else k2_touch(c);
    }
  }
  const unsigned abase = lds0 + (TR ? (g == 0 ? 16384u : 0u) : (g == 0 ? 0u : 16384u));
  const bool aIsQ = TR && g == 1;
  const unsigned aoff0 = TR ? abase + (unsigned)fq * 2048u + (unsigned)(wmi * 64 + fr) * 16u : abase + (unsigned)(wmi * 64 + fr) * 128u + (((unsigned)fq ^ sw) << 4);
  const unsigned aoff1 = aoff0 ^ 64u;
  constexpr int AM = TR ? 256 : 2048;
  unsigned boff[4];
  if (TR) {
    const unsigned q = (lane & 15) >> 2, p = lane & 3, g4 = lane >> 4, s8 = q + 4u * (g4 & 1u);
#pragma unroll
    for (int n = 0; n < 4; ++n) boff[n] = lds0 + (s8 + 16u * (g4 >> 1)) * 256u + ((((unsigned)(wn * 4 + n)) ^ s8) << 5) + p * 8u;
  } else {
    boff[0] = lds0 + (unsigned)fq * 2048u + (unsigned)(wn * 64 + fr) * 16u; boff[1] = boff[2] = boff[3] = 0;
  }
  unsigned curR = K2_R0, curQ = K2_Q0;
  u32x4 rb, rs; unsigned mb, ms, pa0, pa1, pb[4];
  auto prep = [&](int t) {
    const unsigned prvR = curR == 0u ? 65536u : curR - 32768u, prvQ = curQ == K2_Q0 ? K2_Q2 : curQ - 16384u;
    const int tsm = t + 2 + g;
    rb = mksrd(c.bigbase + k2_phys(c, t + 2) * c.bigstep, t + 2 < NT ? 0x7fffffffu : 0u);
    rs = mksrd(c.Abase + k2_phys(c, tsm) * c.astep, tsm < NT ? 0x7fffffffu : 0u);
    mb = c.mBig + (c.bigIsQ ? prvQ : prvR); ms = c.mSm + (g == 0 ? prvR : curR);
    const unsigned sa = aIsQ ? curQ : curR, sb = TR ? curR : curQ;
    pa0 = aoff0 + sa; pa1 = aoff1 + sa;
#pragma unroll
    for (int n = 0; n < (TR ? 4 : 1); ++n) pb[n] = boff[n] + sb;
  };
  prep(0);
  if (TR) {
    if (g == 0) { WAIT_V(2 + NPT); BAR(); } else { WAIT_V(12); BAR(); WAIT_V(6); BAR(); }
  } else {
    if (g == 0) { if (early) WAIT_V(6 + LEADT - 2); else WAIT_V(4); BAR(); } else { WAIT_V(12 + NPT); BAR(); WAIT_V(6 + NPT); BAR(); }
  }
  SCHED();
  ST(5); STC(2);
  auto rdA = [&](int m, int ks) -> bf16x8 { return TR ? ldsd128(pa0 + m * AM + ks * 8192) : ldsd128((ks ? pa1 : pa0) + m * AM); };
  auto rdB = [&](int n, int ks) -> bf16x8 { return TR ? ldstr(pb[n] + ks * 8192, pb[n] + ks * 8192 + 2048) : ldsd128(pb[0] + n * 256 + ks * 8192); };
  for (int t = 0; t < NT; ++t) {
    bf16x8 af[4][2], bfr[4][2];
    dma_m0(rb, c.voffBig[0], c.sBig[0], mb);
    af[0][0] = rdA(0, 0); af[0][1] = rdA(0, 1); bfr[0][0] = rdB(0, 0);
    dma_off<1024>(rb, c.voffBig[1], c.sBig[1]);
    af[1][0] = rdA(1, 0); af[1][1] = rdA(1, 1); bfr[1][0] = rdB(1, 0);
    dma_off<2048>(rb, c.voffBig[0], c.sBig[2]);
    af[2][0] = rdA(2, 0); af[2][1] = rdA(2, 1); bfr[2][0] = rdB(2, 0);
    dma_off<3072>(rb, c.voffBig[1], c.sBig[3]);
    af[3][0] = rdA(3, 0); af[3][1] = rdA(3, 1);
    dma_m0(rs, c.voffA[0], c.sSm[0], ms);
    bfr[3][0] = rdB(3, 0); bfr[0][1] = rdB(0, 1); bfr[1][1] = rdB(1, 1);
    dma_off<1024>(rs, c.voffA[1], c.sSm[1]);
    bfr[2][1] = rdB(2, 1); bfr[3][1] = rdB(3, 1);
    WAIT_L(0); BAR(); SCHED();
    __builtin_amdgcn_s_setprio(1);
#pragma unroll
    for (int ks = 0; ks < 2; ++ks)
#pragma unroll
      for (int n = 0; n < 4; ++n)
#pragma unroll
        for (int m = 0; m < 4; ++m) acc[m][n] = MFMA(bfr[n][ks], af[m][ks], acc[m][n]);
    curR = curR == 65536u ? 0u : curR + 32768u; curQ = curQ == K2_Q2 ? K2_Q0 : curQ + 16384u;
    prep(t + 1);
    mfma_shadow<32>();
    if (TR) asm volatile("" : "+s"(rb), "+s"(rs), "+s"(mb), "+s"(ms), "+s"(curR), "+s"(curQ), "+v"(pa0), "+v"(pb[0]), "+v"(pb[1]), "+v"(pb[2]), "+v"(pb[3]));
    else asm volatile("" : "+s"(rb), "+s"(rs), "+s"(mb), "+s"(ms), "+s"(curR), "+s"(curQ), "+v"(pa0), "+v"(pa1), "+v"(pb[0]));
    __builtin_amdgcn_s_setprio(0);
    SCHED();
    WAIT_V(6);
    if (!(g == 1 && t == NT - 1)) BAR();
    SCHED();
  }
  WAIT_V(0);
}

__global__ __launch_bounds__(512, 2) void k_main(u16* xn, const u16* __restrict__ Dr, const u16* __restrict__ Di, const u16* __restrict__ w1,
                                                 const u16* __restrict__ b1, u16* abT, u16* h, const u16* __restrict__ Cr, const u16* __restrict__ Ci,
                                                 const u16* __restrict__ w2, const u16* __restrict__ x, const u16* __restrict__ b2, u16* __restrict__ out,
                                                 unsigned char* xbuf, unsigned long long* sync DBG_ARG) {
  ST_DECL; ST(0);
  extern __shared__ __attribute__((aligned(16))) unsigned char smem[];
  const unsigned lds0 = (unsigned)(size_t)(LAS unsigned char*)smem;
  asm volatile("" : "+s"(xn), "+s"(Dr), "+s"(Di), "+s"(w1), "+s"(b1), "+s"(x), "+s"(sync));
  const int bid = blockIdx.x, xcd = bid & 7, j = bid >> 3;
  const int pm = (xcd & 1) * 4 + (j & 3), ct = CTMAP(xcd, j);
  const int role = xcd >> 1, pn = j >> 2, tile = pm * 8 + pn;
  const int tid = threadIdx.x, wid = (int)rfl(tid >> 6), lane = tid & 63, g = wid >> 2, wi = wid & 3;
  gu64* sy = (gu64*)sync;
  const int lnrow = pm * 256 + ct * 8 + wid;
  const unsigned xl = lds0 + 65536u + (unsigned)wid * 2048u;
  { const u32x4 rx = mksrd(x + (size_t)lnrow * D, D * 2u); dma_m0(rx, (unsigned)lane * 16u, 0u, xl); dma_off<1024>(rx, (unsigned)lane * 16u, 0u); }
  u64 tag;
  if (tid < 2) *(LAS unsigned*)(size_t)(lds0 + KM_CNT + tid * 4) = 0u;
  const int type0 = ct < 13 ? 0 : 1, col00 = ct < 13 ? 96 * ct : 96 * (ct - 13);
  const int type1 = ct < 10 ? 0 : 1, col01 = ct < 10 ? 1248 + 80 * ct : 1824 + 80 * (ct - 10);
  const bool perm = type0 == 0;
  K1Issue c;
  c.ra = mksrd(xn + (size_t)pm * 256 * D, 0x7fffffffu);
  c.leadmask = 1 << (((j & 3) + (j >> 2)) & 3);
  const unsigned i = (unsigned)lane;
#pragma unroll
  for (int par = 0; par < 2; ++par) c.voffA[par] = (perm ? 8u * (i >> 5) + ((i >> 3) & 3u) : (i >> 3)) * (D * 2u) + (((i & 7u) ^ (i >> 4) ^ ((unsigned)par << 2)) << 4);
#pragma unroll
  for (int q = 0; q < 4; ++q) { const unsigned p = (unsigned)(g * 16 + wi * 4 + q); c.sA[q] = (perm ? 16u * (p & 1u) + 4u * ((p >> 1) & 1u) + 32u * (p >> 2) : 8u * p) * (D * 2u) - q * 1024u; }
  c.mA = lds0 + (unsigned)(g * 16 + wi * 4) * 1024u;
  const int otype = g == 0 ? type1 : type0, ocol = g == 0 ? col01 : col00;
  const unsigned cpr = g == 0 ? 10u : 12u, oring = g == 0 ? K1_B1 : K1_B0;
  c.bstep = otype == 0 ? 128u : 64u * F * 2u;
  c.mB = lds0 + oring + (unsigned)wi * 3072u;
#pragma unroll
  for (int q = 0; q < 3; ++q) {
    const unsigned p = (unsigned)(3 * wi + q), nr = p < cpr ? 0x7fffffffu : 0u;
    if (otype == 0) {
      int e0 = ocol + 8 * (int)p; if (e0 > 2040) e0 = 2040;
      c.rb[q] = mksrd((const unsigned char*)(e0 < 1024 ? Dr + (size_t)e0 * D : Di + (size_t)(e0 - 1024) * D) - q * 1024, nr);
      c.voffB[q] = (i >> 3) * (D * 2u) + (((i & 7u) ^ (i >> 4) ^ ((p & 1u) << 2)) << 4);
    } else {
      const unsigned ci = 64u * p + i, rho = ci / cpr, pos = ci - rho * cpr, sp = pos >> 1;
      const unsigned seg = cpr == 12u ? sp ^ ((rho >> 2) & 1u) : sp;
      const unsigned k = (rho & 3u) + 8u * ((rho >> 2) & 1u) + 4u * ((rho >> 3) & 1u) + (rho & 48u);
      c.rb[q] = mksrd((const unsigned char*)(w1 + ocol) - q * 1024, nr);
      c.voffB[q] = k * (F * 2u) + seg * 32u + (pos & 1u) * 16u;
    }
  }
  const bool tRole = role < 2;
  const K2Args a2 = {Cr, Ci, abT, h, w2};
  {
    const K1Step s0 = k1_prep(c, 0, 0u, 0), s1 = k1_prep(c, 1, 32768u, g);
    if (g == 1) { k1_dma<4>(c, s0); k1_dma<5>(c, s0); k1_dma<6>(c, s0); }
    k1_dma<4>(c, s1); k1_dma<5>(c, s1); k1_dma<6>(c, s1);
    if (g == 1) WAIT_V(6); else WAIT_V(3);
    const u32x4 v0 = *(const LAS u32x4*)(size_t)(xl + lane * 16u), v1 = *(const LAS u32x4*)(size_t)(xl + 1024u + lane * 16u);
    float f[16];
#pragma unroll
    for (int q = 0; q < 4; ++q) { f[2 * q] = bflo(v0[q]); f[2 * q + 1] = bfhi(v0[q]); f[8 + 2 * q] = bflo(v1[q]); f[9 + 2 * q] = bfhi(v1[q]); }
    float sm = 0.f;
#pragma unroll
    for (int q = 0; q < 16; ++q) sm += f[q];
    asm volatile("" : "+v"(sm)); STL(0);
    sm = wave_sum(sm);
    const float mean = sm * (1.0f / D);
    float sq = 0.f;
#pragma unroll
    for (int q = 0; q < 16; ++q) { f[q] -= mean; sq += f[q] * f[q]; }
    sq = wave_sum(sq);
    const float rs = 1.0f / sqrtf(sq * (1.0f / D) + 1e-5f);
    u32x4 o0, o1;
#pragma unroll
    for (int q = 0; q < 4; ++q) { o0[q] = cvtpk(f[2 * q] * rs, f[2 * q + 1] * rs); o1[q] = cvtpk(f[8 + 2 * q] * rs, f[9 + 2 * q] * rs); }
    const __amdgpu_buffer_rsrc_t ro = mkrsrc(xn + (size_t)lnrow * D, D * 2u);
    __builtin_amdgcn_raw_buffer_store_b128(o0, ro, lane * 16, 0, 16); __builtin_amdgcn_raw_buffer_store_b128(o1, ro, lane * 16 + 1024, 0, 16);
    asm volatile("s_load_dwordx2 %0, %1, 0x0\n\ts_waitcnt lgkmcnt(0)" : "=s"(tag) : "s"(sync + SY_EPOCH) : "memory");
    tag += 1ull;
    STL(1);
  }
  if (g == 0) {
    if (type0 == 0) k1_group<0, 6>(lds0, c, a2, tRole, perm, wi, lane, pm, col00, abT, h, b1, sy, tag, ct, x, xn ST_PASS);
    else k1_group<1, 6>(lds0, c, a2, tRole, perm, wi, lane, pm, col00, abT, h, b1, sy, tag, ct, x, xn ST_PASS);
  } else {
    if (type1 == 0) k1_group<0, 5>(lds0, c, a2, tRole, perm, wi, lane, pm, col01, abT, h, b1, sy, tag, ct, x, xn ST_PASS);
    else k1_group<1, 5>(lds0, c, a2, tRole, perm, wi, lane, pm, col01, abT, h, b1, sy, tag, ct, x, xn ST_PASS);
  }
  const K2Issue c2 = k2_make(a2, lds0);
  auto wait_deps = [&]() {
    if (tRole) {
      const int eb = role * 8 + pn, lo = eb * 128, hi = lo + 127;
      const int hlo = lo < 1248 ? lo / 96 : 13 + (lo - 1248) / 80, hhi = hi < 1248 ? hi / 96 : 13 + (hi - 1248) / 80;
      wait_words(sy + SY_D + hlo * 8, (hhi - hlo + 1) * 8, tag, lane);
    } else wait_words(sy + SY_M + pm * 48 + (role == 2 ? 0 : 18), role == 2 ? 19 : 23, tag, lane);
  };
  const bool early = EARLYT && tRole && type1 == 1;
  if (early && wid == 0) wait_deps();
  BAR();
  asm volatile("" ::: "memory");
  if (!early) {
    k2_pre(c2, g, tRole, 0); k2_pre(c2, g, tRole, 1);
    if (tRole) { if (g == 0) k2_touch<LEADT>(c2); else k2_sm(c2, 2, K2_R2); }
    else if (g == 1) k2_touch(c2);
    if (wid == 0) wait_deps();
    BAR();
    asm volatile("" ::: "memory");
  }
  ST(4);
  f32x4 acc[4][4];
#pragma unroll
  for (int m = 0; m < 4; ++m)
#pragma unroll
    for (int n = 0; n < 4; ++n) acc[m][n] = (f32x4){0.f, 0.f, 0.f, 0.f};
  if (tRole) k2_loop<false>(lds0, c2, g, wi, lane, acc, early ST_PASS);
  else k2_loop<true>(lds0, c2, g, wi, lane, acc, false ST_PASS);
  ST(6); STC(3);
  const __amdgpu_buffer_rsrc_t xr = mkrsrc(xbuf + (size_t)tile * (4 * 8 * 8 * 1024), 4u * 8u * 8u * 1024u);
  const __amdgpu_buffer_rsrc_t xw = mkrsrc(xbuf + (size_t)tile * (4 * 8 * 8 * 1024) + (role * 8 + wid) * 8192, 8192u);
  const int fr = lane & 15, fq = lane >> 4, wmi = wi >> 1, wn = wi & 1;
  const int trow = pm * 256 + g * 128 + wmi * 64 + role * 16 + fr, ecol = pn * 128 + wn * 64 + 4 * fq;
#pragma unroll
  for (int m = 0; m < 4; ++m)
    if (m != role) {
#pragma unroll
      for (int pr = 0; pr < 2; ++pr) {
        const f32x4 a = acc[m][2 * pr], b = acc[m][2 * pr + 1];
        u32x4 w; w[0] = cvtpk(a[0], a[1]); w[1] = cvtpk(a[2], a[3]); w[2] = cvtpk(b[0], b[1]); w[3] = cvtpk(b[2], b[3]);
        __builtin_amdgcn_raw_buffer_store_b128(w, xw, lane * 16 + (m * 2 + pr) * 1024, 0, 16);
      }
    }
  gu64* fl = sy + SY_X + (tile * 8 + wid) * 8;
  u64 f0 = ld_sc1(fl + (lane & 3));
  u32x2 xv[4], bv[4];
#pragma unroll
  for (int n = 0; n < 4; ++n) { xv[n] = *(const u32x2*)(x + (size_t)trow * D + ecol + 16 * n); bv[n] = *(const u32x2*)(b2 + ecol + 16 * n); }
  f32x4 own[4];
#pragma unroll
  for (int n = 0; n < 4; ++n) own[n] = role == 0 ? acc[0][n] : role == 1 ? acc[1][n] : role == 2 ? acc[2][n] : acc[3][n];
  WAIT_V(0);
  if (lane == 0) st_sc1(fl + role, tag);
  u32x4 pk[4][2];
#pragma unroll
  for (int r = 0; r < 4; ++r) { pk[r][0] = (u32x4){0u, 0u, 0u, 0u}; pk[r][1] = (u32x4){0u, 0u, 0u, 0u}; }
  unsigned have = 1u << role;
  for (unsigned spins = 0;;) {
    const unsigned rdy = (unsigned)__builtin_amdgcn_ballot_w64(f0 == tag) & 15u;
    const unsigned nw = rdy & ~have;
    asm volatile("" ::: "memory");
    have |= nw;
    if (have != 15u) f0 = ld_sc1(fl + (lane & 3));
#pragma unroll
    for (int r = 0; r < 4; ++r)
      if (nw & (1u << r)) {
#pragma unroll
        for (int pr = 0; pr < 2; ++pr) pk[r][pr] = __builtin_bit_cast(u32x4, __builtin_amdgcn_raw_buffer_load_b128(xr, lane * 16, ((r * 8 + wid) * 8 + role * 2 + pr) * 1024, 16));
      }
    if (have == 15u || ++spins > (1u << 20)) break;
    if (!nw) __builtin_amdgcn_s_sleep(XSLEEP);
  }
#pragma unroll
  for (int n = 0; n < 4; ++n) {
    const float xb[4] = {bflo(xv[n][0]), bfhi(xv[n][0]), bflo(xv[n][1]), bfhi(xv[n][1])};
    const float bb[4] = {bflo(bv[n][0]), bfhi(bv[n][0]), bflo(bv[n][1]), bfhi(bv[n][1])};
    float pv[4][4];
#pragma unroll
    for (int r = 0; r < 4; ++r) {
      const unsigned w0 = pk[r][n >> 1][(n & 1) * 2], w1_ = pk[r][n >> 1][(n & 1) * 2 + 1];
      pv[r][0] = r == role ? own[n][0] : bflo(w0); pv[r][1] = r == role ? own[n][1] : bfhi(w0);
      pv[r][2] = r == role ? own[n][2] : bflo(w1_); pv[r][3] = r == role ? own[n][3] : bfhi(w1_);
    }
    float o[4];
#pragma unroll
    for (int e = 0; e < 4; ++e) {
      const float y = pv[0][e] - pv[1][e], z = pv[2][e] + pv[3][e] + bb[e];
      const float gz = z * __builtin_amdgcn_rcpf(1.0f + __builtin_amdgcn_exp2f(-1.4426950408889634f * z));
      o[e] = xb[e] + y * gz;
    }
    u32x2 w; w[0] = cvtpk(o[0], o[1]); w[1] = cvtpk(o[2], o[3]);
    __builtin_amdgcn_raw_buffer_store_b64(w, mkrsrc(out, (unsigned)(S * D * 2)), (unsigned)(trow * D + ecol + 16 * n) * 2u, 0, OUTAUX);
  }
  if (bid == 0 && wid == 0) {
#pragma unroll
    for (int q = 0; q < 4; ++q) { gu64* pq = sy + SY_LN + q * 64 + lane; for (unsigned spins = 0; __builtin_amdgcn_ballot_w64(ld_sc1(pq) != tag) != 0ull && spins < (1u << 20); ++spins) __builtin_amdgcn_s_sleep(2); }
    if (lane == 0) st_sc1(sy + SY_EPOCH, tag);
  }
}

extern "C" void kernel_launch(void* const* d_in, const int* in_sizes, int n_in, void* d_out, int out_size, void* d_ws, size_t ws_size, hipStream_t stream) {
  const u16* x = (const u16*)d_in[0]; const u16* Cr = (const u16*)d_in[1]; const u16* Ci = (const u16*)d_in[2];
  const u16* Dr = (const u16*)d_in[3]; const u16* Di = (const u16*)d_in[4];
  const u16* w1 = (const u16*)d_in[6]; const u16* b1 = (const u16*)d_in[7]; const u16* w2 = (const u16*)d_in[8]; const u16* b2 = (const u16*)d_in[9];
  char* ws = (char*)d_ws;
  unsigned long long* sync = (unsigned long long*)ws;
  u16* xn = (u16*)(ws + (1 << 20));
  u16* abT = (u16*)(ws + (6 << 20));
  u16* h = (u16*)(ws + (18 << 20));
  unsigned char* xbuf = (unsigned char*)(ws + (40 << 20));
  (void)hipFuncSetAttribute((const void*)k_main, hipFuncAttributeMaxDynamicSharedMemorySize, KM_LDS);
  k_main<<<256, 512, KM_LDS, stream>>>(xn, Dr, Di, w1, b1, abT, h, Cr, Ci, w2, x, b2, (u16*)d_out, xbuf, sync DBG_PASS);
}
```

```cpp
#include <hip/hip_runtime.h>
#include <hip/hip_bf16.h>

typedef unsigned short u16;
typedef __bf16 bf16x8 __attribute__((ext_vector_type(8)));
typedef short s16x4 __attribute__((ext_vector_type(4)));
typedef short s16x8 __attribute__((ext_vector_type(8)));
typedef float f32x4 __attribute__((ext_vector_type(4)));
typedef float f32x2 __attribute__((ext_vector_type(2)));
typedef __bf16 bf16x2 __attribute__((ext_vector_type(2)));
typedef unsigned u32x4 __attribute__((ext_vector_type(4)));
typedef unsigned u32x2 __attribute__((ext_vector_type(2)));
#define LAS __attribute__((address_space(3)))
#define GAS __attribute__((address_space(1)))
#define DI __device__ __forceinline__

constexpr int S = 2048, D = 1024, F = 3584;

#define LEAD1 4
#define LEAD2 4
#define LEADT 8
#define LNKT 6
#define PREIN 0
#define XSLEEP 4
#define WARMC 0
#define SWARM 0
#define OUTAUX 0
#define XNAUX 0
#define TLBW 1
#define EARLYT 1
#define PREBAR 1
#define DBAL 2

#define CTMAP(xcd, j) (DBAL == 2 ? ((xcd) >> 1) + 4 * ((j) >> 2) : (DBAL && (xcd) < 4) ? 2 * ((j) >> 2) + ((xcd) >> 1) : ((xcd) >> 1) * 8 + ((j) >> 2))
#define NOACQ 1
#define SC1S " sc1"
#define WAIT_V(nn) asm volatile("s_waitcnt vmcnt(%0)" ::"n"(nn) : "memory")
#define WAIT_L(n) do { asm volatile("" ::: "memory"); __builtin_amdgcn_s_waitcnt(0xC07F); asm volatile("" ::: "memory"); } while (0)
#define BAR() __builtin_amdgcn_s_barrier()
#define SCHED() __builtin_amdgcn_sched_barrier(0)

DI unsigned rfl(unsigned x) { return __builtin_amdgcn_readfirstlane(x); }
DI float bflo(unsigned v) { return __uint_as_float(v << 16); }
DI float bfhi(unsigned v) { return __uint_as_float(v & 0xffff0000u); }
DI unsigned cvtpk(float lo, float hi) { f32x2 v = {lo, hi}; bf16x2 b = __builtin_convertvector(v, bf16x2); return __builtin_bit_cast(unsigned, b); }
DI unsigned cvtpk2(f32x2 v) { bf16x2 b = __builtin_convertvector(v, bf16x2); return __builtin_bit_cast(unsigned, b); }
DI __amdgpu_buffer_rsrc_t mkrsrc(const void* p, unsigned n) { return __builtin_amdgcn_make_buffer_rsrc((void*)p, (short)0, (int)n, 0x00020000); }
DI u32x4 mksrd(const void* p, unsigned n) { const unsigned long long a = (unsigned long long)p; u32x4 r; r[0] = (unsigned)a; r[1] = (unsigned)(a >> 32) & 0xffffu; r[2] = n; r[3] = 0x00020000u; return r; }
DI void dma_m0(const u32x4& r, unsigned voff, unsigned soff, unsigned m0v) {
  asm volatile("s_mov_b32 m0, %0\n\ts_nop 0\n\tbuffer_load_dwordx4 %1, %2, %3 offen" SC1S " lds" ::"s"(m0v), "v"(voff), "s"(r), "s"(soff) : "memory");
}
template <int OFF> DI void dma_off(const u32x4& r, unsigned voff, unsigned soff) {
  asm volatile("buffer_load_dwordx4 %0, %1, %2 offen offset:%3" SC1S " lds" ::"v"(voff), "s"(r), "s"(soff), "n"(OFF) : "memory");
}
DI void touch(const u32x4& r, unsigned voff, unsigned soff, unsigned m0v) {
  asm volatile("s_mov_b32 m0, %0\n\ts_nop 0\n\tbuffer_load_dword %1, %2, %3 offen" SC1S " lds" ::"s"(m0v), "v"(voff), "s"(r), "s"(soff) : "memory");
}
DI bf16x8 ldsd128(unsigned a) { return *(const LAS bf16x8*)(size_t)a; }
DI bf16x8 ldstr(unsigned a0, unsigned a1) {
  s16x4 a = __builtin_amdgcn_ds_read_tr16_b64_v4i16((LAS s16x4*)(size_t)a0);
  s16x4 b = __builtin_amdgcn_ds_read_tr16_b64_v4i16((LAS s16x4*)(size_t)a1);
  s16x8 c = __builtin_shufflevector(a, b, 0, 1, 2, 3, 4, 5, 6, 7);
  return __builtin_bit_cast(bf16x8, c);
}
DI float wave_sum(float v) {
#define DPPADD(ctrl, rm) v += __int_as_float(__builtin_amdgcn_update_dpp(0, __float_as_int(v), ctrl, rm, 0xf, false))
  DPPADD(0x111, 0xf); DPPADD(0x112, 0xf); DPPADD(0x114, 0xf); DPPADD(0x118, 0xf); DPPADD(0x142, 0xa); DPPADD(0x143, 0xc);
#undef DPPADD
  return __int_as_float(__builtin_amdgcn_readlane(__float_as_int(v), 63));
}
#define MFMA(a, b, c) __builtin_amdgcn_mfma_f32_16x16x32_bf16(a, b, c, 0, 0, 0)
template <int V> struct IC { static constexpr int value = V; };
template <int N> DI void mfma_shadow() {
#pragma unroll
  for (int i = 0; i < N; ++i) { __builtin_amdgcn_sched_group_barrier(0x8, 1, 0); __builtin_amdgcn_sched_group_barrier(0x6, 2, 0); }
}

#define ST_DECL
#define ST_ARG
#define ST_PASS
#define DBG_ARG
#define DBG_PASS
#define ST(i)
#define STC(i)
#define STL(i)
#define ST_OUT(dbg, kid, w)

constexpr unsigned K1_B0 = 98304, K1_B1 = 122880, K1_BSLOT = 12288, KM_SCR = 147456  , KM_CNT = KM_SCR + 2048  , KM_LDS = KM_CNT + 64;
constexpr unsigned K2_R0 = 32768, K2_R1 = 65536, K2_R2 = 0, K2_Q0 = 98304, K2_Q1 = 114688, K2_Q2 = 131072;
#define KBSV 32768
constexpr unsigned KBS = KBSV;
constexpr int K1_NT = D / 64;
constexpr int NPT = LEAD2 > 2 ? LEAD2 - 2 : 0;
typedef unsigned long long u64;
typedef GAS u64 gu64;
constexpr int SY_EPOCH = 0, SY_LN = 16  , SY_D = SY_LN + 256  , SY_M = SY_D + 192  , SY_X = SY_M + 384  ;
DI u64 ld_sc1(gu64* p) { return __hip_atomic_load(p, __ATOMIC_RELAXED, __HIP_MEMORY_SCOPE_AGENT); }
DI void st_sc1(gu64* p, u64 v) { __hip_atomic_store(p, v, __ATOMIC_RELAXED, __HIP_MEMORY_SCOPE_AGENT); }
DI void wait_words(gu64* p, int n, u64 tag, int lane) {
  gu64* q = p + (lane < n ? lane : 0);
  for (unsigned spins = 0; __builtin_amdgcn_ballot_w64(ld_sc1(q) != tag) != 0ull && spins < (1u << 20); ++spins) __builtin_amdgcn_s_sleep(2);
  if (!NOACQ) { __builtin_amdgcn_fence(__ATOMIC_ACQUIRE, "agent"); WAIT_V(0); }
  asm volatile("" ::: "memory");
}

struct K1Issue {
  u32x4 ra, rb[3];
  unsigned voffA[2], voffB[3];
  unsigned sA[4];
  unsigned mA, mB, bstep;
  int leadmask;
};
struct K1Step { unsigned sa[4], kb, ma, mb; };
DI unsigned k1_phys(const K1Issue& c, int t) { return (unsigned)(t + (((c.leadmask >> (t & 3)) & 1) * LEAD1)) & (unsigned)(K1_NT - 1); }
DI K1Step k1_prep(const K1Issue& c, int tA, unsigned slotA, int tB) {
  K1Step s; const unsigned ka = k1_phys(c, tA) * 128u;
#pragma unroll
  for (int q = 0; q < 4; ++q) s.sa[q] = c.sA[q] + ka;
  s.kb = k1_phys(c, tB) * c.bstep; s.ma = c.mA + slotA; s.mb = c.mB + ((tB & 1) ? K1_BSLOT : 0u);
  return s;
}
template <int Q> DI void k1_dma(const K1Issue& c, const K1Step& s) {
  if (Q == 0) dma_m0(c.ra, c.voffA[0], s.sa[0], s.ma);
  else if (Q < 4) dma_off<(Q & 3) * 1024>(c.ra, c.voffA[Q & 1], s.sa[Q & 3]);
  else if (Q == 4) dma_m0(c.rb[0], c.voffB[0], s.kb, s.mb);
  else if (Q == 5) dma_off<1024>(c.rb[1], c.voffB[1], s.kb);
  else dma_off<2048>(c.rb[2], c.voffB[2], s.kb);
}

struct K2Issue {
  const unsigned char* Abase; const unsigned char* bigbase; const unsigned char* tchbase;
  unsigned voffA[2], voffBig[2], sBig[4], sSm[2], bigstep, astep, mBig, mSm, mScr, tchvoff, tchstep;
  int NT, leadmask, lead; bool bigIsQ;
};
DI unsigned k2_phys(const K2Issue& c, int t) { int p = t + (((c.leadmask >> (t & 7)) & 1) * c.lead); p = p >= c.NT ? p - c.NT : p; return (unsigned)p; }
DI K2Issue k2_setup(unsigned lds0, bool TR, const unsigned char* Abase, const unsigned char* Bbase, int NT, int g, int wi, int lane, int pm4, int pn8) {
  K2Issue c; const unsigned i = (unsigned)lane;
  c.Abase = Abase; c.NT = NT;
  if (!TR && LEADT == 8) { c.leadmask = 1 << ((2 * pm4 + pn8) & 7); c.lead = 8; }
  else { c.leadmask = 0x11 << ((pm4 + pn8) & 3); c.lead = 4; }
  c.astep = TR ? 8u * KBS : 128u;
#pragma unroll
  for (int par = 0; par < 2; ++par) c.voffA[par] = TR ? i * 16u : (i >> 3) * (S * 2u) + (((i & 7u) ^ (i >> 4) ^ ((unsigned)par << 2)) << 4);
  auto sApiece = [&](unsigned p, unsigned half) -> unsigned { return TR ? (p >> 1) * KBS + (p & 1u) * 1024u + half * 2048u : (half * 128u + p * 8u) * (S * 2u); };
  const unsigned idx = (unsigned)(wi * 8 + (lane & 7));
  if (g == 0) {
    c.bigbase = Bbase;
    if (TR) {
      c.bigstep = 64u * D * 2u;
#pragma unroll
      for (int par = 0; par < 2; ++par) c.voffBig[par] = (i >> 4) * (D * 2u) + ((((i & 15u) >> 1) ^ ((i >> 4) + 4u * par)) << 5) + (i & 1u) * 16u;
#pragma unroll
      for (int q = 0; q < 4; ++q) { const unsigned p = (unsigned)(4 * wi + q); c.sBig[q] = (8u * (p & 1u) + 4u * ((p >> 1) & 1u) + 16u * (p >> 2)) * (D * 2u); }
    } else {
      c.bigstep = 8u * KBS;
#pragma unroll
      for (int par = 0; par < 2; ++par) c.voffBig[par] = i * 16u;
#pragma unroll
      for (int q = 0; q < 4; ++q) { const unsigned p = (unsigned)(4 * wi + q); c.sBig[q] = (p >> 1) * KBS + (p & 1u) * 1024u; }
    }
    c.tchbase = Abase; c.tchstep = c.astep;
    c.tchvoff = TR ? (unsigned)pn8 * KBS + idx * 128u : ((unsigned)pn8 * 32u + idx) * (S * 2u);
  } else {
    c.bigbase = Abase; c.bigstep = c.astep;
#pragma unroll
    for (int par = 0; par < 2; ++par) c.voffBig[par] = c.voffA[par];
#pragma unroll
    for (int q = 0; q < 4; ++q) c.sBig[q] = sApiece((unsigned)(4 * wi + q), 1u);
    c.tchbase = Bbase;
    if (!TR) { c.tchvoff = (2u * pm4 + (idx >> 4)) * KBS + (idx & 15u) * 128u; c.tchstep = 8u * KBS; }
    else { c.tchvoff = (unsigned)(pm4 * 16 + wi * 4 + ((lane & 7) >> 1)) * (D * 2u) + (lane & 1) * 128u; c.tchstep = 64u * D * 2u; }
  }
  const unsigned psm = (unsigned)((g == 0 ? 8 : 0) + 2 * wi);
#pragma unroll
  for (int q = 0; q < 2; ++q) c.sSm[q] = sApiece(psm + q, 0u);
#pragma unroll
  for (int q = 0; q < 4; ++q) c.sBig[q] -= q * 1024u;
  c.sSm[1] -= 1024u;
  c.bigIsQ = (g == 0) != TR; c.mScr = lds0 + KM_SCR + (unsigned)(g * 4 + wi) * 256u;
  c.mBig = lds0 + ((g == 1 && !TR) ? 16384u : 0u) + (unsigned)wi * 4096u; c.mSm = lds0 + (TR ? 16384u : 0u) + psm * 1024u;
  return c;
}
DI void k2_big(const K2Issue& c, int tile, unsigned sR, unsigned sQ) {
  const u32x4 r = mksrd(c.bigbase + k2_phys(c, tile) * c.bigstep, 0x7fffffffu);
  dma_m0(r, c.voffBig[0], c.sBig[0], c.mBig + (c.bigIsQ ? sQ : sR));
  dma_off<1024>(r, c.voffBig[1], c.sBig[1]); dma_off<2048>(r, c.voffBig[0], c.sBig[2]); dma_off<3072>(r, c.voffBig[1], c.sBig[3]);
}
DI void k2_sm(const K2Issue& c, int tile, unsigned sR) {
  const u32x4 r = mksrd(c.Abase + k2_phys(c, tile) * c.astep, 0x7fffffffu);
  dma_m0(r, c.voffA[0], c.sSm[0], c.mSm + sR);
  dma_off<1024>(r, c.voffA[1], c.sSm[1]);
}
template <int TEND = LEAD2> DI void k2_touch(const K2Issue& c) {
#pragma unroll
  for (int t = 2; t < TEND; ++t) touch(mksrd(c.tchbase, 0x7fffffffu), c.tchvoff, (unsigned)t * c.tchstep, c.mScr);
}
DI void k2_pre(const K2Issue& c, int g, bool tRole, int tile) {
  const unsigned sR = tile ? K2_R1 : K2_R0, sQ = tile ? K2_Q1 : K2_Q0;
  if (tRole) { k2_sm(c, tile, sR); if (g == 1) k2_big(c, tile, sR, sQ); }
  else if (g == 0) k2_big(c, tile, sR, sQ);
}
struct K2Args { const u16* Cr; const u16* Ci; const u16* abT; const u16* h; const u16* w2; };
DI K2Issue k2_make(const K2Args& a, unsigned lds0) {
  int bid = blockIdx.x, tid = threadIdx.x; asm volatile("" : "+s"(bid), "+v"(tid));
  const int xcd = bid & 7, j = bid >> 3, pm = (xcd & 1) * 4 + (j & 3), role = xcd >> 1, pn = j >> 2;
  const int wid = (int)rfl(tid >> 6), lane = tid & 63, g = wid >> 2, wi = wid & 3;
  const bool tRole = role < 2; const int k0 = (role & 1) * (F / 2);
  const u16* A2 = tRole ? (role ? a.Ci : a.Cr) + (size_t)pm * 256 * S : a.h + (size_t)(k0 / 8) * (KBS / 2) + pm * 256 * 8;
  const u16* B2 = tRole ? a.abT + (size_t)(role * 1024 + pn * 128) * 8 : a.w2 + (size_t)k0 * D + pn * 128;
  K2Issue c = k2_setup(lds0, !tRole, (const unsigned char*)A2, (const unsigned char*)B2, tRole ? S / 64 : F / 128, g, wi, lane, j & 3, pn);
#pragma unroll
  for (int q = 0; q < 4; ++q) c.sBig[q] = rfl(c.sBig[q]);
#pragma unroll
  for (int q = 0; q < 2; ++q) c.sSm[q] = rfl(c.sSm[q]);
  c.bigstep = rfl(c.bigstep); c.astep = rfl(c.astep); c.mBig = rfl(c.mBig); c.mSm = rfl(c.mSm); c.mScr = rfl(c.mScr); c.tchstep = rfl(c.tchstep);
  asm volatile("s_nop 4" : "+s"(c.sBig[0]), "+s"(c.sBig[1]), "+s"(c.sBig[2]), "+s"(c.sBig[3]), "+s"(c.sSm[0]), "+s"(c.sSm[1]), "+s"(c.tchstep) :: "memory");
  return c;
}

template <int TYPE  , int NB>
DI void k1_group(unsigned lds0, const K1Issue& c, const K2Args& a2, bool tRole, bool perm, int wi, int lane, int pm, int col0, u16* abT, u16* h, const u16* __restrict__ b1,
                 gu64* sy, u64 tag, int ct, const unsigned char* tw ST_ARG) {
  constexpr int g = NB == 6 ? 0 : 1;
  const int fr = lane & 15, fq = lane >> 4, srow0 = pm * 256;
  constexpr unsigned BRING = NB == 6 ? K1_B0 : K1_B1, PITCH = NB * 32u;
  const unsigned sw = (unsigned)((fr >> 1) & 7);
  const unsigned aoff0 = lds0 + (unsigned)(wi * 64 + fr) * 128u + (((unsigned)fq ^ sw) << 4), aoff1 = aoff0 ^ 64u;
  unsigned boff0, boff1;
  if (TYPE == 0) { boff0 = lds0 + BRING + (unsigned)fr * 128u + (((unsigned)fq ^ sw) << 4); boff1 = boff0 ^ 64u; }
  else {
    const unsigned q = (lane & 15) >> 2, p = lane & 3, g4 = lane >> 4;
    const unsigned base = lds0 + BRING + (q + 4u * (g4 & 1u) + 16u * (g4 >> 1)) * PITCH + p * 8u;
    if (NB == 6) { boff0 = base + (g4 & 1u) * 32u; boff1 = base - (g4 & 1u) * 32u; } else { boff0 = base; boff1 = base; }
  }
  f32x4 acc[4][NB];
  const unsigned scr = lds0 + KM_SCR + (unsigned)(g * 4 + wi) * 256u;
  const K1Step s0 = k1_prep(c, 0, 0u, 0), s1 = k1_prep(c, 1, 32768u, g);
  if (TYPE == 1) touch(mksrd(b1 + col0, NB * 32u), (unsigned)lane * 4u, 0u, scr);
  WAIT_V(TYPE);
  STL(2);
  BAR();
  if (g == 0 && wi == 0) {
    if (lane == 0) st_sc1(sy + SY_LN + pm * 32 + ct, tag);
    wait_words(sy + SY_LN + pm * 32, 32, tag, lane);
  }
  BAR();
  asm volatile("" ::: "memory");
  STL(3);
  k1_dma<0>(c, s0); k1_dma<1>(c, s0); k1_dma<2>(c, s0); k1_dma<3>(c, s0);
  k1_dma<0>(c, s1); k1_dma<1>(c, s1); k1_dma<2>(c, s1); k1_dma<3>(c, s1);
  unsigned cur = 0;
  K1Step st = k1_prep(c, 2, 65536u, 1 + g);
  unsigned pa0 = aoff0, pa1 = aoff1, pb0 = boff0, pb1 = boff1;
  WAIT_V(4);
  u32x2 bb[NB];
  if (TYPE == 1) {
#pragma unroll
    for (int n = 0; n < NB; ++n) bb[n] = *(const LAS u32x2*)(size_t)(scr + (unsigned)fq * 8u + n * 32);
  }
#pragma unroll
  for (int m = 0; m < 4; ++m)
#pragma unroll
    for (int n = 0; n < NB; ++n) acc[m][n] = TYPE == 1 ? (f32x4){bflo(bb[n][0]), bfhi(bb[n][0]), bflo(bb[n][1]), bfhi(bb[n][1])} : (f32x4){0.f, 0.f, 0.f, 0.f};
  BAR();
  if (g == 1) {
    if (TLBW && wi == 3) asm volatile("s_mov_b32 m0, %0\n\ts_nop 0\n\tglobal_load_lds_dword %1, off sc1" ::"s"(scr), "v"(tw) : "memory");
    BAR();
  }
  SCHED();
  ST(1); STC(0);
  auto iter = [&](auto modec, int t) {
    constexpr int MODE = decltype(modec)::value;
    bf16x8 af[4][2], bfr[NB][2];
    constexpr bool va = MODE == 0, vb = MODE == 0 || (MODE == 1 && g == 0);
    auto rdB = [&](int n) {
      if (TYPE == 0) { bfr[n][0] = ldsd128(pb0 + n * 2048); bfr[n][1] = ldsd128(pb1 + n * 2048); }
      else {
        const unsigned pb = (n & 1) ? pb1 : pb0;
#pragma unroll
        for (int ks = 0; ks < 2; ++ks) bfr[n][ks] = ldstr(pb + n * 32 + (32 * ks) * PITCH, pb + n * 32 + (32 * ks + 8) * PITCH);
      }
    };
    auto rdA = [&](int m) { af[m][0] = ldsd128(pa0 + m * 2048); af[m][1] = ldsd128(pa1 + m * 2048); };
    if (vb) k1_dma<4>(c, st);
    rdA(0); rdA(1);
    if (vb) k1_dma<5>(c, st);
    rdA(2); rdA(3);
    if (vb) k1_dma<6>(c, st);
    rdB(0);
    if (va) k1_dma<0>(c, st);
    rdB(1);
    if (va) k1_dma<1>(c, st);
    rdB(2);
    if (va) k1_dma<2>(c, st);
    rdB(3);
    if (va) k1_dma<3>(c, st);
    rdB(4);
    if (NB == 6) rdB(5);
    if (MODE == 0) WAIT_V(7);
    else { if (vb) WAIT_V(3); else WAIT_V(0); }
    WAIT_L(0); BAR(); SCHED();
    __builtin_amdgcn_s_setprio(1);
#pragma unroll
    for (int ks = 0; ks < 2; ++ks)
#pragma unroll
      for (int n = 0; n < NB; ++n)
#pragma unroll
        for (int m = 0; m < 4; ++m) {
          acc[m][n] = TYPE == 0 ? MFMA(af[m][ks], bfr[n][ks], acc[m][n]) : MFMA(bfr[n][ks], af[m][ks], acc[m][n]);
        }
    if (MODE != 2) {
      st = k1_prep(c, t + 3, cur, t + 2 + g);
      cur = cur == 65536u ? 0u : cur + 32768u;
      const unsigned bs = (t & 1) ? 0u : K1_BSLOT;
      pa0 = aoff0 + cur; pa1 = aoff1 + cur; pb0 = boff0 + bs; pb1 = boff1 + bs;
      mfma_shadow<8 * NB>();
      asm volatile("" : "+s"(st.sa[0]), "+s"(st.sa[1]), "+s"(st.sa[2]), "+s"(st.sa[3]), "+s"(st.kb), "+s"(st.ma), "+s"(st.mb), "+s"(cur), "+v"(pa0), "+v"(pa1), "+v"(pb0), "+v"(pb1));
    }
    __builtin_amdgcn_s_setprio(0);
    SCHED();
    if (!(g == 1 && MODE == 2)) BAR();
    SCHED();
  };
  for (int t = 0; t < K1_NT - 2; ++t) iter(IC<0>{}, t);
  iter(IC<1>{}, K1_NT - 2);
  iter(IC<2>{}, K1_NT - 1);
  ST(2); STC(1);
  if (TYPE == 0) {
    const __amdgpu_buffer_rsrc_t ro = mkrsrc(abT + (size_t)(srow0 / 8 + 8 * wi) * (KBS / 2) + col0 * 8, 0x7fffffffu);
    const unsigned vo = (unsigned)fq * KBS + (unsigned)fr * 16u;
#pragma unroll
    for (int j = 0; j < 2; ++j)
#pragma unroll
      for (int n = 0; n < NB; ++n) {
        u32x4 w; const f32x4 a = acc[2 * j][n], b = acc[2 * j + 1][n];
        w[0] = cvtpk(a[0], a[1]); w[1] = cvtpk(a[2], a[3]); w[2] = cvtpk(b[0], b[1]); w[3] = cvtpk(b[2], b[3]);
        __builtin_amdgcn_raw_buffer_store_b128(w, ro, vo + j * 4 * KBS + n * 256, 0, 16);
      }
  } else {
    const __amdgpu_buffer_rsrc_t ro = mkrsrc(h + (size_t)(col0 / 8) * (KBS / 2) + (srow0 + 64 * wi) * 8, 0x7fffffffu);
    const unsigned rowl = perm ? (unsigned)(8 * (fr >> 2) + 4 * (fq & 1) + (fr & 3)) : (unsigned)(16 * (fq & 1) + fr);
    const unsigned vo = (unsigned)(fq >> 1) * KBS + rowl * 16u;
    auto gelu2 = [&](f32x2 v) -> unsigned {
      constexpr float c1 = -2.0f * 0.7978845608028654f * 1.4426950408889634f, c3 = c1 * 0.044715f;
      const f32x2 u = v * __builtin_elementwise_fma(v * v, (f32x2){c3, c3}, (f32x2){c1, c1});
      f32x2 e; e[0] = __builtin_amdgcn_exp2f(u[0]); e[1] = __builtin_amdgcn_exp2f(u[1]);
      const f32x2 d = e + 1.0f;
      f32x2 r; r[0] = __builtin_amdgcn_rcpf(d[0]); r[1] = __builtin_amdgcn_rcpf(d[1]);
      return cvtpk2(v * r);
    };
#pragma unroll
    for (int n = 0; n < NB; ++n)
#pragma unroll
      for (int j = 0; j < 2; ++j) {
        const f32x4 a = acc[2 * j][n], b = acc[2 * j + 1][n];
        const unsigned a0 = gelu2((f32x2){a[0], a[1]}), a1 = gelu2((f32x2){a[2], a[3]}), b0 = gelu2((f32x2){b[0], b[1]}), b1_ = gelu2((f32x2){b[2], b[3]});
        auto r0 = __builtin_amdgcn_permlane16_swap(a0, b0, false, false);
        auto r1 = __builtin_amdgcn_permlane16_swap(a1, b1_, false, false);
        u32x4 w; w[0] = r0[0]; w[1] = r1[0]; w[2] = r0[1]; w[3] = r1[1];
        __builtin_amdgcn_raw_buffer_store_b128(w, ro, vo + n * 2 * KBS + j * 512, 0, 16);
      }
  }
  WAIT_V(0);
  if (lane == 0) {
    const unsigned old = __hip_atomic_fetch_add((LAS unsigned*)(size_t)(lds0 + KM_CNT + g * 4), 1u, __ATOMIC_RELAXED, __HIP_MEMORY_SCOPE_WORKGROUP);
    if (old == 3u) {
      const int hid = TYPE == 0 ? (g == 0 ? ct : 13 + ct) : (g == 0 ? ct - 13 : 19 + ct - 10);
      st_sc1(TYPE == 0 ? sy + SY_D + hid * 8 + pm : sy + SY_M + pm * 48 + hid, tag);
    }
  }
  ST(3);
}

template <bool TR>
DI void k2_loop(unsigned lds0, const K2Issue& c, int g, int wi, int lane, f32x4 (&acc)[4][4], bool early ST_ARG) {
  const int fr = lane & 15, fq = lane >> 4, wmi = wi >> 1, wn = wi & 1;
  constexpr int NT = TR ? F / 128 : S / 64;
  const unsigned sw = (unsigned)((fr >> 1) & 7);
  if (TR) {
    if (g == 0) { k2_sm(c, 0, K2_R0); k2_sm(c, 1, K2_R1); k2_touch(c); }
    else { k2_sm(c, 0, K2_R0); k2_big(c, 0, K2_R0, K2_Q0); k2_sm(c, 1, K2_R1); k2_big(c, 1, K2_R1, K2_Q1); k2_sm(c, 2, K2_R2); }
  } else {
    if (early) {
      if (g == 0) { k2_big(c, 0, K2_R0, K2_Q0); k2_sm(c, 0, K2_R0); k2_big(c, 1, K2_R1, K2_Q1); k2_sm(c, 1, K2_R1); k2_touch<LEADT>(c); }
      else { k2_sm(c, 0, K2_R0); k2_big(c, 0, K2_R0, K2_Q0); k2_sm(c, 1, K2_R1); k2_big(c, 1, K2_R1, K2_Q1); k2_sm(c, 2, K2_R2); k2_touch(c); }
    } else {
      if (g == 0) { k2_big(c, 0, K2_R0, K2_Q0); k2_big(c, 1, K2_R1, K2_Q1); }
      else k2_touch(c);
    }
  }
  const unsigned abase = lds0 + (TR ? (g == 0 ? 16384u : 0u) : (g == 0 ? 0u : 16384u));
  const bool aIsQ = TR && g == 1;
  const unsigned aoff0 = TR ? abase + (unsigned)fq * 2048u + (unsigned)(wmi * 64 + fr) * 16u : abase + (unsigned)(wmi * 64 + fr) * 128u + (((unsigned)fq ^ sw) << 4);
  const unsigned aoff1 = aoff0 ^ 64u;
  constexpr int AM = TR ? 256 : 2048;
  unsigned boff[4];
  if (TR) {
    const unsigned q = (lane & 15) >> 2, p = lane & 3, g4 = lane >> 4, s8 = q + 4u * (g4 & 1u);
#pragma unroll
    for (int n = 0; n < 4; ++n) boff[n] = lds0 + (s8 + 16u * (g4 >> 1)) * 256u + ((((unsigned)(wn * 4 + n)) ^ s8) << 5) + p * 8u;
  } else {
    boff[0] = lds0 + (unsigned)fq * 2048u + (unsigned)(wn * 64 + fr) * 16u; boff[1] = boff[2] = boff[3] = 0;
  }
  unsigned curR = K2_R0, curQ = K2_Q0;
  u32x4 rb, rs; unsigned mb, ms, pa0, pa1, pb[4];
  auto prep = [&](int t) {
    const unsigned prvR = curR == 0u ? 65536u : curR - 32768u, prvQ = curQ == K2_Q0 ? K2_Q2 : curQ - 16384u;
    const int tsm = t + 2 + g;
    rb = mksrd(c.bigbase + k2_phys(c, t + 2) * c.bigstep, t + 2 < NT ? 0x7fffffffu : 0u);
    rs = mksrd(c.Abase + k2_phys(c, tsm) * c.astep, tsm < NT ? 0x7fffffffu : 0u);
    mb = c.mBig + (c.bigIsQ ? prvQ : prvR); ms = c.mSm + (g == 0 ? prvR : curR);
    const unsigned sa = aIsQ ? curQ : curR, sb = TR ? curR : curQ;
    pa0 = aoff0 + sa; pa1 = aoff1 + sa;
#pragma unroll
    for (int n = 0; n < (TR ? 4 : 1); ++n) pb[n] = boff[n] + sb;
  };
  prep(0);
  if (TR) {
    if (g == 0) { WAIT_V(2 + NPT); BAR(); } else { WAIT_V(12); BAR(); WAIT_V(6); BAR(); }
  } else {
    if (g == 0) { if (early) WAIT_V(6 + LEADT - 2); else WAIT_V(4); BAR(); } else { WAIT_V(12 + NPT); BAR(); WAIT_V(6 + NPT); BAR(); }
  }
  SCHED();
  ST(5); STC(2);
  auto rdA = [&](int m, int ks) -> bf16x8 { return TR ? ldsd128(pa0 + m * AM + ks * 8192) : ldsd128((ks ? pa1 : pa0) + m * AM); };
  auto rdB = [&](int n, int ks) -> bf16x8 { return TR ? ldstr(pb[n] + ks * 8192, pb[n] + ks * 8192 + 2048) : ldsd128(pb[0] + n * 256 + ks * 8192); };
  for (int t = 0; t < NT; ++t) {
    bf16x8 af[4][2], bfr[4][2];
    dma_m0(rb, c.voffBig[0], c.sBig[0], mb);
    af[0][0] = rdA(0, 0); af[0][1] = rdA(0, 1); bfr[0][0] = rdB(0, 0);
    dma_off<1024>(rb, c.voffBig[1], c.sBig[1]);
    af[1][0] = rdA(1, 0); af[1][1] = rdA(1, 1); bfr[1][0] = rdB(1, 0);
    dma_off<2048>(rb, c.voffBig[0], c.sBig[2]);
    af[2][0] = rdA(2, 0); af[2][1] = rdA(2, 1); bfr[2][0] = rdB(2, 0);
    dma_off<3072>(rb, c.voffBig[1], c.sBig[3]);
    af[3][0] = rdA(3, 0); af[3][1] = rdA(3, 1);
    dma_m0(rs, c.voffA[0], c.sSm[0], ms);
    bfr[3][0] = rdB(3, 0); bfr[0][1] = rdB(0, 1); bfr[1][1] = rdB(1, 1);
    dma_off<1024>(rs, c.voffA[1], c.sSm[1]);
    bfr[2][1] = rdB(2, 1); bfr[3][1] = rdB(3, 1);
    WAIT_L(0); BAR(); SCHED();
    __builtin_amdgcn_s_setprio(1);
#pragma unroll
    for (int ks = 0; ks < 2; ++ks)
#pragma unroll
      for (int n = 0; n < 4; ++n)
#pragma unroll
        for (int m = 0; m < 4; ++m) acc[m][n] = MFMA(bfr[n][ks], af[m][ks], acc[m][n]);
    curR = curR == 65536u ? 0u : curR + 32768u; curQ = curQ == K2_Q2 ? K2_Q0 : curQ + 16384u;
    prep(t + 1);
    mfma_shadow<32>();
    if (TR) asm volatile("" : "+s"(rb), "+s"(rs), "+s"(mb), "+s"(ms), "+s"(curR), "+s"(curQ), "+v"(pa0), "+v"(pb[0]), "+v"(pb[1]), "+v"(pb[2]), "+v"(pb[3]));
    else asm volatile("" : "+s"(rb), "+s"(rs), "+s"(mb), "+s"(ms), "+s"(curR), "+s"(curQ), "+v"(pa0), "+v"(pa1), "+v"(pb[0]));
    __builtin_amdgcn_s_setprio(0);
    SCHED();
    WAIT_V(6);
    if (!(g == 1 && t == NT - 1)) BAR();
    SCHED();
  }
  WAIT_V(0);
}

__global__ __launch_bounds__(512, 2) void k_main(u16* xn, const u16* __restrict__ Dr, const u16* __restrict__ Di, const u16* __restrict__ w1,
                                                 const u16* __restrict__ b1, u16* abT, u16* h, const u16* __restrict__ Cr, const u16* __restrict__ Ci,
                                                 const u16* __restrict__ w2, const u16* __restrict__ x, const u16* __restrict__ b2, u16* __restrict__ out,
                                                 unsigned char* xbuf, unsigned long long* sync DBG_ARG) {
  ST_DECL; ST(0);
  extern __shared__ __attribute__((aligned(16))) unsigned char smem[];
  const unsigned lds0 = (unsigned)(size_t)(LAS unsigned char*)smem;
  asm volatile("" : "+s"(xn), "+s"(Dr), "+s"(Di), "+s"(w1), "+s"(b1), "+s"(x), "+s"(sync));
  const int bid = blockIdx.x, xcd = bid & 7, j = bid >> 3;
  const int pm = (xcd & 1) * 4 + (j & 3), ct = CTMAP(xcd, j);
  const int role = xcd >> 1, pn = j >> 2, tile = pm * 8 + pn;
  const int tid = threadIdx.x, wid = (int)rfl(tid >> 6), lane = tid & 63, g = wid >> 2, wi = wid & 3;
  gu64* sy = (gu64*)sync;
  const int lnrow = pm * 256 + ct * 8 + wid;
  const unsigned xl = lds0 + 65536u + (unsigned)wid * 2048u;
  { const u32x4 rx = mksrd(x + (size_t)lnrow * D, D * 2u); dma_m0(rx, (unsigned)lane * 16u, 0u, xl); dma_off<1024>(rx, (unsigned)lane * 16u, 0u); }
  u64 tag;
  if (tid < 2) *(LAS unsigned*)(size_t)(lds0 + KM_CNT + tid * 4) = 0u;
  const int type0 = ct < 13 ? 0 : 1, col00 = ct < 13 ? 96 * ct : 96 * (ct - 13);
  const int type1 = ct < 10 ? 0 : 1, col01 = ct < 10 ? 1248 + 80 * ct : 1824 + 80 * (ct - 10);
  const bool perm = type0 == 0;
  K1Issue c;
  c.ra = mksrd(xn + (size_t)pm * 256 * D, 0x7fffffffu);
  c.leadmask = 1 << (((j & 3) + (j >> 2)) & 3);
  const unsigned i = (unsigned)lane;
#pragma unroll
  for (int par = 0; par < 2; ++par) c.voffA[par] = (perm ? 8u * (i >> 5) + ((i >> 3) & 3u) : (i >> 3)) * (D * 2u) + (((i & 7u) ^ (i >> 4) ^ ((unsigned)par << 2)) << 4);
#pragma unroll
  for (int q = 0; q < 4; ++q) { const unsigned p = (unsigned)(g * 16 + wi * 4 + q); c.sA[q] = (perm ? 16u * (p & 1u) + 4u * ((p >> 1) & 1u) + 32u * (p >> 2) : 8u * p) * (D * 2u) - q * 1024u; }
  c.mA = lds0 + (unsigned)(g * 16 + wi * 4) * 1024u;
  const int otype = g == 0 ? type1 : type0, ocol = g == 0 ? col01 : col00;
  const unsigned cpr = g == 0 ? 10u : 12u, oring = g == 0 ? K1_B1 : K1_B0;
  c.bstep = otype == 0 ? 128u : 64u * F * 2u;
  c.mB = lds0 + oring + (unsigned)wi * 3072u;
#pragma unroll
  for (int q = 0; q < 3; ++q) {
    const unsigned p = (unsigned)(3 * wi + q), nr = p < cpr ? 0x7fffffffu : 0u;
    if (otype == 0) {
      int e0 = ocol + 8 * (int)p; if (e0 > 2040) e0 = 2040;
      c.rb[q] = mksrd((const unsigned char*)(e0 < 1024 ? Dr + (size_t)e0 * D : Di + (size_t)(e0 - 1024) * D) - q * 1024, nr);
      c.voffB[q] = (i >> 3) * (D * 2u) + (((i & 7u) ^ (i >> 4) ^ ((p & 1u) << 2)) << 4);
    } else {
      const unsigned ci = 64u * p + i, rho = ci / cpr, pos = ci - rho * cpr, sp = pos >> 1;
      const unsigned seg = cpr == 12u ? sp ^ ((rho >> 2) & 1u) : sp;
      const unsigned k = (rho & 3u) + 8u * ((rho >> 2) & 1u) + 4u * ((rho >> 3) & 1u) + (rho & 48u);
      c.rb[q] = mksrd((const unsigned char*)(w1 + ocol) - q * 1024, nr);
      c.voffB[q] = k * (F * 2u) + seg * 32u + (pos & 1u) * 16u;
    }
  }
  const bool tRole = role < 2;
  const K2Args a2 = {Cr, Ci, abT, h, w2};
  const unsigned char* tw = lane == 0 ? (const unsigned char*)(role ? Ci : Cr) + (size_t)pm * 256 * S * 2 : lane == 1 ? (const unsigned char*)w2 + (size_t)(role & 1) * (F / 2) * D * 2
                          : lane == 3 ? (const unsigned char*)out + (size_t)pm * 256 * D * 2 : (const unsigned char*)b2 + pn * 256 + (lane & 1) * 128;
  {
    const K1Step s0 = k1_prep(c, 0, 0u, 0), s1 = k1_prep(c, 1, 32768u, g);
    if (g == 1) { k1_dma<4>(c, s0); k1_dma<5>(c, s0); k1_dma<6>(c, s0); }
    k1_dma<4>(c, s1); k1_dma<5>(c, s1); k1_dma<6>(c, s1);
    if (g == 1) WAIT_V(6); else WAIT_V(3);
    const u32x4 v0 = *(const LAS u32x4*)(size_t)(xl + lane * 16u), v1 = *(const LAS u32x4*)(size_t)(xl + 1024u + lane * 16u);
    float f[16];
#pragma unroll
    for (int q = 0; q < 4; ++q) { f[2 * q] = bflo(v0[q]); f[2 * q + 1] = bfhi(v0[q]); f[8 + 2 * q] = bflo(v1[q]); f[9 + 2 * q] = bfhi(v1[q]); }
    float sm = 0.f;
#pragma unroll
    for (int q = 0; q < 16; ++q) sm += f[q];
    asm volatile("" : "+v"(sm)); STL(0);
    sm = wave_sum(sm);
    const float mean = sm * (1.0f / D);
    float sq = 0.f;
#pragma unroll
    for (int q = 0; q < 16; ++q) { f[q] -= mean; sq += f[q] * f[q]; }
    sq = wave_sum(sq);
    const float rs = 1.0f / sqrtf(sq * (1.0f / D) + 1e-5f);
    u32x4 o0, o1;
#pragma unroll
    for (int q = 0; q < 4; ++q) { o0[q] = cvtpk(f[2 * q] * rs, f[2 * q + 1] * rs); o1[q] = cvtpk(f[8 + 2 * q] * rs, f[9 + 2 * q] * rs); }
    const __amdgpu_buffer_rsrc_t ro = mkrsrc(xn + (size_t)lnrow * D, D * 2u);
    __builtin_amdgcn_raw_buffer_store_b128(o0, ro, lane * 16, 0, 16); __builtin_amdgcn_raw_buffer_store_b128(o1, ro, lane * 16 + 1024, 0, 16);
    asm volatile("s_load_dwordx2 %0, %1, 0x0\n\ts_waitcnt lgkmcnt(0)" : "=s"(tag) : "s"(sync + SY_EPOCH) : "memory");
    tag += 1ull;
    STL(1);
  }
  if (g == 0) {
    if (type0 == 0) k1_group<0, 6>(lds0, c, a2, tRole, perm, wi, lane, pm, col00, abT, h, b1, sy, tag, ct, tw ST_PASS);
    else k1_group<1, 6>(lds0, c, a2, tRole, perm, wi, lane, pm, col00, abT, h, b1, sy, tag, ct, tw ST_PASS);
  } else {
    if (type1 == 0) k1_group<0, 5>(lds0, c, a2, tRole, perm, wi, lane, pm, col01, abT, h, b1, sy, tag, ct, tw ST_PASS);
    else k1_group<1, 5>(lds0, c, a2, tRole, perm, wi, lane, pm, col01, abT, h, b1, sy, tag, ct, tw ST_PASS);
  }
  const K2Issue c2 = k2_make(a2, lds0);
  auto wait_deps = [&]() {
    if (tRole) {
      const int eb = role * 8 + pn, lo = eb * 128, hi = lo + 127;
      const int hlo = lo < 1248 ? lo / 96 : 13 + (lo - 1248) / 80, hhi = hi < 1248 ? hi / 96 : 13 + (hi - 1248) / 80;
      wait_words(sy + SY_D + hlo * 8, (hhi - hlo + 1) * 8, tag, lane);
    } else wait_words(sy + SY_M + pm * 48 + (role == 2 ? 0 : 18), role == 2 ? 19 : 23, tag, lane);
  };
  const bool early = EARLYT && tRole && type1 == 1;
  if (early && wid == 0) wait_deps();
  BAR();
  asm volatile("" ::: "memory");
  if (!early) {
    k2_pre(c2, g, tRole, 0); k2_pre(c2, g, tRole, 1);
    if (tRole) { if (g == 0) k2_touch<LEADT>(c2); else k2_sm(c2, 2, K2_R2); }
    else if (g == 1) k2_touch(c2);
    if (wid == 0) wait_deps();
    BAR();
    asm volatile("" ::: "memory");
  }
  ST(4);
  f32x4 acc[4][4];
#pragma unroll
  for (int m = 0; m < 4; ++m)
#pragma unroll
    for (int n = 0; n < 4; ++n) acc[m][n] = (f32x4){0.f, 0.f, 0.f, 0.f};
  if (tRole) k2_loop<false>(lds0, c2, g, wi, lane, acc, early ST_PASS);
  else k2_loop<true>(lds0, c2, g, wi, lane, acc, false ST_PASS);
  ST(6); STC(3);
  const __amdgpu_buffer_rsrc_t xr = mkrsrc(xbuf + (size_t)tile * (4 * 8 * 8 * 1024), 4u * 8u * 8u * 1024u);
  const __amdgpu_buffer_rsrc_t xw = mkrsrc(xbuf + (size_t)tile * (4 * 8 * 8 * 1024) + (role * 8 + wid) * 8192, 8192u);
  const int fr = lane & 15, fq = lane >> 4, wmi = wi >> 1, wn = wi & 1;
  const int trow = pm * 256 + g * 128 + wmi * 64 + role * 16 + fr, ecol = pn * 128 + wn * 64 + 4 * fq;
#pragma unroll
  for (int m = 0; m < 4; ++m)
    if (m != role) {
#pragma unroll
      for (int pr = 0; pr < 2; ++pr) {
        const f32x4 a = acc[m][2 * pr], b = acc[m][2 * pr + 1];
        u32x4 w; w[0] = cvtpk(a[0], a[1]); w[1] = cvtpk(a[2], a[3]); w[2] = cvtpk(b[0], b[1]); w[3] = cvtpk(b[2], b[3]);
        __builtin_amdgcn_raw_buffer_store_b128(w, xw, lane * 16 + (m * 2 + pr) * 1024, 0, 16);
      }
    }
  gu64* fl = sy + SY_X + (tile * 8 + wid) * 8;
  u64 f0 = ld_sc1(fl + (lane & 3));
  u32x2 xv[4], bv[4];
#pragma unroll
  for (int n = 0; n < 4; ++n) { xv[n] = *(const u32x2*)(x + (size_t)trow * D + ecol + 16 * n); bv[n] = *(const u32x2*)(b2 + ecol + 16 * n); }
  f32x4 own[4];
#pragma unroll
  for (int n = 0; n < 4; ++n) own[n] = role == 0 ? acc[0][n] : role == 1 ? acc[1][n] : role == 2 ? acc[2][n] : acc[3][n];
  WAIT_V(0);
  if (lane == 0) st_sc1(fl + role, tag);
  u32x4 pk[4][2];
#pragma unroll
  for (int r = 0; r < 4; ++r) { pk[r][0] = (u32x4){0u, 0u, 0u, 0u}; pk[r][1] = (u32x4){0u, 0u, 0u, 0u}; }
  unsigned have = 1u << role;
  for (unsigned spins = 0;;) {
    const unsigned rdy = (unsigned)__builtin_amdgcn_ballot_w64(f0 == tag) & 15u;
    const unsigned nw = rdy & ~have;
    asm volatile("" ::: "memory");
    have |= nw;
    if (have != 15u) f0 = ld_sc1(fl + (lane & 3));
#pragma unroll
    for (int r = 0; r < 4; ++r)
      if (nw & (1u << r)) {
#pragma unroll
        for (int pr = 0; pr < 2; ++pr) pk[r][pr] = __builtin_bit_cast(u32x4, __builtin_amdgcn_raw_buffer_load_b128(xr, lane * 16, ((r * 8 + wid) * 8 + role * 2 + pr) * 1024, 16));
      }
    if (have == 15u || ++spins > (1u << 20)) break;
    if (!nw) __builtin_amdgcn_s_sleep(XSLEEP);
  }
#pragma unroll
  for (int n = 0; n < 4; ++n) {
    const float xb[4] = {bflo(xv[n][0]), bfhi(xv[n][0]), bflo(xv[n][1]), bfhi(xv[n][1])};
    const float bb[4] = {bflo(bv[n][0]), bfhi(bv[n][0]), bflo(bv[n][1]), bfhi(bv[n][1])};
    float pv[4][4];
#pragma unroll
    for (int r = 0; r < 4; ++r) {
      const unsigned w0 = pk[r][n >> 1][(n & 1) * 2], w1_ = pk[r][n >> 1][(n & 1) * 2 + 1];
      pv[r][0] = r == role ? own[n][0] : bflo(w0); pv[r][1] = r == role ? own[n][1] : bfhi(w0);
      pv[r][2] = r == role ? own[n][2] : bflo(w1_); pv[r][3] = r == role ? own[n][3] : bfhi(w1_);
    }
    float o[4];
#pragma unroll
    for (int e = 0; e < 4; ++e) {
      const float y = pv[0][e] - pv[1][e], z = pv[2][e] + pv[3][e] + bb[e];
      const float gz = z * __builtin_amdgcn_rcpf(1.0f + __builtin_amdgcn_exp2f(-1.4426950408889634f * z));
      o[e] = xb[e] + y * gz;
    }
    u32x2 w; w[0] = cvtpk(o[0], o[1]); w[1] = cvtpk(o[2], o[3]);
    __builtin_amdgcn_raw_buffer_store_b64(w, mkrsrc(out, (unsigned)(S * D * 2)), (unsigned)(trow * D + ecol + 16 * n) * 2u, 0, OUTAUX);
  }
  if (bid == 0 && wid == 0) {
#pragma unroll
    for (int q = 0; q < 4; ++q) { gu64* pq = sy + SY_LN + q * 64 + lane; for (unsigned spins = 0; __builtin_amdgcn_ballot_w64(ld_sc1(pq) != tag) != 0ull && spins < (1u << 20); ++spins) __builtin_amdgcn_s_sleep(2); }
    if (lane == 0) st_sc1(sy + SY_EPOCH, tag);
  }
}

extern "C" void kernel_launch(void* const* d_in, const int* in_sizes, int n_in, void* d_out, int out_size, void* d_ws, size_t ws_size, hipStream_t stream) {
  const u16* x = (const u16*)d_in[0]; const u16* Cr = (const u16*)d_in[1]; const u16* Ci = (const u16*)d_in[2];
  const u16* Dr = (const u16*)d_in[3]; const u16* Di = (const u16*)d_in[4];
  const u16* w1 = (const u16*)d_in[6]; const u16* b1 = (const u16*)d_in[7]; const u16* w2 = (const u16*)d_in[8]; const u16* b2 = (const u16*)d_in[9];
  char* ws = (char*)d_ws;
  unsigned long long* sync = (unsigned long long*)ws;
  u16* xn = (u16*)(ws + (1 << 20));
  u16* abT = (u16*)(ws + (6 << 20));
  u16* h = (u16*)(ws + (18 << 20));
  unsigned char* xbuf = (unsigned char*)(ws + (40 << 20));
  (void)hipFuncSetAttribute((const void*)k_main, hipFuncAttributeMaxDynamicSharedMemorySize, KM_LDS);
  k_main<<<256, 512, KM_LDS, stream>>>(xn, Dr, Di, w1, b1, abT, h, Cr, Ci, w2, x, b2, (u16*)d_out, xbuf, sync DBG_PASS);
}
```

```cpp
#include <hip/hip_runtime.h>
#include <hip/hip_bf16.h>

typedef unsigned short u16;
typedef __bf16 bf16x8 __attribute__((ext_vector_type(8)));
typedef short s16x4 __attribute__((ext_vector_type(4)));
typedef short s16x8 __attribute__((ext_vector_type(8)));
typedef float f32x4 __attribute__((ext_vector_type(4)));
typedef float f32x2 __attribute__((ext_vector_type(2)));
typedef __bf16 bf16x2 __attribute__((ext_vector_type(2)));
typedef unsigned u32x4 __attribute__((ext_vector_type(4)));
typedef unsigned u32x2 __attribute__((ext_vector_type(2)));
#define LAS __attribute__((address_space(3)))
#define GAS __attribute__((address_space(1)))
#define DI __device__ __forceinline__

constexpr int S = 2048, D = 1024, F = 3584;

#define LEAD1 4
#define LEAD2 4
#define LEADT 8
#define LNKT 6
#define PREIN 0
#define XSLEEP 1
#define WARMC 0
#define SWARM 0
#define OUTAUX 0
#define XNAUX 0
#define CODEW 40960
#define EARLYT 1
#define PREBAR 1
#define DBAL 2

#define CTMAP(xcd, j) (DBAL == 2 ? ((xcd) >> 1) + 4 * ((j) >> 2) : (DBAL && (xcd) < 4) ? 2 * ((j) >> 2) + ((xcd) >> 1) : ((xcd) >> 1) * 8 + ((j) >> 2))
#define NOACQ 1
#define SC1S " sc1"
#define WAIT_V(nn) asm volatile("s_waitcnt vmcnt(%0)" ::"n"(nn) : "memory")
#define WAIT_L(n) do { asm volatile("" ::: "memory"); __builtin_amdgcn_s_waitcnt(0xC07F); asm volatile("" ::: "memory"); } while (0)
#define BAR() __builtin_amdgcn_s_barrier()
#define SCHED() __builtin_amdgcn_sched_barrier(0)

DI unsigned rfl(unsigned x) { return __builtin_amdgcn_readfirstlane(x); }
DI float bflo(unsigned v) { return __uint_as_float(v << 16); }
DI float bfhi(unsigned v) { return __uint_as_float(v & 0xffff0000u); }
DI float fadd1(float a, float b) { float r; asm("v_add_f32_e32 %0, %1, %2" : "=v"(r) : "v"(a), "v"(b)); return r; }
DI unsigned cvtpk(float lo, float hi) { f32x2 v = {lo, hi}; bf16x2 b = __builtin_convertvector(v, bf16x2); return __builtin_bit_cast(unsigned, b); }
DI unsigned cvtpk2(f32x2 v) { bf16x2 b = __builtin_convertvector(v, bf16x2); return __builtin_bit_cast(unsigned, b); }
DI __amdgpu_buffer_rsrc_t mkrsrc(const void* p, unsigned n) { return __builtin_amdgcn_make_buffer_rsrc((void*)p, (short)0, (int)n, 0x00020000); }
DI u32x4 mksrd(const void* p, unsigned n) { const unsigned long long a = (unsigned long long)p; u32x4 r; r[0] = (unsigned)a; r[1] = (unsigned)(a >> 32) & 0xffffu; r[2] = n; r[3] = 0x00020000u; return r; }
DI void dma_m0(const u32x4& r, unsigned voff, unsigned soff, unsigned m0v) {
  asm volatile("s_mov_b32 m0, %0\n\ts_nop 0\n\tbuffer_load_dwordx4 %1, %2, %3 offen" SC1S " lds" ::"s"(m0v), "v"(voff), "s"(r), "s"(soff) : "memory");
}
template <int OFF> DI void dma_off(const u32x4& r, unsigned voff, unsigned soff) {
  asm volatile("buffer_load_dwordx4 %0, %1, %2 offen offset:%3" SC1S " lds" ::"v"(voff), "s"(r), "s"(soff), "n"(OFF) : "memory");
}
DI void touch(const u32x4& r, unsigned voff, unsigned soff, unsigned m0v) {
  asm volatile("s_mov_b32 m0, %0\n\ts_nop 0\n\tbuffer_load_dword %1, %2, %3 offen" SC1S " lds" ::"s"(m0v), "v"(voff), "s"(r), "s"(soff) : "memory");
}
DI bf16x8 ldsd128(unsigned a) { return *(const LAS bf16x8*)(size_t)a; }
DI bf16x8 ldstr(unsigned a0, unsigned a1) {
  s16x4 a = __builtin_amdgcn_ds_read_tr16_b64_v4i16((LAS s16x4*)(size_t)a0);
  s16x4 b = __builtin_amdgcn_ds_read_tr16_b64_v4i16((LAS s16x4*)(size_t)a1);
  s16x8 c = __builtin_shufflevector(a, b, 0, 1, 2, 3, 4, 5, 6, 7);
  return __builtin_bit_cast(bf16x8, c);
}
DI float wave_sum(float v) {
#define DPPADD(ctrl, rm) v += __int_as_float(__builtin_amdgcn_update_dpp(0, __float_as_int(v), ctrl, rm, 0xf, false))
  DPPADD(0x111, 0xf); DPPADD(0x112, 0xf); DPPADD(0x114, 0xf); DPPADD(0x118, 0xf); DPPADD(0x142, 0xa); DPPADD(0x143, 0xc);
#undef DPPADD
  return __int_as_float(__builtin_amdgcn_readlane(__float_as_int(v), 63));
}
#define MFMA(a, b, c) __builtin_amdgcn_mfma_f32_16x16x32_bf16(a, b, c, 0, 0, 0)
template <int V> struct IC { static constexpr int value = V; };
template <int N> DI void mfma_shadow() {
#pragma unroll
  for (int i = 0; i < N; ++i) { __builtin_amdgcn_sched_group_barrier(0x8, 1, 0); __builtin_amdgcn_sched_group_barrier(0x6, 2, 0); }
}
template <int N> DI void mfma_shadow3() {
#pragma unroll
  for (int i = 0; i < N / 2; ++i) {
    __builtin_amdgcn_sched_group_barrier(0x8, 1, 0); __builtin_amdgcn_sched_group_barrier(0x2, 2, 0);
    __builtin_amdgcn_sched_group_barrier(0x8, 1, 0); __builtin_amdgcn_sched_group_barrier(0x2, 1, 0); __builtin_amdgcn_sched_group_barrier(0x4, 2, 0);
  }
}

#define ST_DECL
#define ST_ARG
#define ST_PASS
#define DBG_ARG
#define DBG_PASS
#define ST(i)
#define STC(i)
#define STL(i)
#define STX(i)
#define ST_OUT(dbg, kid, w)

constexpr unsigned K1_B0 = 98304, K1_B1 = 122880, K1_BSLOT = 12288, KM_SCR = 147456  , KM_CNT = KM_SCR + 2048  ,
                   KM_ST = KM_CNT + 64  , KM_CS = KM_ST + 8192  , KM_LDS = KM_CS + 1536;
constexpr unsigned K2_R0 = 32768, K2_R1 = 65536, K2_R2 = 0, K2_Q0 = 98304, K2_Q1 = 114688, K2_Q2 = 131072;
#define KBSV 32768
constexpr unsigned KBS = KBSV;
constexpr int K1_NT = D / 64;
constexpr int NPT = LEAD2 > 2 ? LEAD2 - 2 : 0;
typedef unsigned long long u64;
typedef GAS u64 gu64;
constexpr int SY_EPOCH = 0, SY_LN = 16  , SY_D = SY_LN + 256  , SY_M = SY_D + 192  , SY_X = SY_M + 384  , SY_ST = 8192  ;
DI u64 ld_sc1(gu64* p) { return __hip_atomic_load(p, __ATOMIC_RELAXED, __HIP_MEMORY_SCOPE_AGENT); }
DI void st_sc1(gu64* p, u64 v) { __hip_atomic_store(p, v, __ATOMIC_RELAXED, __HIP_MEMORY_SCOPE_AGENT); }
DI void wait_words(gu64* p, int n, u64 tag, int lane) {
  gu64* q = p + (lane < n ? lane : 0);
#pragma unroll 1
  for (unsigned spins = 0; __builtin_amdgcn_ballot_w64(ld_sc1(q) != tag) != 0ull && spins < (1u << 20); ++spins) __builtin_amdgcn_s_sleep(2);
  if (!NOACQ) { __builtin_amdgcn_fence(__ATOMIC_ACQUIRE, "agent"); WAIT_V(0); }
  asm volatile("" ::: "memory");
}

struct K1Issue {
  u32x4 ra, rb[3];
  unsigned voffA[2], voffB[3];
  unsigned sA[4];
  unsigned mA, mB, bstep;
  int leadmask;
};
struct K1Step { unsigned sa[4], kb, ma, mb; };
DI unsigned k1_phys(const K1Issue& c, int t) { return (unsigned)(t + (((c.leadmask >> (t & 3)) & 1) * LEAD1)) & (unsigned)(K1_NT - 1); }
DI K1Step k1_prep(const K1Issue& c, int tA, unsigned slotA, int tB) {
  K1Step s; const unsigned ka = k1_phys(c, tA) * 128u;
#pragma unroll
  for (int q = 0; q < 4; ++q) s.sa[q] = c.sA[q] + ka;
  s.kb = k1_phys(c, tB) * c.bstep; s.ma = c.mA + slotA; s.mb = c.mB + ((tB & 1) ? K1_BSLOT : 0u);
  return s;
}
template <int Q> DI void k1_dma(const K1Issue& c, const K1Step& s) {
  if (Q == 0) dma_m0(c.ra, c.voffA[0], s.sa[0], s.ma);
  else if (Q < 4) dma_off<(Q & 3) * 1024>(c.ra, c.voffA[Q & 1], s.sa[Q & 3]);
  else if (Q == 4) dma_m0(c.rb[0], c.voffB[0], s.kb, s.mb);
  else if (Q == 5) dma_off<1024>(c.rb[1], c.voffB[1], s.kb);
  else dma_off<2048>(c.rb[2], c.voffB[2], s.kb);
}

struct K2Issue {
  const unsigned char* Abase; const unsigned char* bigbase; const unsigned char* tchbase;
  unsigned voffA[2], voffBig[2], sBig[4], sSm[2], bigstep, astep, mBig, mSm, mScr, tchvoff, tchstep;
  int NT, leadmask, lead; bool bigIsQ;
};
DI unsigned k2_phys(const K2Issue& c, int t) { int p = t + (((c.leadmask >> (t & 7)) & 1) * c.lead); p = p >= c.NT ? p - c.NT : p; return (unsigned)p; }
DI K2Issue k2_setup(unsigned lds0, bool TR, const unsigned char* Abase, const unsigned char* Bbase, int NT, int g, int wi, int lane, int pm4, int pn8) {
  K2Issue c; const unsigned i = (unsigned)lane;
  c.Abase = Abase; c.NT = NT;
  if (!TR && LEADT == 8) { c.leadmask = 1 << ((2 * pm4 + pn8) & 7); c.lead = 8; }
  else { c.leadmask = 0x11 << ((pm4 + pn8) & 3); c.lead = 4; }
  c.astep = TR ? 8u * KBS : 128u;
#pragma unroll
  for (int par = 0; par < 2; ++par) c.voffA[par] = TR ? i * 16u : (i >> 3) * (S * 2u) + (((i & 7u) ^ (i >> 4) ^ ((unsigned)par << 2)) << 4);
  auto sApiece = [&](unsigned p, unsigned half) -> unsigned { return TR ? (p >> 1) * KBS + (p & 1u) * 1024u + half * 2048u : (half * 128u + p * 8u) * (S * 2u); };
  const unsigned idx = (unsigned)(wi * 8 + (lane & 7));
  if (g == 0) {
    c.bigbase = Bbase;
    if (TR) {
      c.bigstep = 64u * D * 2u;
#pragma unroll
      for (int par = 0; par < 2; ++par) c.voffBig[par] = (i >> 4) * (D * 2u) + ((((i & 15u) >> 1) ^ ((i >> 4) + 4u * par)) << 5) + (i & 1u) * 16u;
#pragma unroll
      for (int q = 0; q < 4; ++q) { const unsigned p = (unsigned)(4 * wi + q); c.sBig[q] = (8u * (p & 1u) + 4u * ((p >> 1) & 1u) + 16u * (p >> 2)) * (D * 2u); }
    } else {
      c.bigstep = 8u * KBS;
#pragma unroll
      for (int par = 0; par < 2; ++par) c.voffBig[par] = i * 16u;
#pragma unroll
      for (int q = 0; q < 4; ++q) { const unsigned p = (unsigned)(4 * wi + q); c.sBig[q] = (p >> 1) * KBS + (p & 1u) * 1024u; }
    }
    c.tchbase = Abase; c.tchstep = c.astep;
    c.tchvoff = TR ? (unsigned)pn8 * KBS + idx * 128u : ((unsigned)pn8 * 32u + idx) * (S * 2u);
  } else {
    c.bigbase = Abase; c.bigstep = c.astep;
#pragma unroll
    for (int par = 0; par < 2; ++par) c.voffBig[par] = c.voffA[par];
#pragma unroll
    for (int q = 0; q < 4; ++q) c.sBig[q] = sApiece((unsigned)(4 * wi + q), 1u);
    c.tchbase = Bbase;
    if (!TR) { c.tchvoff = (2u * pm4 + (idx >> 4)) * KBS + (idx & 15u) * 128u; c.tchstep = 8u * KBS; }
    else { c.tchvoff = (unsigned)(pm4 * 16 + wi * 4 + ((lane & 7) >> 1)) * (D * 2u) + (lane & 1) * 128u; c.tchstep = 64u * D * 2u; }
  }
  const unsigned psm = (unsigned)((g == 0 ? 8 : 0) + 2 * wi);
#pragma unroll
  for (int q = 0; q < 2; ++q) c.sSm[q] = sApiece(psm + q, 0u);
#pragma unroll
  for (int q = 0; q < 4; ++q) c.sBig[q] -= q * 1024u;
  c.sSm[1] -= 1024u;
  c.bigIsQ = (g == 0) != TR; c.mScr = lds0 + KM_SCR + (unsigned)(g * 4 + wi) * 256u;
  c.mBig = lds0 + ((g == 1 && !TR) ? 16384u : 0u) + (unsigned)wi * 4096u; c.mSm = lds0 + (TR ? 16384u : 0u) + psm * 1024u;
  return c;
}
DI void k2_big(const K2Issue& c, int tile, unsigned sR, unsigned sQ) {
  const u32x4 r = mksrd(c.bigbase + k2_phys(c, tile) * c.bigstep, 0x7fffffffu);
  dma_m0(r, c.voffBig[0], c.sBig[0], c.mBig + (c.bigIsQ ? sQ : sR));
  dma_off<1024>(r, c.voffBig[1], c.sBig[1]); dma_off<2048>(r, c.voffBig[0], c.sBig[2]); dma_off<3072>(r, c.voffBig[1], c.sBig[3]);
}
DI void k2_sm(const K2Issue& c, int tile, unsigned sR) {
  const u32x4 r = mksrd(c.Abase + k2_phys(c, tile) * c.astep, 0x7fffffffu);
  dma_m0(r, c.voffA[0], c.sSm[0], c.mSm + sR);
  dma_off<1024>(r, c.voffA[1], c.sSm[1]);
}
template <int TEND = LEAD2> DI void k2_touch(const K2Issue& c) {
#pragma unroll
  for (int t = 2; t < TEND; ++t) touch(mksrd(c.tchbase, 0x7fffffffu), c.tchvoff, (unsigned)t * c.tchstep, c.mScr);
}
DI void k2_pre(const K2Issue& c, int g, bool tRole, int tile) {
  const unsigned sR = tile ? K2_R1 : K2_R0, sQ = tile ? K2_Q1 : K2_Q0;
  if (tRole) { k2_sm(c, tile, sR); if (g == 1) k2_big(c, tile, sR, sQ); }
  else if (g == 0) k2_big(c, tile, sR, sQ);
}
struct K2Args { const u16* Cr; const u16* Ci; const u16* abT; const u16* h; const u16* w2; };
DI K2Issue k2_make(const K2Args& a, unsigned lds0) {
  int bid = blockIdx.x, tid = threadIdx.x; asm volatile("" : "+s"(bid), "+v"(tid));
  const int xcd = bid & 7, j = bid >> 3, pm = (xcd & 1) * 4 + (j & 3), role = xcd >> 1, pn = j >> 2;
  const int wid = (int)rfl(tid >> 6), lane = tid & 63, g = wid >> 2, wi = wid & 3;
  const bool tRole = role < 2; const int k0 = (role & 1) * (F / 2);
  const u16* A2 = tRole ? (role ? a.Ci : a.Cr) + (size_t)pm * 256 * S : a.h + (size_t)(k0 / 8) * (KBS / 2) + pm * 256 * 8;
  const u16* B2 = tRole ? a.abT + (size_t)(role * 1024 + pn * 128) * 8 : a.w2 + (size_t)k0 * D + pn * 128;
  K2Issue c = k2_setup(lds0, !tRole, (const unsigned char*)A2, (const unsigned char*)B2, tRole ? S / 64 : F / 128, g, wi, lane, j & 3, pn);
#pragma unroll
  for (int q = 0; q < 4; ++q) c.sBig[q] = rfl(c.sBig[q]);
#pragma unroll
  for (int q = 0; q < 2; ++q) c.sSm[q] = rfl(c.sSm[q]);
  c.bigstep = rfl(c.bigstep); c.astep = rfl(c.astep); c.mBig = rfl(c.mBig); c.mSm = rfl(c.mSm); c.mScr = rfl(c.mScr); c.tchstep = rfl(c.tchstep);
  asm volatile("s_nop 4" : "+s"(c.sBig[0]), "+s"(c.sBig[1]), "+s"(c.sBig[2]), "+s"(c.sBig[3]), "+s"(c.sSm[0]), "+s"(c.sSm[1]), "+s"(c.tchstep) :: "memory");
  return c;
}

#define CSM(acc, frag) "v_mfma_f32_16x16x32_bf16 %" #acc ", %" #frag ", %2, %" #acc "\n\t"
template <int NB> DI void cs_mma_bar(f32x4 (&csa)[2], const bf16x8& ones, int wi, const bf16x8 (&bfr)[NB][2]) {
  asm volatile("s_cmp_lt_u32 %3, 2\n\ts_cbranch_scc0 .Lcb2_%=\n\ts_cmp_eq_u32 %3, 0\n\ts_cbranch_scc0 .Lcb1_%=\n\t"
               "s_barrier\n\t" CSM(0, 4) CSM(1, 6) CSM(0, 5) "s_branch .Lcb4_%=\n.Lcb1_%=:\n\t"
               "s_barrier\n\t" CSM(1, 8) CSM(0, 7) CSM(1, 9) "s_branch .Lcb4_%=\n.Lcb2_%=:\n\ts_cmp_eq_u32 %3, 2\n\ts_cbranch_scc0 .Lcb3_%=\n\t"
               "s_barrier\n\t" CSM(0, 10) CSM(1, 12) CSM(0, 11) "s_branch .Lcb4_%=\n.Lcb3_%=:\n\t"
               "s_barrier\n\t" CSM(1, 14) CSM(0, 13) CSM(1, 15) ".Lcb4_%=:"
               : "+v"(csa[0]), "+v"(csa[1])
               : "v"(ones), "s"(wi), "v"(bfr[0][0]), "v"(bfr[0][1]), "v"(bfr[1][0]), "v"(bfr[1][1]), "v"(bfr[2][0]), "v"(bfr[2][1]), "v"(bfr[3][0]), "v"(bfr[3][1]),
                 "v"(bfr[4][0]), "v"(bfr[4][1]), "v"(bfr[NB - 1][0]), "v"(bfr[NB - 1][1])
               : "scc", "memory");
}
template <int NB> DI void cs_mma(f32x4 (&csa)[2], const bf16x8& ones, int wi, const bf16x8 (&bfr)[NB][2]) {
  asm volatile("s_cmp_lt_u32 %3, 2\n\ts_cbranch_scc0 .Lcs2_%=\n\ts_cmp_eq_u32 %3, 0\n\ts_cbranch_scc0 .Lcs1_%=\n\t"
               CSM(0, 4) CSM(1, 6) CSM(0, 5) "s_branch .Lcs4_%=\n.Lcs1_%=:\n\t"
               CSM(1, 8) CSM(0, 7) CSM(1, 9) "s_branch .Lcs4_%=\n.Lcs2_%=:\n\ts_cmp_eq_u32 %3, 2\n\ts_cbranch_scc0 .Lcs3_%=\n\t"
               CSM(0, 10) CSM(1, 12) CSM(0, 11) "s_branch .Lcs4_%=\n.Lcs3_%=:\n\t"
               CSM(1, 14) CSM(0, 13) CSM(1, 15) ".Lcs4_%=:"
               : "+v"(csa[0]), "+v"(csa[1])
               : "v"(ones), "s"(wi), "v"(bfr[0][0]), "v"(bfr[0][1]), "v"(bfr[1][0]), "v"(bfr[1][1]), "v"(bfr[2][0]), "v"(bfr[2][1]), "v"(bfr[3][0]), "v"(bfr[3][1]),
                 "v"(bfr[4][0]), "v"(bfr[4][1]), "v"(bfr[NB - 1][0]), "v"(bfr[NB - 1][1])
               : "scc");
}
#undef CSM

template <int TYPE  , int NB>
DI void k1_group(unsigned lds0, const K1Issue& c, const K2Args& a2, bool tRole, bool perm, int wi, int lane, int pm, int col0, u16* abT, u16* h, const u16* __restrict__ b1,
                 gu64* sy, u64 tag, int ct, u64 stv ST_ARG) {
  constexpr int g = NB == 6 ? 0 : 1;
  const int fr = lane & 15, fq = lane >> 4, srow0 = pm * 256;
  constexpr unsigned BRING = NB == 6 ? K1_B0 : K1_B1, PITCH = NB * 32u;
  const unsigned sw = (unsigned)((fr >> 1) & 7);
  const unsigned aoff0 = lds0 + (unsigned)(wi * 64 + fr) * 128u + (((unsigned)fq ^ sw) << 4), aoff1 = aoff0 ^ 64u;
  unsigned boff0, boff1;
  if (TYPE == 0) { boff0 = lds0 + BRING + (unsigned)fr * 128u + (((unsigned)fq ^ sw) << 4); boff1 = boff0 ^ 64u; }
  else {
    const unsigned q = (lane & 15) >> 2, p = lane & 3, g4 = lane >> 4;
    const unsigned base = lds0 + BRING + (q + 4u * (g4 & 1u) + 16u * (g4 >> 1)) * PITCH + p * 8u;
    if (NB == 6) { boff0 = base + (g4 & 1u) * 32u; boff1 = base - (g4 & 1u) * 32u; } else { boff0 = base; boff1 = base; }
  }
  f32x4 acc[4][NB];
  const unsigned scr = lds0 + KM_SCR + (unsigned)(g * 4 + wi) * 256u;
  f32x4 csa[2] = {(f32x4){0.f, 0.f, 0.f, 0.f}, (f32x4){0.f, 0.f, 0.f, 0.f}};
  bf16x8 ones = __builtin_bit_cast(bf16x8, (u32x4){0x3f803f80u, 0x3f803f80u, 0x3f803f80u, 0x3f803f80u});
  asm volatile("" : "+v"(ones), "+v"(csa[0]), "+v"(csa[1]));
  {
    const K1Step s1 = k1_prep(c, 1, 32768u, g);
    k1_dma<4>(c, s1); k1_dma<5>(c, s1); k1_dma<6>(c, s1);
    k1_dma<0>(c, s1); k1_dma<1>(c, s1); k1_dma<2>(c, s1); k1_dma<3>(c, s1);
  }
  if (lane < 2) st_sc1(sy + SY_ST + (srow0 + ct * 8 + g * 4 + wi) * 2 + lane, stv);
  asm volatile("" ::: "memory");
  if (TYPE == 1) touch(mksrd(b1 + col0, NB * 32u), (unsigned)lane * 4u, 0u, scr);
  unsigned cur = 0;
  K1Step st = k1_prep(c, 2, 65536u, 1 + g);
  unsigned pa0 = aoff0, pa1 = aoff1, pb0 = boff0, pb1 = boff1;
#pragma unroll
  for (int m = 0; m < 4; ++m)
#pragma unroll
    for (int n = 0; n < NB; ++n) { acc[m][n] = (f32x4){0.f, 0.f, 0.f, 0.f}; asm volatile("" : "+v"(acc[m][n])); }
  WAIT_V(8 + TYPE);
  BAR();
  if (g == 1) {
    if (wi == 0 && lane == 0) st_sc1(sy + SY_LN + pm * 32 + ct, tag);
    BAR();
  }
  const u32x4 rst = mksrd((const void*)(size_t)(sy + SY_ST + (srow0 + 64 * wi) * 2), 1024u);
  const unsigned stl = lds0 + KM_ST + (unsigned)(g * 4 + wi) * 1024u, csl = lds0 + KM_CS + g * 768u;
  SCHED();
  ST(1); STC(0);
  auto iter = [&](auto modec, int t) {
    constexpr int MODE = decltype(modec)::value;
    bf16x8 af[4][2], bfr[NB][2];
    constexpr bool va = MODE == 0, vb = MODE == 0 || (MODE == 1 && g == 0);
    auto rdB = [&](int n) {
      if (TYPE == 0) { bfr[n][0] = ldsd128(pb0 + n * 2048); bfr[n][1] = ldsd128(pb1 + n * 2048); }
      else {
        const unsigned pb = (n & 1) ? pb1 : pb0;
#pragma unroll
        for (int ks = 0; ks < 2; ++ks) bfr[n][ks] = ldstr(pb + n * 32 + (32 * ks) * PITCH, pb + n * 32 + (32 * ks + 8) * PITCH);
      }
    };
    auto rdA = [&](int m) { af[m][0] = ldsd128(pa0 + m * 2048); af[m][1] = ldsd128(pa1 + m * 2048); };
    if (vb) k1_dma<4>(c, st);
    rdA(0); rdA(1);
    if (vb) k1_dma<5>(c, st);
    rdA(2); rdA(3);
    if (vb) k1_dma<6>(c, st);
    rdB(0);
    if (va) k1_dma<0>(c, st);
    rdB(1);
    if (va) k1_dma<1>(c, st);
    rdB(2);
    if (va) k1_dma<2>(c, st);
    rdB(3);
    if (va) k1_dma<3>(c, st);
    rdB(4);
    if (NB == 6) rdB(5);
    auto cs_all = [&]() { cs_mma<NB>(csa, ones, wi, bfr); };
    if (MODE == 0) WAIT_V(7);
    else if (MODE == 1) { dma_m0(rst, (unsigned)lane * 16u, 0u, stl); if (vb) WAIT_V(4); else WAIT_V(1); }
    else {
      WAIT_V(0);
      WAIT_L(0);
      cs_all();
      asm volatile("s_nop 7\n\ts_nop 3" : "+v"(csa[0]), "+v"(csa[1]));
      if (fr == 0) { *(LAS f32x4*)(size_t)(csl + (unsigned)(2 * wi) * 64u + (unsigned)fq * 16u) = csa[0]; *(LAS f32x4*)(size_t)(csl + (unsigned)(2 * wi + 1) * 64u + (unsigned)fq * 16u) = csa[1]; }
    }
    WAIT_L(0);
    __builtin_amdgcn_s_setprio(1);
    SCHED();
    if (MODE != 2) cs_mma_bar<NB>(csa, ones, wi, bfr); else BAR();
    SCHED();
#pragma unroll
    for (int ks = 0; ks < 2; ++ks)
#pragma unroll
      for (int n = 0; n < NB; ++n)
#pragma unroll
        for (int m = 0; m < 4; ++m) {
          acc[m][n] = TYPE == 0 ? MFMA(af[m][ks], bfr[n][ks], acc[m][n]) : MFMA(bfr[n][ks], af[m][ks], acc[m][n]);
        }
    if (MODE != 2) {
      st = k1_prep(c, t + 3, cur, t + 2 + g);
      cur = cur == 65536u ? 0u : cur + 32768u;
      const unsigned bs = (t & 1) ? 0u : K1_BSLOT;
      pa0 = aoff0 + cur; pa1 = aoff1 + cur; pb0 = boff0 + bs; pb1 = boff1 + bs;
      mfma_shadow<8 * NB>();
      asm volatile("" : "+s"(st.sa[0]), "+s"(st.sa[1]), "+s"(st.sa[2]), "+s"(st.sa[3]), "+s"(st.kb), "+s"(st.ma), "+s"(st.mb), "+s"(cur), "+v"(pa0), "+v"(pa1), "+v"(pb0), "+v"(pb1));
    }
    __builtin_amdgcn_s_setprio(0);
    SCHED();
    if (!(g == 1 && MODE == 2)) BAR();
    SCHED();
  };
  for (int t = 0; t < K1_NT - 2; ++t) iter(IC<0>{}, t);
  iter(IC<1>{}, K1_NT - 2);
  iter(IC<2>{}, K1_NT - 1);
  ST(2); STC(1);
#pragma unroll 1
  for (unsigned spins = 0; spins < (1u << 20); ++spins) {
    const u32x4 gq = *(const LAS u32x4*)(size_t)(stl + (unsigned)lane * 16u);
    if (__builtin_amdgcn_ballot_w64(gq[1] != (unsigned)tag || gq[3] != (unsigned)tag) == 0ull) break;
    __builtin_amdgcn_s_sleep(2);
    WAIT_L(0);
    dma_m0(rst, (unsigned)lane * 16u, 0u, stl); WAIT_V(0);
  }
  if (TYPE == 0) {
    const __amdgpu_buffer_rsrc_t ro = mkrsrc(abT + (size_t)(srow0 / 8 + 8 * wi) * (KBS / 2) + col0 * 8, 0x7fffffffu);
    const unsigned vo = (unsigned)fq * KBS + (unsigned)fr * 16u;
    float cs[NB];
#pragma unroll
    for (int n = 0; n < NB; ++n) { constexpr int sl[6] = {0, 1, 3, 4, 5, 7}; cs[n] = *(const LAS float*)(size_t)(csl + sl[n] * 64 + fr * 4); if (n == 1 || n == 4) cs[n] += *(const LAS float*)(size_t)(csl + (sl[n] + 1) * 64 + fr * 4); }
#pragma unroll
    for (int j = 0; j < 2; ++j) {
      float mu[8], rs[8];
#pragma unroll
      for (int r = 0; r < 8; ++r) { const u32x4 gq = *(const LAS u32x4*)(size_t)(stl + (unsigned)(32 * j + 8 * fq + r) * 16u); mu[r] = __uint_as_float(gq[0]); rs[r] = __uint_as_float(gq[2]); }
#pragma unroll
      for (int n = 0; n < NB; ++n) {
        float v[8];
#pragma unroll
        for (int r = 0; r < 8; ++r) v[r] = __builtin_fmaf(-mu[r], cs[n], acc[2 * j + (r >> 2)][n][r & 3]) * rs[r];
        u32x4 w; w[0] = cvtpk(v[0], v[1]); w[1] = cvtpk(v[2], v[3]); w[2] = cvtpk(v[4], v[5]); w[3] = cvtpk(v[6], v[7]);
        __builtin_amdgcn_raw_buffer_store_b128(w, ro, vo + j * 4 * KBS + n * 256, 0, 16);
      }
    }
  } else {
    const __amdgpu_buffer_rsrc_t ro = mkrsrc(h + (size_t)(col0 / 8) * (KBS / 2) + (srow0 + 64 * wi) * 8, 0x7fffffffu);
    const unsigned rowl = perm ? (unsigned)(8 * (fr >> 2) + 4 * (fq & 1) + (fr & 3)) : (unsigned)(16 * (fq & 1) + fr);
    const unsigned vo = (unsigned)(fq >> 1) * KBS + rowl * 16u;
    float rs[4], rm[4];
#pragma unroll
    for (int m = 0; m < 4; ++m) {
      const unsigned row = perm ? (unsigned)(32 * (m >> 1) + 8 * (fr >> 2) + 4 * (m & 1) + (fr & 3)) : (unsigned)(16 * m + fr);
      const u32x4 gq = *(const LAS u32x4*)(size_t)(stl + row * 16u); rs[m] = __uint_as_float(gq[2]); rm[m] = -__uint_as_float(gq[0]) * rs[m];
    }
    auto gelu2 = [&](f32x2 a, f32x2 cs, f32x2 bv, float r_, float m_) -> unsigned {
      constexpr float c1 = -2.0f * 0.7978845608028654f * 1.4426950408889634f, c3 = c1 * 0.044715f;
      const f32x2 v = __builtin_elementwise_fma(a, (f32x2){r_, r_}, __builtin_elementwise_fma(cs, (f32x2){m_, m_}, bv));
      const f32x2 u = v * __builtin_elementwise_fma(v * v, (f32x2){c3, c3}, (f32x2){c1, c1});
      f32x2 e; e[0] = __builtin_amdgcn_exp2f(u[0]); e[1] = __builtin_amdgcn_exp2f(u[1]);
      const f32x2 d = e + 1.0f;
      f32x2 r; r[0] = __builtin_amdgcn_rcpf(d[0]); r[1] = __builtin_amdgcn_rcpf(d[1]);
      return cvtpk2(v * r);
    };
#pragma unroll
    for (int n = 0; n < NB; ++n) {
      constexpr int sl[6] = {0, 1, 3, 4, 5, 7};
      f32x4 cs = *(const LAS f32x4*)(size_t)(csl + sl[n] * 64 + fq * 16); if (n == 1 || n == 4) cs += *(const LAS f32x4*)(size_t)(csl + (sl[n] + 1) * 64 + fq * 16);
      const u32x2 bb = *(const LAS u32x2*)(size_t)(scr + (unsigned)fq * 8u + n * 32);
      const f32x2 c01 = {cs[0], cs[1]}, c23 = {cs[2], cs[3]}, b01 = {bflo(bb[0]), bfhi(bb[0])}, b23 = {bflo(bb[1]), bfhi(bb[1])};
#pragma unroll
      for (int j = 0; j < 2; ++j) {
        const f32x4 a = acc[2 * j][n], b = acc[2 * j + 1][n];
        const unsigned a0 = gelu2((f32x2){a[0], a[1]}, c01, b01, rs[2 * j], rm[2 * j]), a1 = gelu2((f32x2){a[2], a[3]}, c23, b23, rs[2 * j], rm[2 * j]);
        const unsigned b0 = gelu2((f32x2){b[0], b[1]}, c01, b01, rs[2 * j + 1], rm[2 * j + 1]), b1_ = gelu2((f32x2){b[2], b[3]}, c23, b23, rs[2 * j + 1], rm[2 * j + 1]);
        auto r0 = __builtin_amdgcn_permlane16_swap(a0, b0, false, false);
        auto r1 = __builtin_amdgcn_permlane16_swap(a1, b1_, false, false);
        u32x4 w; w[0] = r0[0]; w[1] = r1[0]; w[2] = r0[1]; w[3] = r1[1];
        __builtin_amdgcn_raw_buffer_store_b128(w, ro, vo + n * 2 * KBS + j * 512, 0, 16);
      }
    }
  }
  WAIT_V(0);
  if (lane == 0) {
    const unsigned old = __hip_atomic_fetch_add((LAS unsigned*)(size_t)(lds0 + KM_CNT + g * 4), 1u, __ATOMIC_RELAXED, __HIP_MEMORY_SCOPE_WORKGROUP);
    if (old == 3u) {
      const int hid = TYPE == 0 ? (g == 0 ? ct : 13 + ct) : (g == 0 ? ct - 13 : 19 + ct - 10);
      st_sc1(TYPE == 0 ? sy + SY_D + hid * 8 + pm : sy + SY_M + pm * 48 + hid, tag);
    }
  }
  ST(3);
}

template <bool TR>
DI void k2_loop(unsigned lds0, const K2Issue& c, int g, int wi, int lane, f32x4 (&acc)[4][4], bool early ST_ARG) {
  const int fr = lane & 15, fq = lane >> 4, wmi = wi >> 1, wn = wi & 1;
  constexpr int NT = TR ? F / 128 : S / 64;
  const unsigned sw = (unsigned)((fr >> 1) & 7);
  if (TR) {
    if (g == 0) { k2_sm(c, 0, K2_R0); k2_sm(c, 1, K2_R1); k2_touch(c); }
    else { k2_sm(c, 0, K2_R0); k2_big(c, 0, K2_R0, K2_Q0); k2_sm(c, 1, K2_R1); k2_big(c, 1, K2_R1, K2_Q1); k2_sm(c, 2, K2_R2); }
  } else {
    if (early) {
      if (g == 0) { k2_big(c, 0, K2_R0, K2_Q0); k2_sm(c, 0, K2_R0); k2_big(c, 1, K2_R1, K2_Q1); k2_sm(c, 1, K2_R1); k2_touch<LEADT>(c); }
      else { k2_sm(c, 0, K2_R0); k2_big(c, 0, K2_R0, K2_Q0); k2_sm(c, 1, K2_R1); k2_big(c, 1, K2_R1, K2_Q1); k2_sm(c, 2, K2_R2); k2_touch(c); }
    } else {
      if (g == 0) { k2_big(c, 0, K2_R0, K2_Q0); k2_big(c, 1, K2_R1, K2_Q1); }
      else k2_touch(c);
    }
  }
  const unsigned abase = lds0 + (TR ? (g == 0 ? 16384u : 0u) : (g == 0 ? 0u : 16384u));
  const bool aIsQ = TR && g == 1;
  const unsigned aoff0 = TR ? abase + (unsigned)fq * 2048u + (unsigned)(wmi * 64 + fr) * 16u : abase + (unsigned)(wmi * 64 + fr) * 128u + (((unsigned)fq ^ sw) << 4);
  const unsigned aoff1 = aoff0 ^ 64u;
  constexpr int AM = TR ? 256 : 2048;
  unsigned boff[4];
  if (TR) {
    const unsigned q = (lane & 15) >> 2, p = lane & 3, g4 = lane >> 4, s8 = q + 4u * (g4 & 1u);
#pragma unroll
    for (int n = 0; n < 4; ++n) boff[n] = lds0 + (s8 + 16u * (g4 >> 1)) * 256u + ((((unsigned)(wn * 4 + n)) ^ s8) << 5) + p * 8u;
  } else {
    boff[0] = lds0 + (unsigned)fq * 2048u + (unsigned)(wn * 64 + fr) * 16u; boff[1] = boff[2] = boff[3] = 0;
  }
  unsigned curR = K2_R0, curQ = K2_Q0;
  u32x4 rb, rs; unsigned mb, ms, pa0, pa1, pb[4];
  auto prep = [&](int t) {
    const unsigned prvR = curR == 0u ? 65536u : curR - 32768u, prvQ = curQ == K2_Q0 ? K2_Q2 : curQ - 16384u;
    const int tsm = t + 2 + g;
    rb = mksrd(c.bigbase + k2_phys(c, t + 2) * c.bigstep, t + 2 < NT ? 0x7fffffffu : 0u);
    rs = mksrd(c.Abase + k2_phys(c, tsm) * c.astep, tsm < NT ? 0x7fffffffu : 0u);
    mb = c.mBig + (c.bigIsQ ? prvQ : prvR); ms = c.mSm + (g == 0 ? prvR : curR);
    const unsigned sa = aIsQ ? curQ : curR, sb = TR ? curR : curQ;
    pa0 = aoff0 + sa; pa1 = aoff1 + sa;
#pragma unroll
    for (int n = 0; n < (TR ? 4 : 1); ++n) pb[n] = boff[n] + sb;
  };
  prep(0);
  if (TR) {
    if (g == 0) { WAIT_V(2 + NPT); BAR(); } else { WAIT_V(12); BAR(); WAIT_V(6); BAR(); }
  } else {
    if (g == 0) { if (early) WAIT_V(6 + LEADT - 2); else WAIT_V(4); BAR(); } else { WAIT_V(12 + NPT); BAR(); WAIT_V(6 + NPT); BAR(); }
  }
  SCHED();
  ST(5); STC(2);
  auto rdA = [&](int m, int ks) -> bf16x8 { return TR ? ldsd128(pa0 + m * AM + ks * 8192) : ldsd128((ks ? pa1 : pa0) + m * AM); };
  auto rdB = [&](int n, int ks) -> bf16x8 { return TR ? ldstr(pb[n] + ks * 8192, pb[n] + ks * 8192 + 2048) : ldsd128(pb[0] + n * 256 + ks * 8192); };
  for (int t = 0; t < NT; ++t) {
    bf16x8 af[4][2], bfr[4][2];
    dma_m0(rb, c.voffBig[0], c.sBig[0], mb);
    af[0][0] = rdA(0, 0); af[0][1] = rdA(0, 1); bfr[0][0] = rdB(0, 0);
    dma_off<1024>(rb, c.voffBig[1], c.sBig[1]);
    af[1][0] = rdA(1, 0); af[1][1] = rdA(1, 1); bfr[1][0] = rdB(1, 0);
    dma_off<2048>(rb, c.voffBig[0], c.sBig[2]);
    af[2][0] = rdA(2, 0); af[2][1] = rdA(2, 1); bfr[2][0] = rdB(2, 0);
    dma_off<3072>(rb, c.voffBig[1], c.sBig[3]);
    af[3][0] = rdA(3, 0); af[3][1] = rdA(3, 1);
    dma_m0(rs, c.voffA[0], c.sSm[0], ms);
    bfr[3][0] = rdB(3, 0); bfr[0][1] = rdB(0, 1); bfr[1][1] = rdB(1, 1);
    dma_off<1024>(rs, c.voffA[1], c.sSm[1]);
    bfr[2][1] = rdB(2, 1); bfr[3][1] = rdB(3, 1);
    WAIT_L(0); BAR(); SCHED();
    __builtin_amdgcn_s_setprio(1);
#pragma unroll
    for (int ks = 0; ks < 2; ++ks)
#pragma unroll
      for (int n = 0; n < 4; ++n)
#pragma unroll
        for (int m = 0; m < 4; ++m) acc[m][n] = MFMA(bfr[n][ks], af[m][ks], acc[m][n]);
    curR = curR == 65536u ? 0u : curR + 32768u; curQ = curQ == K2_Q2 ? K2_Q0 : curQ + 16384u;
    prep(t + 1);
    mfma_shadow<32>();
    if (TR) asm volatile("" : "+s"(rb), "+s"(rs), "+s"(mb), "+s"(ms), "+s"(curR), "+s"(curQ), "+v"(pa0), "+v"(pb[0]), "+v"(pb[1]), "+v"(pb[2]), "+v"(pb[3]));
    else asm volatile("" : "+s"(rb), "+s"(rs), "+s"(mb), "+s"(ms), "+s"(curR), "+s"(curQ), "+v"(pa0), "+v"(pa1), "+v"(pb[0]));
    __builtin_amdgcn_s_setprio(0);
    SCHED();
    WAIT_V(6);
    if (!(g == 1 && t == NT - 1)) BAR();
    SCHED();
  }
  WAIT_V(0);
}

__global__ __launch_bounds__(512, 2) void k_main(u16* xn, const u16* __restrict__ Dr, const u16* __restrict__ Di, const u16* __restrict__ w1,
                                                 const u16* __restrict__ b1, u16* abT, u16* h, const u16* __restrict__ Cr, const u16* __restrict__ Ci,
                                                 const u16* __restrict__ w2, const u16* __restrict__ x, const u16* __restrict__ b2, u16* __restrict__ out,
                                                 unsigned char* xbuf, unsigned long long* sync DBG_ARG) {
  ST_DECL; ST(0);
  extern __shared__ __attribute__((aligned(16))) unsigned char smem[];
  const unsigned lds0 = (unsigned)(size_t)(LAS unsigned char*)smem;
  asm volatile("" : "+s"(xn), "+s"(Dr), "+s"(Di), "+s"(w1), "+s"(b1), "+s"(x), "+s"(sync));
  const int bid = blockIdx.x, xcd = bid & 7, j = bid >> 3;
  const int pm = (xcd & 1) * 4 + (j & 3), ct = CTMAP(xcd, j);
  const int role = xcd >> 1, pn = j >> 2, tile = pm * 8 + pn;
  const int tid = threadIdx.x, wid = (int)rfl(tid >> 6), lane = tid & 63, g = wid >> 2, wi = wid & 3;
  gu64* sy = (gu64*)sync;
  const int lnrow = pm * 256 + ct * 8 + wid;
  const unsigned xl = lds0 + 65536u + (unsigned)wid * 2048u;
  { const u32x4 rx = mksrd(x + (size_t)lnrow * D, D * 2u); dma_m0(rx, (unsigned)lane * 16u, 0u, xl); dma_off<1024>(rx, (unsigned)lane * 16u, 0u); }
  if (CODEW && wid == 1) {
    unsigned long long pc; asm volatile("s_getpc_b64 %0" : "=s"(pc));
    touch(mksrd((const void*)(pc & ~127ull), CODEW), (unsigned)(j * 16 + (lane & 15)) * 128u, 0u, lds0 + KM_SCR + 256u);
  }
  u64 stv;
  u64 tag;
  if (tid < 2) *(LAS unsigned*)(size_t)(lds0 + KM_CNT + tid * 4) = 0u;
  const int type0 = ct < 13 ? 0 : 1, col00 = ct < 13 ? 96 * ct : 96 * (ct - 13);
  const int type1 = ct < 10 ? 0 : 1, col01 = ct < 10 ? 1248 + 80 * ct : 1824 + 80 * (ct - 10);
  const bool perm = type0 == 0;
  K1Issue c;
  c.ra = mksrd(x + (size_t)pm * 256 * D, 0x7fffffffu);
  c.leadmask = 1 << (((j & 3) + (j >> 2)) & 3);
  const unsigned i = (unsigned)lane;
#pragma unroll
  for (int par = 0; par < 2; ++par) c.voffA[par] = (perm ? 8u * (i >> 5) + ((i >> 3) & 3u) : (i >> 3)) * (D * 2u) + (((i & 7u) ^ (i >> 4) ^ ((unsigned)par << 2)) << 4);
#pragma unroll
  for (int q = 0; q < 4; ++q) { const unsigned p = (unsigned)(g * 16 + wi * 4 + q); c.sA[q] = (perm ? 16u * (p & 1u) + 4u * ((p >> 1) & 1u) + 32u * (p >> 2) : 8u * p) * (D * 2u) - q * 1024u; }
  c.mA = lds0 + (unsigned)(g * 16 + wi * 4) * 1024u;
  { const unsigned ka = k1_phys(c, 0) * 128u;
    dma_m0(c.ra, c.voffA[0], c.sA[0] + ka, c.mA); dma_off<1024>(c.ra, c.voffA[1], c.sA[1] + ka); dma_off<2048>(c.ra, c.voffA[0], c.sA[2] + ka); dma_off<3072>(c.ra, c.voffA[1], c.sA[3] + ka); }
  SCHED();
  const int otype = g == 0 ? type1 : type0, ocol = g == 0 ? col01 : col00;
  const unsigned cpr = g == 0 ? 10u : 12u, oring = g == 0 ? K1_B1 : K1_B0;
  c.bstep = otype == 0 ? 128u : 64u * F * 2u;
  c.mB = lds0 + oring + (unsigned)wi * 3072u;
#pragma unroll
  for (int q = 0; q < 3; ++q) {
    const unsigned p = (unsigned)(3 * wi + q), nr = p < cpr ? 0x7fffffffu : 0u;
    if (otype == 0) {
      int e0 = ocol + 8 * (int)p; if (e0 > 2040) e0 = 2040;
      c.rb[q] = mksrd((const unsigned char*)(e0 < 1024 ? Dr + (size_t)e0 * D : Di + (size_t)(e0 - 1024) * D) - q * 1024, nr);
      c.voffB[q] = (i >> 3) * (D * 2u) + (((i & 7u) ^ (i >> 4) ^ ((p & 1u) << 2)) << 4);
    } else {
      const unsigned ci = 64u * p + i, rho = ci / cpr, pos = ci - rho * cpr, sp = pos >> 1;
      const unsigned seg = cpr == 12u ? sp ^ ((rho >> 2) & 1u) : sp;
      const unsigned k = (rho & 3u) + 8u * ((rho >> 2) & 1u) + 4u * ((rho >> 3) & 1u) + (rho & 48u);
      c.rb[q] = mksrd((const unsigned char*)(w1 + ocol) - q * 1024, nr);
      c.voffB[q] = k * (F * 2u) + seg * 32u + (pos & 1u) * 16u;
    }
  }
  const bool tRole = role < 2;
  const K2Args a2 = {Cr, Ci, abT, h, w2};
  {
    const K1Step s0 = k1_prep(c, 0, 0u, 0);
    if (g == 1) { k1_dma<4>(c, s0); k1_dma<5>(c, s0); k1_dma<6>(c, s0); }
    asm volatile("s_load_dwordx2 %0, %1, 0x0\n\ts_waitcnt lgkmcnt(0)" : "=s"(tag) : "s"(sync + SY_EPOCH) : "memory");
    tag += 1ull;
    if (g == 1) WAIT_V(7); else WAIT_V(4);
    const u32x4 v0 = *(const LAS u32x4*)(size_t)(xl + lane * 16u), v1 = *(const LAS u32x4*)(size_t)(xl + 1024u + lane * 16u);
    float f[16];
#pragma unroll
    for (int q = 0; q < 4; ++q) { f[2 * q] = bflo(v0[q]); f[2 * q + 1] = bfhi(v0[q]); f[8 + 2 * q] = bflo(v1[q]); f[9 + 2 * q] = bfhi(v1[q]); }
    float sm = 0.f;
#pragma unroll
    for (int q = 0; q < 16; ++q) sm += f[q];
    asm volatile("" : "+v"(sm)); STL(0);
    sm = wave_sum(sm);
    const float mean = sm * (1.0f / D);
    float sq = 0.f;
#pragma unroll
    for (int q = 0; q < 16; ++q) { f[q] -= mean; sq += f[q] * f[q]; }
    sq = wave_sum(sq);
    const float rs = 1.0f / sqrtf(sq * (1.0f / D) + 1e-5f);
    stv = (tag << 32) | (u64)__float_as_uint(lane ? rs : mean);
    STL(1);
  }
  if (g == 0) {
    if (type0 == 0) k1_group<0, 6>(lds0, c, a2, tRole, perm, wi, lane, pm, col00, abT, h, b1, sy, tag, ct, stv ST_PASS);
    else k1_group<1, 6>(lds0, c, a2, tRole, perm, wi, lane, pm, col00, abT, h, b1, sy, tag, ct, stv ST_PASS);
  } else {
    if (type1 == 0) k1_group<0, 5>(lds0, c, a2, tRole, perm, wi, lane, pm, col01, abT, h, b1, sy, tag, ct, stv ST_PASS);
    else k1_group<1, 5>(lds0, c, a2, tRole, perm, wi, lane, pm, col01, abT, h, b1, sy, tag, ct, stv ST_PASS);
  }
  if (bid == 0 && wid == 1) {
    gu64* pq = sy + SY_LN + lane;
#pragma unroll 1
    for (unsigned spins = 0; spins < (1u << 20); ++spins) {
      const u64 a0 = ld_sc1(pq), a1 = ld_sc1(pq + 64), a2_ = ld_sc1(pq + 128), a3 = ld_sc1(pq + 192);
      if (__builtin_amdgcn_ballot_w64(a0 != tag || a1 != tag || a2_ != tag || a3 != tag) == 0ull) break;
      __builtin_amdgcn_s_sleep(2);
    }
    if (lane == 0) st_sc1(sy + SY_EPOCH, tag);
  }
  const K2Issue c2 = k2_make(a2, lds0);
  auto wait_deps = [&]() {
    if (tRole) {
      const int eb = role * 8 + pn, lo = eb * 128, hi = lo + 127;
      const int hlo = lo < 1248 ? lo / 96 : 13 + (lo - 1248) / 80, hhi = hi < 1248 ? hi / 96 : 13 + (hi - 1248) / 80;
      wait_words(sy + SY_D + hlo * 8, (hhi - hlo + 1) * 8, tag, lane);
    } else wait_words(sy + SY_M + pm * 48 + (role == 2 ? 0 : 18), role == 2 ? 19 : 23, tag, lane);
  };
  const bool early = EARLYT && tRole && type1 == 1;
  if (early && wid == 0) wait_deps();
  BAR();
  asm volatile("" ::: "memory");
  if (!early) {
    k2_pre(c2, g, tRole, 0); k2_pre(c2, g, tRole, 1);
    if (tRole) { if (g == 0) k2_touch<LEADT>(c2); else k2_sm(c2, 2, K2_R2); }
    else if (g == 1) k2_touch(c2);
    if (wid == 0) wait_deps();
    BAR();
    asm volatile("" ::: "memory");
  }
  ST(4);
  f32x4 acc[4][4];
#pragma unroll
  for (int m = 0; m < 4; ++m)
#pragma unroll
    for (int n = 0; n < 4; ++n) acc[m][n] = (f32x4){0.f, 0.f, 0.f, 0.f};
  if (tRole) k2_loop<false>(lds0, c2, g, wi, lane, acc, early ST_PASS);
  else k2_loop<true>(lds0, c2, g, wi, lane, acc, false ST_PASS);
  ST(6); STC(3);
  const __amdgpu_buffer_rsrc_t xr = mkrsrc(xbuf + (size_t)tile * (4 * 8 * 8 * 1024), 4u * 8u * 8u * 1024u);
  const __amdgpu_buffer_rsrc_t xw = mkrsrc(xbuf + (size_t)tile * (4 * 8 * 8 * 1024) + (role * 8 + wid) * 8192, 8192u);
  const int fr = lane & 15, fq = lane >> 4, wmi = wi >> 1, wn = wi & 1;
  const int trow = pm * 256 + g * 128 + wmi * 64 + role * 16 + fr, ecol = pn * 128 + wn * 64 + 4 * fq;
#pragma unroll
  for (int m = 0; m < 4; ++m)
    if (m != role) {
#pragma unroll
      for (int pr = 0; pr < 2; ++pr) {
        const f32x4 a = acc[m][2 * pr], b = acc[m][2 * pr + 1];
        u32x4 w; w[0] = cvtpk(a[0], a[1]); w[1] = cvtpk(a[2], a[3]); w[2] = cvtpk(b[0], b[1]); w[3] = cvtpk(b[2], b[3]);
        __builtin_amdgcn_raw_buffer_store_b128(w, xw, lane * 16 + (m * 2 + pr) * 1024, 0, 16);
      }
    }
  gu64* fl = sy + SY_X + (tile * 8 + wid) * 8;
  u64 f0 = ld_sc1(fl + (lane & 3));
  f32x4 own[4];
#pragma unroll
  for (int n = 0; n < 4; ++n) own[n] = role == 0 ? acc[0][n] : role == 1 ? acc[1][n] : role == 2 ? acc[2][n] : acc[3][n];
  STX(0);
  WAIT_V(0);
  STX(1);
  if (lane == 0) st_sc1(fl + role, tag);
  asm volatile("" ::: "memory");
  u32x2 xv[4], bv[4];
#pragma unroll
  for (int n = 0; n < 4; ++n) { xv[n] = *(const GAS u32x2*)(x + (size_t)trow * D + ecol + 16 * n); bv[n] = *(const u32x2*)(b2 + ecol + 16 * n); }
  u32x4 pk[4][2];
#pragma unroll
  for (int r = 0; r < 4; ++r) { pk[r][0] = (u32x4){0u, 0u, 0u, 0u}; pk[r][1] = (u32x4){0u, 0u, 0u, 0u}; }
  unsigned have = 1u << role;
#pragma unroll 1
  for (unsigned spins = 0;;) {
    const unsigned rdy = (unsigned)__builtin_amdgcn_ballot_w64(f0 == tag) & 15u;
    const unsigned nw = rdy & ~have;
    asm volatile("" ::: "memory");
    have |= nw;
    if (have != 15u) f0 = ld_sc1(fl + (lane & 3));
#pragma unroll
    for (int r = 0; r < 4; ++r)
      if (nw & (1u << r)) {
#pragma unroll
        for (int pr = 0; pr < 2; ++pr) pk[r][pr] = __builtin_bit_cast(u32x4, __builtin_amdgcn_raw_buffer_load_b128(xr, lane * 16, ((r * 8 + wid) * 8 + role * 2 + pr) * 1024, 16));
      }
    if (have == 15u || ++spins > (1u << 20)) break;
    if (!nw) __builtin_amdgcn_s_sleep(XSLEEP);
  }
  STX(2);
  WAIT_V(0);
  STX(3);
#pragma unroll
  for (int n = 0; n < 4; ++n) {
    const float xb[4] = {bflo(xv[n][0]), bfhi(xv[n][0]), bflo(xv[n][1]), bfhi(xv[n][1])};
    const float bb[4] = {bflo(bv[n][0]), bfhi(bv[n][0]), bflo(bv[n][1]), bfhi(bv[n][1])};
    float pv[4][4];
#pragma unroll
    for (int r = 0; r < 4; ++r) {
      const unsigned w0 = pk[r][n >> 1][(n & 1) * 2], w1_ = pk[r][n >> 1][(n & 1) * 2 + 1];
      pv[r][0] = r == role ? own[n][0] : bflo(w0); pv[r][1] = r == role ? own[n][1] : bfhi(w0);
      pv[r][2] = r == role ? own[n][2] : bflo(w1_); pv[r][3] = r == role ? own[n][3] : bfhi(w1_);
    }
    float o[4];
#pragma unroll
    for (int e = 0; e < 4; ++e) {
      const float y = pv[0][e] - pv[1][e], z = pv[2][e] + pv[3][e] + bb[e];
      const float gz = z * __builtin_amdgcn_rcpf(1.0f + __builtin_amdgcn_exp2f(-1.4426950408889634f * z));
      o[e] = xb[e] + y * gz;
    }
    u32x2 w; w[0] = cvtpk(o[0], o[1]); w[1] = cvtpk(o[2], o[3]);
    __builtin_amdgcn_raw_buffer_store_b64(w, mkrsrc(out, (unsigned)(S * D * 2)), (unsigned)(trow * D + ecol + 16 * n) * 2u, 0, OUTAUX);
  }
}

extern "C" void kernel_launch(void* const* d_in, const int* in_sizes, int n_in, void* d_out, int out_size, void* d_ws, size_t ws_size, hipStream_t stream) {
  const u16* x = (const u16*)d_in[0]; const u16* Cr = (const u16*)d_in[1]; const u16* Ci = (const u16*)d_in[2];
  const u16* Dr = (const u16*)d_in[3]; const u16* Di = (const u16*)d_in[4];
  const u16* w1 = (const u16*)d_in[6]; const u16* b1 = (const u16*)d_in[7]; const u16* w2 = (const u16*)d_in[8]; const u16* b2 = (const u16*)d_in[9];
  char* ws = (char*)d_ws;
  unsigned long long* sync = (unsigned long long*)ws;
  u16* xn = (u16*)(ws + (1 << 20));
  u16* abT = (u16*)(ws + (6 << 20));
  u16* h = (u16*)(ws + (18 << 20));
  unsigned char* xbuf = (unsigned char*)(ws + (40 << 20));
  (void)hipFuncSetAttribute((const void*)k_main, hipFuncAttributeMaxDynamicSharedMemorySize, KM_LDS);
  k_main<<<256, 512, KM_LDS, stream>>>(xn, Dr, Di, w1, b1, abT, h, Cr, Ci, w2, x, b2, (u16*)d_out, xbuf, sync DBG_PASS);
}
```

```cpp
#include <hip/hip_runtime.h>
#include <hip/hip_bf16.h>

typedef unsigned short u16;
typedef __bf16 bf16x8 __attribute__((ext_vector_type(8)));
typedef short s16x4 __attribute__((ext_vector_type(4)));
typedef short s16x8 __attribute__((ext_vector_type(8)));
typedef float f32x4 __attribute__((ext_vector_type(4)));
typedef float f32x2 __attribute__((ext_vector_type(2)));
typedef __bf16 bf16x2 __attribute__((ext_vector_type(2)));
typedef unsigned u32x4 __attribute__((ext_vector_type(4)));
typedef unsigned u32x2 __attribute__((ext_vector_type(2)));
#define LAS __attribute__((address_space(3)))
#define GAS __attribute__((address_space(1)))
#define DI __device__ __forceinline__

constexpr int S = 2048, D = 1024, F = 3584;

#define LEAD1 4
#define LEAD2 4
#define LEADT 8
#define LNKT 6
#define PREIN 0
#define XSLEEP 1
#define WARMC 0
#define SWARM 0
#define OUTAUX 0
#define XNAUX 0
#define CODEW 40448
#define PRET 1
#define XMAP 1
#define XCDL(b) (int)(((XMAP == 1 ? 0x73625140u : XMAP == 2 ? 0x76325410u : 0x76543210u) >> (((unsigned)(b) & 7u) * 4u)) & 7u)
#define XWARM 6
#define EARLYT 1
#define PREBAR 1
#define DBAL 0

#define CTMAP(xcd, j) (DBAL == 2 ? ((xcd) >> 1) + 4 * ((j) >> 2) : (DBAL && (xcd) < 4) ? 2 * ((j) >> 2) + ((xcd) >> 1) : ((xcd) >> 1) * 8 + ((j) >> 2))
#define NOACQ 1
#define SC1S " sc1"
#define WAIT_V(nn) asm volatile("s_waitcnt vmcnt(%0)" ::"n"(nn) : "memory")
#define WAIT_L(n) do { asm volatile("" ::: "memory"); __builtin_amdgcn_s_waitcnt(0xC07F); asm volatile("" ::: "memory"); } while (0)
#define BAR() __builtin_amdgcn_s_barrier()
#define SCHED() __builtin_amdgcn_sched_barrier(0)

DI unsigned rfl(unsigned x) { return __builtin_amdgcn_readfirstlane(x); }
DI float bflo(unsigned v) { return __uint_as_float(v << 16); }
DI float bfhi(unsigned v) { return __uint_as_float(v & 0xffff0000u); }
DI float fadd1(float a, float b) { float r; asm("v_add_f32_e32 %0, %1, %2" : "=v"(r) : "v"(a), "v"(b)); return r; }
DI unsigned cvtpk(float lo, float hi) { f32x2 v = {lo, hi}; bf16x2 b = __builtin_convertvector(v, bf16x2); return __builtin_bit_cast(unsigned, b); }
DI unsigned cvtpk2(f32x2 v) { bf16x2 b = __builtin_convertvector(v, bf16x2); return __builtin_bit_cast(unsigned, b); }
DI __amdgpu_buffer_rsrc_t mkrsrc(const void* p, unsigned n) { return __builtin_amdgcn_make_buffer_rsrc((void*)p, (short)0, (int)n, 0x00020000); }
DI u32x4 mksrd(const void* p, unsigned n) { const unsigned long long a = (unsigned long long)p; u32x4 r; r[0] = (unsigned)a; r[1] = (unsigned)(a >> 32) & 0xffffu; r[2] = n; r[3] = 0x00020000u; return r; }
DI void dma_m0(const u32x4& r, unsigned voff, unsigned soff, unsigned m0v) {
  asm volatile("s_mov_b32 m0, %0\n\ts_nop 0\n\tbuffer_load_dwordx4 %1, %2, %3 offen" SC1S " lds" ::"s"(m0v), "v"(voff), "s"(r), "s"(soff) : "memory");
}
template <int OFF> DI void dma_off(const u32x4& r, unsigned voff, unsigned soff) {
  asm volatile("buffer_load_dwordx4 %0, %1, %2 offen offset:%3" SC1S " lds" ::"v"(voff), "s"(r), "s"(soff), "n"(OFF) : "memory");
}
DI void touch(const u32x4& r, unsigned voff, unsigned soff, unsigned m0v) {
  asm volatile("s_mov_b32 m0, %0\n\ts_nop 0\n\tbuffer_load_dword %1, %2, %3 offen" SC1S " lds" ::"s"(m0v), "v"(voff), "s"(r), "s"(soff) : "memory");
}
DI bf16x8 ldsd128(unsigned a) { return *(const LAS bf16x8*)(size_t)a; }
DI bf16x8 ldstr(unsigned a0, unsigned a1) {
  s16x4 a = __builtin_amdgcn_ds_read_tr16_b64_v4i16((LAS s16x4*)(size_t)a0);
  s16x4 b = __builtin_amdgcn_ds_read_tr16_b64_v4i16((LAS s16x4*)(size_t)a1);
  s16x8 c = __builtin_shufflevector(a, b, 0, 1, 2, 3, 4, 5, 6, 7);
  return __builtin_bit_cast(bf16x8, c);
}
DI float wave_sum(float v) {
#define DPPADD(ctrl, rm) v += __int_as_float(__builtin_amdgcn_update_dpp(0, __float_as_int(v), ctrl, rm, 0xf, false))
  DPPADD(0x111, 0xf); DPPADD(0x112, 0xf); DPPADD(0x114, 0xf); DPPADD(0x118, 0xf); DPPADD(0x142, 0xa); DPPADD(0x143, 0xc);
#undef DPPADD
  return __int_as_float(__builtin_amdgcn_readlane(__float_as_int(v), 63));
}
#define MFMA(a, b, c) __builtin_amdgcn_mfma_f32_16x16x32_bf16(a, b, c, 0, 0, 0)
template <int V> struct IC { static constexpr int value = V; };
template <int N> DI void mfma_shadow() {
#pragma unroll
  for (int i = 0; i < N; ++i) { __builtin_amdgcn_sched_group_barrier(0x8, 1, 0); __builtin_amdgcn_sched_group_barrier(0x6, 2, 0); }
}
template <int N> DI void mfma_shadow3() {
#pragma unroll
  for (int i = 0; i < N / 2; ++i) {
    __builtin_amdgcn_sched_group_barrier(0x8, 1, 0); __builtin_amdgcn_sched_group_barrier(0x2, 2, 0);
    __builtin_amdgcn_sched_group_barrier(0x8, 1, 0); __builtin_amdgcn_sched_group_barrier(0x2, 1, 0); __builtin_amdgcn_sched_group_barrier(0x4, 2, 0);
  }
}

#define ST_DECL
#define ST_ARG
#define ST_PASS
#define DBG_ARG
#define DBG_PASS
#define ST(i)
#define STC(i)
#define STL(i)
#define STX(i)
#define ST_OUT(dbg, kid, w)

constexpr unsigned K1_B0 = 98304, K1_B1 = 122880, K1_BSLOT = 12288, KM_SCR = 147456  , KM_CNT = KM_SCR + 2048  ,
                   KM_ST = KM_CNT + 64  , KM_CS = KM_ST + 8192  , KM_LDS = KM_CS + 1536;
constexpr unsigned K2_R0 = 32768, K2_R1 = 65536, K2_R2 = 0, K2_Q0 = 98304, K2_Q1 = 114688, K2_Q2 = 131072;
#define KBSV 32768
constexpr unsigned KBS = KBSV;
constexpr int K1_NT = D / 64;
constexpr int NPT = LEAD2 > 2 ? LEAD2 - 2 : 0;
typedef unsigned long long u64;
typedef GAS u64 gu64;
constexpr int SY_EPOCH = 0, SY_LN = 16  , SY_D = SY_LN + 256  , SY_M = SY_D + 192  , SY_X = SY_M + 384  , SY_ST = 8192  ;
DI u64 ld_sc1(gu64* p) { return __hip_atomic_load(p, __ATOMIC_RELAXED, __HIP_MEMORY_SCOPE_AGENT); }
DI void st_sc1(gu64* p, u64 v) { __hip_atomic_store(p, v, __ATOMIC_RELAXED, __HIP_MEMORY_SCOPE_AGENT); }
DI void wait_words(gu64* p, int n, u64 tag, int lane) {
  gu64* q = p + (lane < n ? lane : 0);
#pragma unroll 1
  for (unsigned spins = 0; __builtin_amdgcn_ballot_w64(ld_sc1(q) != tag) != 0ull && spins < (1u << 20); ++spins) __builtin_amdgcn_s_sleep(2);
  if (!NOACQ) { __builtin_amdgcn_fence(__ATOMIC_ACQUIRE, "agent"); WAIT_V(0); }
  asm volatile("" ::: "memory");
}

struct K1Issue {
  u32x4 ra, rb[3];
  unsigned voffA[2], voffB[3];
  unsigned sA[4];
  unsigned mA, mB, bstep;
  int leadmask;
};
struct K1Step { unsigned sa[4], kb, ma, mb; };
DI unsigned k1_phys(const K1Issue& c, int t) { return (unsigned)(t + (((c.leadmask >> (t & 3)) & 1) * LEAD1)) & (unsigned)(K1_NT - 1); }
DI K1Step k1_prep(const K1Issue& c, int tA, unsigned slotA, int tB) {
  K1Step s; const unsigned ka = k1_phys(c, tA) * 128u;
#pragma unroll
  for (int q = 0; q < 4; ++q) s.sa[q] = c.sA[q] + ka;
  s.kb = k1_phys(c, tB) * c.bstep; s.ma = c.mA + slotA; s.mb = c.mB + ((tB & 1) ? K1_BSLOT : 0u);
  return s;
}
template <int Q> DI void k1_dma(const K1Issue& c, const K1Step& s) {
  if (Q == 0) dma_m0(c.ra, c.voffA[0], s.sa[0], s.ma);
  else if (Q < 4) dma_off<(Q & 3) * 1024>(c.ra, c.voffA[Q & 1], s.sa[Q & 3]);
  else if (Q == 4) dma_m0(c.rb[0], c.voffB[0], s.kb, s.mb);
  else if (Q == 5) dma_off<1024>(c.rb[1], c.voffB[1], s.kb);
  else dma_off<2048>(c.rb[2], c.voffB[2], s.kb);
}

struct K2Issue {
  const unsigned char* Abase; const unsigned char* bigbase; const unsigned char* tchbase;
  unsigned voffA[2], voffBig[2], sBig[4], sSm[2], bigstep, astep, mBig, mSm, mScr, tchvoff, tchstep;
  int NT, leadmask, lead; bool bigIsQ;
};
DI unsigned k2_phys(const K2Issue& c, int t) { int p = t + (((c.leadmask >> (t & 7)) & 1) * c.lead); p = p >= c.NT ? p - c.NT : p; return (unsigned)p; }
DI K2Issue k2_setup(unsigned lds0, bool TR, const unsigned char* Abase, const unsigned char* Bbase, int NT, int g, int wi, int lane, int pm4, int pn8) {
  K2Issue c; const unsigned i = (unsigned)lane;
  c.Abase = Abase; c.NT = NT;
  if (!TR && LEADT == 8) { c.leadmask = 1 << ((2 * pm4 + pn8) & 7); c.lead = 8; }
  else { c.leadmask = 0x11 << ((pm4 + pn8) & 3); c.lead = 4; }
  c.astep = TR ? 8u * KBS : 128u;
#pragma unroll
  for (int par = 0; par < 2; ++par) c.voffA[par] = TR ? i * 16u : (i >> 3) * (S * 2u) + (((i & 7u) ^ (i >> 4) ^ ((unsigned)par << 2)) << 4);
  auto sApiece = [&](unsigned p, unsigned half) -> unsigned { return TR ? (p >> 1) * KBS + (p & 1u) * 1024u + half * 2048u : (half * 128u + p * 8u) * (S * 2u); };
  const unsigned idx = (unsigned)(wi * 8 + (lane & 7));
  if (g == 0) {
    c.bigbase = Bbase;
    if (TR) {
      c.bigstep = 64u * D * 2u;
#pragma unroll
      for (int par = 0; par < 2; ++par) c.voffBig[par] = (i >> 4) * (D * 2u) + ((((i & 15u) >> 1) ^ ((i >> 4) + 4u * par)) << 5) + (i & 1u) * 16u;
#pragma unroll
      for (int q = 0; q < 4; ++q) { const unsigned p = (unsigned)(4 * wi + q); c.sBig[q] = (8u * (p & 1u) + 4u * ((p >> 1) & 1u) + 16u * (p >> 2)) * (D * 2u); }
    } else {
      c.bigstep = 8u * KBS;
#pragma unroll
      for (int par = 0; par < 2; ++par) c.voffBig[par] = i * 16u;
#pragma unroll
      for (int q = 0; q < 4; ++q) { const unsigned p = (unsigned)(4 * wi + q); c.sBig[q] = (p >> 1) * KBS + (p & 1u) * 1024u; }
    }
    c.tchbase = Abase; c.tchstep = c.astep;
    c.tchvoff = TR ? (unsigned)pn8 * KBS + idx * 128u : ((unsigned)pn8 * 32u + idx) * (S * 2u);
  } else {
    c.bigbase = Abase; c.bigstep = c.astep;
#pragma unroll
    for (int par = 0; par < 2; ++par) c.voffBig[par] = c.voffA[par];
#pragma unroll
    for (int q = 0; q < 4; ++q) c.sBig[q] = sApiece((unsigned)(4 * wi + q), 1u);
    c.tchbase = Bbase;
    if (!TR) { c.tchvoff = (2u * pm4 + (idx >> 4)) * KBS + (idx & 15u) * 128u; c.tchstep = 8u * KBS; }
    else { c.tchvoff = (unsigned)(pm4 * 16 + wi * 4 + ((lane & 7) >> 1)) * (D * 2u) + (lane & 1) * 128u; c.tchstep = 64u * D * 2u; }
  }
  const unsigned psm = (unsigned)((g == 0 ? 8 : 0) + 2 * wi);
#pragma unroll
  for (int q = 0; q < 2; ++q) c.sSm[q] = sApiece(psm + q, 0u);
#pragma unroll
  for (int q = 0; q < 4; ++q) c.sBig[q] -= q * 1024u;
  c.sSm[1] -= 1024u;
  c.bigIsQ = (g == 0) != TR; c.mScr = lds0 + KM_SCR + (unsigned)(g * 4 + wi) * 256u;
  c.mBig = lds0 + ((g == 1 && !TR) ? 16384u : 0u) + (unsigned)wi * 4096u; c.mSm = lds0 + (TR ? 16384u : 0u) + psm * 1024u;
  return c;
}
DI void k2_big(const K2Issue& c, int tile, unsigned sR, unsigned sQ) {
  const u32x4 r = mksrd(c.bigbase + k2_phys(c, tile) * c.bigstep, 0x7fffffffu);
  dma_m0(r, c.voffBig[0], c.sBig[0], c.mBig + (c.bigIsQ ? sQ : sR));
  dma_off<1024>(r, c.voffBig[1], c.sBig[1]); dma_off<2048>(r, c.voffBig[0], c.sBig[2]); dma_off<3072>(r, c.voffBig[1], c.sBig[3]);
}
DI void k2_sm(const K2Issue& c, int tile, unsigned sR) {
  const u32x4 r = mksrd(c.Abase + k2_phys(c, tile) * c.astep, 0x7fffffffu);
  dma_m0(r, c.voffA[0], c.sSm[0], c.mSm + sR);
  dma_off<1024>(r, c.voffA[1], c.sSm[1]);
}
template <int TEND = LEAD2> DI void k2_touch(const K2Issue& c) {
#pragma unroll
  for (int t = 2; t < TEND; ++t) touch(mksrd(c.tchbase, 0x7fffffffu), c.tchvoff, (unsigned)t * c.tchstep, c.mScr);
}
DI void k2_pre(const K2Issue& c, int g, bool tRole, int tile) {
  const unsigned sR = tile ? K2_R1 : K2_R0, sQ = tile ? K2_Q1 : K2_Q0;
  if (tRole) { k2_sm(c, tile, sR); if (g == 1) k2_big(c, tile, sR, sQ); }
  else if (g == 0) k2_big(c, tile, sR, sQ);
}
struct K2Args { const u16* Cr; const u16* Ci; const u16* abT; const u16* h; const u16* w2; };
DI K2Issue k2_make(const K2Args& a, unsigned lds0) {
  int bid = blockIdx.x, tid = threadIdx.x; asm volatile("" : "+s"(bid), "+v"(tid));
  int xcd = XCDL(bid); asm volatile("" : "+s"(xcd));
  const int j = bid >> 3, pm = (xcd & 1) * 4 + (j & 3), role = xcd >> 1, pn = j >> 2;
  const int wid = (int)rfl(tid >> 6), lane = tid & 63, g = wid >> 2, wi = wid & 3;
  const bool tRole = role < 2; const int k0 = (role & 1) * (F / 2);
  const u16* A2 = tRole ? (role ? a.Ci : a.Cr) + (size_t)pm * 256 * S : a.h + (size_t)(k0 / 8) * (KBS / 2) + pm * 256 * 8;
  const u16* B2 = tRole ? a.abT + (size_t)(role * 1024 + pn * 128) * 8 : a.w2 + (size_t)k0 * D + pn * 128;
  K2Issue c = k2_setup(lds0, !tRole, (const unsigned char*)A2, (const unsigned char*)B2, tRole ? S / 64 : F / 128, g, wi, lane, j & 3, pn);
#pragma unroll
  for (int q = 0; q < 4; ++q) c.sBig[q] = rfl(c.sBig[q]);
#pragma unroll
  for (int q = 0; q < 2; ++q) c.sSm[q] = rfl(c.sSm[q]);
  c.bigstep = rfl(c.bigstep); c.astep = rfl(c.astep); c.mBig = rfl(c.mBig); c.mSm = rfl(c.mSm); c.mScr = rfl(c.mScr); c.tchstep = rfl(c.tchstep);
  asm volatile("s_nop 4" : "+s"(c.sBig[0]), "+s"(c.sBig[1]), "+s"(c.sBig[2]), "+s"(c.sBig[3]), "+s"(c.sSm[0]), "+s"(c.sSm[1]), "+s"(c.tchstep) :: "memory");
  return c;
}

#define CSM(acc, frag) "v_mfma_f32_16x16x32_bf16 %" #acc ", %" #frag ", %2, %" #acc "\n\t"
template <int NB> DI void cs_mma_bar(f32x4 (&csa)[2], const bf16x8& ones, int wi, const bf16x8 (&bfr)[NB][2]) {
  asm volatile("s_cmp_lt_u32 %3, 2\n\ts_cbranch_scc0 .Lcb2_%=\n\ts_cmp_eq_u32 %3, 0\n\ts_cbranch_scc0 .Lcb1_%=\n\t"
               "s_barrier\n\t" CSM(0, 4) CSM(1, 6) CSM(0, 5) "s_branch .Lcb4_%=\n.Lcb1_%=:\n\t"
               "s_barrier\n\t" CSM(1, 8) CSM(0, 7) CSM(1, 9) "s_branch .Lcb4_%=\n.Lcb2_%=:\n\ts_cmp_eq_u32 %3, 2\n\ts_cbranch_scc0 .Lcb3_%=\n\t"
               "s_barrier\n\t" CSM(0, 10) CSM(1, 12) CSM(0, 11) "s_branch .Lcb4_%=\n.Lcb3_%=:\n\t"
               "s_barrier\n\t" CSM(1, 14) CSM(0, 13) CSM(1, 15) ".Lcb4_%=:"
               : "+v"(csa[0]), "+v"(csa[1])
               : "v"(ones), "s"(wi), "v"(bfr[0][0]), "v"(bfr[0][1]), "v"(bfr[1][0]), "v"(bfr[1][1]), "v"(bfr[2][0]), "v"(bfr[2][1]), "v"(bfr[3][0]), "v"(bfr[3][1]),
                 "v"(bfr[4][0]), "v"(bfr[4][1]), "v"(bfr[NB - 1][0]), "v"(bfr[NB - 1][1])
               : "scc", "memory");
}
template <int NB> DI void cs_mma(f32x4 (&csa)[2], const bf16x8& ones, int wi, const bf16x8 (&bfr)[NB][2]) {
  asm volatile("s_cmp_lt_u32 %3, 2\n\ts_cbranch_scc0 .Lcs2_%=\n\ts_cmp_eq_u32 %3, 0\n\ts_cbranch_scc0 .Lcs1_%=\n\t"
               CSM(0, 4) CSM(1, 6) CSM(0, 5) "s_branch .Lcs4_%=\n.Lcs1_%=:\n\t"
               CSM(1, 8) CSM(0, 7) CSM(1, 9) "s_branch .Lcs4_%=\n.Lcs2_%=:\n\ts_cmp_eq_u32 %3, 2\n\ts_cbranch_scc0 .Lcs3_%=\n\t"
               CSM(0, 10) CSM(1, 12) CSM(0, 11) "s_branch .Lcs4_%=\n.Lcs3_%=:\n\t"
               CSM(1, 14) CSM(0, 13) CSM(1, 15) ".Lcs4_%=:"
               : "+v"(csa[0]), "+v"(csa[1])
               : "v"(ones), "s"(wi), "v"(bfr[0][0]), "v"(bfr[0][1]), "v"(bfr[1][0]), "v"(bfr[1][1]), "v"(bfr[2][0]), "v"(bfr[2][1]), "v"(bfr[3][0]), "v"(bfr[3][1]),
                 "v"(bfr[4][0]), "v"(bfr[4][1]), "v"(bfr[NB - 1][0]), "v"(bfr[NB - 1][1])
               : "scc");
}
#undef CSM

template <int TYPE  , int NB>
DI void k1_group(unsigned lds0, const K1Issue& c, const K2Args& a2, bool tRole, bool perm, int wi, int lane, int pm, int col0, u16* abT, u16* h, const u16* __restrict__ b1,
                 gu64* sy, u64 tag, int ct, u64 stv, const u32x4& rpt, unsigned vpt ST_ARG) {
  constexpr int g = NB == 6 ? 0 : 1;
  const int fr = lane & 15, fq = lane >> 4, srow0 = pm * 256;
  constexpr unsigned BRING = NB == 6 ? K1_B0 : K1_B1, PITCH = NB * 32u;
  const unsigned sw = (unsigned)((fr >> 1) & 7);
  const unsigned aoff0 = lds0 + (unsigned)(wi * 64 + fr) * 128u + (((unsigned)fq ^ sw) << 4), aoff1 = aoff0 ^ 64u;
  unsigned boff0, boff1;
  if (TYPE == 0) { boff0 = lds0 + BRING + (unsigned)fr * 128u + (((unsigned)fq ^ sw) << 4); boff1 = boff0 ^ 64u; }
  else {
    const unsigned q = (lane & 15) >> 2, p = lane & 3, g4 = lane >> 4;
    const unsigned base = lds0 + BRING + (q + 4u * (g4 & 1u) + 16u * (g4 >> 1)) * PITCH + p * 8u;
    if (NB == 6) { boff0 = base + (g4 & 1u) * 32u; boff1 = base - (g4 & 1u) * 32u; } else { boff0 = base; boff1 = base; }
  }
  f32x4 acc[4][NB];
  const unsigned scr = lds0 + KM_SCR + (unsigned)(g * 4 + wi) * 256u;
  f32x4 csa[2] = {(f32x4){0.f, 0.f, 0.f, 0.f}, (f32x4){0.f, 0.f, 0.f, 0.f}};
  bf16x8 ones = __builtin_bit_cast(bf16x8, (u32x4){0x3f803f80u, 0x3f803f80u, 0x3f803f80u, 0x3f803f80u});
  asm volatile("" : "+v"(ones), "+v"(csa[0]), "+v"(csa[1]));
  {
    const K1Step s1 = k1_prep(c, 1, 32768u, g);
    k1_dma<4>(c, s1); k1_dma<5>(c, s1); k1_dma<6>(c, s1);
    k1_dma<0>(c, s1); k1_dma<1>(c, s1); k1_dma<2>(c, s1); k1_dma<3>(c, s1);
  }
  if (lane < 2) st_sc1(sy + SY_ST + (srow0 + ct * 8 + g * 4 + wi) * 2 + lane, stv);
  asm volatile("" ::: "memory");
  if (TYPE == 1) touch(mksrd(b1 + col0, NB * 32u), (unsigned)lane * 4u, 0u, scr);
  unsigned cur = 0;
  K1Step st = k1_prep(c, 2, 65536u, 1 + g);
  unsigned pa0 = aoff0, pa1 = aoff1, pb0 = boff0, pb1 = boff1;
#pragma unroll
  for (int m = 0; m < 4; ++m)
#pragma unroll
    for (int n = 0; n < NB; ++n) { acc[m][n] = (f32x4){0.f, 0.f, 0.f, 0.f}; asm volatile("" : "+v"(acc[m][n])); }
  WAIT_V(8 + TYPE);
  BAR();
  if (g == 1) {
    if (wi == 0 && lane == 0) st_sc1(sy + SY_LN + pm * 32 + ct, tag);
    BAR();
  }
  const u32x4 rst = mksrd((const void*)(size_t)(sy + SY_ST + (srow0 + 64 * wi) * 2), 1024u);
  const unsigned stl = lds0 + KM_ST + (unsigned)(g * 4 + wi) * 1024u, csl = lds0 + KM_CS + g * 768u;
  SCHED();
  ST(1); STC(0);
  auto iter = [&](auto modec, int t) {
    constexpr int MODE = decltype(modec)::value;
    bf16x8 af[4][2], bfr[NB][2];
    constexpr bool va = MODE == 0, vb = MODE == 0 || (MODE == 1 && g == 0);
    auto rdB = [&](int n) {
      if (TYPE == 0) { bfr[n][0] = ldsd128(pb0 + n * 2048); bfr[n][1] = ldsd128(pb1 + n * 2048); }
      else {
        const unsigned pb = (n & 1) ? pb1 : pb0;
#pragma unroll
        for (int ks = 0; ks < 2; ++ks) bfr[n][ks] = ldstr(pb + n * 32 + (32 * ks) * PITCH, pb + n * 32 + (32 * ks + 8) * PITCH);
      }
    };
    auto rdA = [&](int m) { af[m][0] = ldsd128(pa0 + m * 2048); af[m][1] = ldsd128(pa1 + m * 2048); };
    if (vb) k1_dma<4>(c, st);
    rdA(0); rdA(1);
    if (vb) k1_dma<5>(c, st);
    rdA(2); rdA(3);
    if (vb) k1_dma<6>(c, st);
    rdB(0);
    if (va) k1_dma<0>(c, st);
    rdB(1);
    if (va) k1_dma<1>(c, st);
    rdB(2);
    if (va) k1_dma<2>(c, st);
    rdB(3);
    if (va) k1_dma<3>(c, st);
    rdB(4);
    if (NB == 6) rdB(5);
    auto cs_all = [&]() { cs_mma<NB>(csa, ones, wi, bfr); };
    if (MODE == 0) WAIT_V(7);
    else if (MODE == 1) {
      dma_m0(rst, (unsigned)lane * 16u, 0u, stl);
      if (PRET && g == 1) { touch(rpt, vpt, 0u, csl + 512u); WAIT_V(2); }
      else if (vb) WAIT_V(4); else WAIT_V(1);
    } else {
      if (PRET && g == 1) WAIT_V(1); else WAIT_V(0);
      WAIT_L(0);
      cs_all();
      asm volatile("s_nop 7\n\ts_nop 3" : "+v"(csa[0]), "+v"(csa[1]));
      if (fr == 0) { *(LAS f32x4*)(size_t)(csl + (unsigned)(2 * wi) * 64u + (unsigned)fq * 16u) = csa[0]; *(LAS f32x4*)(size_t)(csl + (unsigned)(2 * wi + 1) * 64u + (unsigned)fq * 16u) = csa[1]; }
    }
    WAIT_L(0);
    __builtin_amdgcn_s_setprio(1);
    SCHED();
    if (MODE != 2) cs_mma_bar<NB>(csa, ones, wi, bfr); else BAR();
    SCHED();
#pragma unroll
    for (int ks = 0; ks < 2; ++ks)
#pragma unroll
      for (int n = 0; n < NB; ++n)
#pragma unroll
        for (int m = 0; m < 4; ++m) {
          acc[m][n] = TYPE == 0 ? MFMA(af[m][ks], bfr[n][ks], acc[m][n]) : MFMA(bfr[n][ks], af[m][ks], acc[m][n]);
        }
    if (MODE != 2) {
      st = k1_prep(c, t + 3, cur, t + 2 + g);
      cur = cur == 65536u ? 0u : cur + 32768u;
      const unsigned bs = (t & 1) ? 0u : K1_BSLOT;
      pa0 = aoff0 + cur; pa1 = aoff1 + cur; pb0 = boff0 + bs; pb1 = boff1 + bs;
      mfma_shadow<8 * NB>();
      asm volatile("" : "+s"(st.sa[0]), "+s"(st.sa[1]), "+s"(st.sa[2]), "+s"(st.sa[3]), "+s"(st.kb), "+s"(st.ma), "+s"(st.mb), "+s"(cur), "+v"(pa0), "+v"(pa1), "+v"(pb0), "+v"(pb1));
    }
    __builtin_amdgcn_s_setprio(0);
    SCHED();
    if (!(g == 1 && MODE == 2)) BAR();
    SCHED();
  };
  for (int t = 0; t < K1_NT - 2; ++t) iter(IC<0>{}, t);
  iter(IC<1>{}, K1_NT - 2);
  iter(IC<2>{}, K1_NT - 1);
  ST(2); STC(1);
#pragma unroll 1
  for (unsigned spins = 0; spins < (1u << 20); ++spins) {
    const u32x4 gq = *(const LAS u32x4*)(size_t)(stl + (unsigned)lane * 16u);
    if (__builtin_amdgcn_ballot_w64(gq[1] != (unsigned)tag || gq[3] != (unsigned)tag) == 0ull) break;
    __builtin_amdgcn_s_sleep(2);
    WAIT_L(0);
    dma_m0(rst, (unsigned)lane * 16u, 0u, stl); WAIT_V(0);
  }
  if (TYPE == 0) {
    const __amdgpu_buffer_rsrc_t ro = mkrsrc(abT + (size_t)(srow0 / 8 + 8 * wi) * (KBS / 2) + col0 * 8, 0x7fffffffu);
    const unsigned vo = (unsigned)fq * KBS + (unsigned)fr * 16u;
    float cs[NB];
#pragma unroll
    for (int n = 0; n < NB; ++n) { constexpr int sl[6] = {0, 1, 3, 4, 5, 7}; cs[n] = *(const LAS float*)(size_t)(csl + sl[n] * 64 + fr * 4); if (n == 1 || n == 4) cs[n] += *(const LAS float*)(size_t)(csl + (sl[n] + 1) * 64 + fr * 4); }
#pragma unroll
    for (int j = 0; j < 2; ++j) {
      float mu[8], rs[8];
#pragma unroll
      for (int r = 0; r < 8; ++r) { const u32x4 gq = *(const LAS u32x4*)(size_t)(stl + (unsigned)(32 * j + 8 * fq + r) * 16u); mu[r] = __uint_as_float(gq[0]); rs[r] = __uint_as_float(gq[2]); }
#pragma unroll
      for (int n = 0; n < NB; ++n) {
        float v[8];
#pragma unroll
        for (int r = 0; r < 8; ++r) v[r] = __builtin_fmaf(-mu[r], cs[n], acc[2 * j + (r >> 2)][n][r & 3]) * rs[r];
        u32x4 w; w[0] = cvtpk(v[0], v[1]); w[1] = cvtpk(v[2], v[3]); w[2] = cvtpk(v[4], v[5]); w[3] = cvtpk(v[6], v[7]);
        __builtin_amdgcn_raw_buffer_store_b128(w, ro, vo + j * 4 * KBS + n * 256, 0, 16);
      }
    }
  } else {
    const __amdgpu_buffer_rsrc_t ro = mkrsrc(h + (size_t)(col0 / 8) * (KBS / 2) + (srow0 + 64 * wi) * 8, 0x7fffffffu);
    const unsigned rowl = perm ? (unsigned)(8 * (fr >> 2) + 4 * (fq & 1) + (fr & 3)) : (unsigned)(16 * (fq & 1) + fr);
    const unsigned vo = (unsigned)(fq >> 1) * KBS + rowl * 16u;
    float rs[4], rm[4];
#pragma unroll
    for (int m = 0; m < 4; ++m) {
      const unsigned row = perm ? (unsigned)(32 * (m >> 1) + 8 * (fr >> 2) + 4 * (m & 1) + (fr & 3)) : (unsigned)(16 * m + fr);
      const u32x4 gq = *(const LAS u32x4*)(size_t)(stl + row * 16u); rs[m] = __uint_as_float(gq[2]); rm[m] = -__uint_as_float(gq[0]) * rs[m];
    }
    auto gelu2 = [&](f32x2 a, f32x2 cs, f32x2 bv, float r_, float m_) -> unsigned {
      constexpr float c1 = -2.0f * 0.7978845608028654f * 1.4426950408889634f, c3 = c1 * 0.044715f;
      const f32x2 v = __builtin_elementwise_fma(a, (f32x2){r_, r_}, __builtin_elementwise_fma(cs, (f32x2){m_, m_}, bv));
      const f32x2 u = v * __builtin_elementwise_fma(v * v, (f32x2){c3, c3}, (f32x2){c1, c1});
      f32x2 e; e[0] = __builtin_amdgcn_exp2f(u[0]); e[1] = __builtin_amdgcn_exp2f(u[1]);
      const f32x2 d = e + 1.0f;
      f32x2 r; r[0] = __builtin_amdgcn_rcpf(d[0]); r[1] = __builtin_amdgcn_rcpf(d[1]);
      return cvtpk2(v * r);
    };
#pragma unroll
    for (int n = 0; n < NB; ++n) {
      constexpr int sl[6] = {0, 1, 3, 4, 5, 7};
      f32x4 cs = *(const LAS f32x4*)(size_t)(csl + sl[n] * 64 + fq * 16); if (n == 1 || n == 4) cs += *(const LAS f32x4*)(size_t)(csl + (sl[n] + 1) * 64 + fq * 16);
      const u32x2 bb = *(const LAS u32x2*)(size_t)(scr + (unsigned)fq * 8u + n * 32);
      const f32x2 c01 = {cs[0], cs[1]}, c23 = {cs[2], cs[3]}, b01 = {bflo(bb[0]), bfhi(bb[0])}, b23 = {bflo(bb[1]), bfhi(bb[1])};
#pragma unroll
      for (int j = 0; j < 2; ++j) {
        const f32x4 a = acc[2 * j][n], b = acc[2 * j + 1][n];
        const unsigned a0 = gelu2((f32x2){a[0], a[1]}, c01, b01, rs[2 * j], rm[2 * j]), a1 = gelu2((f32x2){a[2], a[3]}, c23, b23, rs[2 * j], rm[2 * j]);
        const unsigned b0 = gelu2((f32x2){b[0], b[1]}, c01, b01, rs[2 * j + 1], rm[2 * j + 1]), b1_ = gelu2((f32x2){b[2], b[3]}, c23, b23, rs[2 * j + 1], rm[2 * j + 1]);
        auto r0 = __builtin_amdgcn_permlane16_swap(a0, b0, false, false);
        auto r1 = __builtin_amdgcn_permlane16_swap(a1, b1_, false, false);
        u32x4 w; w[0] = r0[0]; w[1] = r1[0]; w[2] = r0[1]; w[3] = r1[1];
        __builtin_amdgcn_raw_buffer_store_b128(w, ro, vo + n * 2 * KBS + j * 512, 0, 16);
      }
    }
  }
  WAIT_V(0);
  if (lane == 0) {
    const unsigned old = __hip_atomic_fetch_add((LAS unsigned*)(size_t)(lds0 + KM_CNT + g * 4), 1u, __ATOMIC_RELAXED, __HIP_MEMORY_SCOPE_WORKGROUP);
    if (old == 3u) {
      const int hid = TYPE == 0 ? (g == 0 ? ct : 13 + ct) : (g == 0 ? ct - 13 : 19 + ct - 10);
      st_sc1(TYPE == 0 ? sy + SY_D + hid * 8 + pm : sy + SY_M + pm * 48 + hid, tag);
    }
  }
  ST(3);
}

template <bool TR>
DI void k2_loop(unsigned lds0, const K2Issue& c, int g, int wi, int lane, f32x4 (&acc)[4][4], bool early, const u32x4& rxx, const u32x4& rb2, unsigned& xaddr ST_ARG) {
  const int fr = lane & 15, fq = lane >> 4, wmi = wi >> 1, wn = wi & 1;
  constexpr int NT = TR ? F / 128 : S / 64;
  const unsigned sw = (unsigned)((fr >> 1) & 7);
  if (TR) {
    if (g == 0) { k2_sm(c, 0, K2_R0); k2_sm(c, 1, K2_R1); k2_touch(c); }
    else { k2_sm(c, 0, K2_R0); k2_big(c, 0, K2_R0, K2_Q0); k2_sm(c, 1, K2_R1); k2_big(c, 1, K2_R1, K2_Q1); k2_sm(c, 2, K2_R2); }
  } else {
    if (early) {
      if (g == 0) { k2_big(c, 0, K2_R0, K2_Q0); k2_sm(c, 0, K2_R0); k2_big(c, 1, K2_R1, K2_Q1); k2_sm(c, 1, K2_R1); if (!PRET) k2_touch<LEADT>(c); }
      else { k2_sm(c, 0, K2_R0); k2_big(c, 0, K2_R0, K2_Q0); k2_sm(c, 1, K2_R1); k2_big(c, 1, K2_R1, K2_Q1); k2_sm(c, 2, K2_R2); k2_touch(c); }
    } else {
      if (g == 0) { k2_big(c, 0, K2_R0, K2_Q0); k2_big(c, 1, K2_R1, K2_Q1); }
      else k2_touch(c);
    }
  }
  const unsigned abase = lds0 + (TR ? (g == 0 ? 16384u : 0u) : (g == 0 ? 0u : 16384u));
  const bool aIsQ = TR && g == 1;
  const unsigned aoff0 = TR ? abase + (unsigned)fq * 2048u + (unsigned)(wmi * 64 + fr) * 16u : abase + (unsigned)(wmi * 64 + fr) * 128u + (((unsigned)fq ^ sw) << 4);
  const unsigned aoff1 = aoff0 ^ 64u;
  constexpr int AM = TR ? 256 : 2048;
  unsigned boff[4];
  if (TR) {
    const unsigned q = (lane & 15) >> 2, p = lane & 3, g4 = lane >> 4, s8 = q + 4u * (g4 & 1u);
#pragma unroll
    for (int n = 0; n < 4; ++n) boff[n] = lds0 + (s8 + 16u * (g4 >> 1)) * 256u + ((((unsigned)(wn * 4 + n)) ^ s8) << 5) + p * 8u;
  } else {
    boff[0] = lds0 + (unsigned)fq * 2048u + (unsigned)(wn * 64 + fr) * 16u; boff[1] = boff[2] = boff[3] = 0;
  }
  unsigned curR = K2_R0, curQ = K2_Q0;
  u32x4 rb, rs; unsigned mb, ms, pa0, pa1, pb[4];
  auto prep = [&](int t) {
    const unsigned prvR = curR == 0u ? 65536u : curR - 32768u, prvQ = curQ == K2_Q0 ? K2_Q2 : curQ - 16384u;
    const int tsm = t + 2 + g;
    rb = mksrd(c.bigbase + k2_phys(c, t + 2) * c.bigstep, t + 2 < NT ? 0x7fffffffu : 0u);
    rs = mksrd(c.Abase + k2_phys(c, tsm) * c.astep, tsm < NT ? 0x7fffffffu : 0u);
    mb = c.mBig + (c.bigIsQ ? prvQ : prvR); ms = c.mSm + (g == 0 ? prvR : curR);
    const unsigned sa = aIsQ ? curQ : curR, sb = TR ? curR : curQ;
    pa0 = aoff0 + sa; pa1 = aoff1 + sa;
#pragma unroll
    for (int n = 0; n < (TR ? 4 : 1); ++n) pb[n] = boff[n] + sb;
  };
  prep(0);
  if (TR) {
    if (g == 0) { WAIT_V(2 + NPT); BAR(); } else { WAIT_V(12); BAR(); WAIT_V(6); BAR(); }
  } else {
    if (g == 0) { if (early) WAIT_V(PRET ? 6 : 6 + LEADT - 2); else WAIT_V(4); BAR(); } else { WAIT_V(12 + NPT); BAR(); WAIT_V(6 + NPT); BAR(); }
  }
  SCHED();
  ST(5); STC(2);
  auto rdA = [&](int m, int ks) -> bf16x8 { return TR ? ldsd128(pa0 + m * AM + ks * 8192) : ldsd128((ks ? pa1 : pa0) + m * AM); };
  auto rdB = [&](int n, int ks) -> bf16x8 { return TR ? ldstr(pb[n] + ks * 8192, pb[n] + ks * 8192 + 2048) : ldsd128(pb[0] + n * 256 + ks * 8192); };
  auto body = [&](auto lastc, int t) {
    constexpr bool LAST = decltype(lastc)::value;
    bf16x8 af[4][2], bfr[4][2];
    if (!LAST) dma_m0(rb, c.voffBig[0], c.sBig[0], mb);
    af[0][0] = rdA(0, 0); af[0][1] = rdA(0, 1); bfr[0][0] = rdB(0, 0);
    if (!LAST) dma_off<1024>(rb, c.voffBig[1], c.sBig[1]);
    af[1][0] = rdA(1, 0); af[1][1] = rdA(1, 1); bfr[1][0] = rdB(1, 0);
    if (!LAST) dma_off<2048>(rb, c.voffBig[0], c.sBig[2]);
    af[2][0] = rdA(2, 0); af[2][1] = rdA(2, 1); bfr[2][0] = rdB(2, 0);
    if (!LAST) dma_off<3072>(rb, c.voffBig[1], c.sBig[3]);
    af[3][0] = rdA(3, 0); af[3][1] = rdA(3, 1);
    if (!LAST) dma_m0(rs, c.voffA[0], c.sSm[0], ms);
    bfr[3][0] = rdB(3, 0); bfr[0][1] = rdB(0, 1); bfr[1][1] = rdB(1, 1);
    if (!LAST) dma_off<1024>(rs, c.voffA[1], c.sSm[1]);
    bfr[2][1] = rdB(2, 1); bfr[3][1] = rdB(3, 1);
    WAIT_L(0); BAR(); SCHED();
    __builtin_amdgcn_s_setprio(1);
#pragma unroll
    for (int ks = 0; ks < 2; ++ks)
#pragma unroll
      for (int n = 0; n < 4; ++n)
#pragma unroll
        for (int m = 0; m < 4; ++m) acc[m][n] = MFMA(bfr[n][ks], af[m][ks], acc[m][n]);
    if (!LAST) {
    curR = curR == 65536u ? 0u : curR + 32768u; curQ = curQ == K2_Q2 ? K2_Q0 : curQ + 16384u;
    prep(t + 1);
    mfma_shadow<32>();
    if (TR) asm volatile("" : "+s"(rb), "+s"(rs), "+s"(mb), "+s"(ms), "+s"(curR), "+s"(curQ), "+v"(pa0), "+v"(pb[0]), "+v"(pb[1]), "+v"(pb[2]), "+v"(pb[3]));
    else asm volatile("" : "+s"(rb), "+s"(rs), "+s"(mb), "+s"(ms), "+s"(curR), "+s"(curQ), "+v"(pa0), "+v"(pa1), "+v"(pb[0]));
    }
    __builtin_amdgcn_s_setprio(0);
    SCHED();
    if (!LAST) { WAIT_V(6); BAR(); } else if (g == 0) BAR();
    SCHED();
  };
  for (int t = 0; t < NT - 1; ++t) body(IC<0>{}, t);
  xaddr = c.mBig + (NT % 3 == 1 ? (c.bigIsQ ? K2_Q1 : K2_R1) : (c.bigIsQ ? K2_Q2 : K2_R2));
  static_assert(NT % 3 != 0, "slot of K-tile NT");
  dma_m0(rxx, (unsigned)(lane >> 3) * (D * 2u) + (unsigned)(lane & 7) * 16u, 0u, xaddr);
  dma_off<1024>(rxx, (unsigned)(lane >> 3) * (D * 2u) + (unsigned)(lane & 7) * 16u, 8u * D * 2u - 1024u);
  touch(rb2, (unsigned)lane * 4u, 0u, c.mScr);
  body(IC<1>{}, NT - 1);
}

__global__ __launch_bounds__(512, 2) void k_main(u16* xn, const u16* __restrict__ Dr, const u16* __restrict__ Di, const u16* __restrict__ w1,
                                                 const u16* __restrict__ b1, u16* abT, u16* h, const u16* __restrict__ Cr, const u16* __restrict__ Ci,
                                                 const u16* __restrict__ w2, const u16* __restrict__ x, const u16* __restrict__ b2, u16* __restrict__ out,
                                                 unsigned char* xbuf, unsigned long long* sync DBG_ARG) {
  ST_DECL; ST(0);
  extern __shared__ __attribute__((aligned(16))) unsigned char smem[];
  const unsigned lds0 = (unsigned)(size_t)(LAS unsigned char*)smem;
  asm volatile("" : "+s"(xn), "+s"(Dr), "+s"(Di), "+s"(w1), "+s"(b1), "+s"(x), "+s"(sync));
  const int bid = blockIdx.x, j = bid >> 3;
  int xcd = XCDL(bid); asm volatile("" : "+s"(xcd));
  const int pm = (xcd & 1) * 4 + (j & 3), ct = CTMAP(xcd, j);
  const int role = xcd >> 1, pn = j >> 2, tile = pm * 8 + pn;
  const int tid = threadIdx.x, wid = (int)rfl(tid >> 6), lane = tid & 63, g = wid >> 2, wi = wid & 3;
  gu64* sy = (gu64*)sync;
  const int lnrow = pm * 256 + ct * 8 + wid;
  const unsigned xl = lds0 + 65536u + (unsigned)wid * 2048u;
  { const u32x4 rx = mksrd(x + (size_t)lnrow * D, D * 2u); dma_m0(rx, (unsigned)lane * 16u, 0u, xl); dma_off<1024>(rx, (unsigned)lane * 16u, 0u); }
  if (CODEW && wid == 1) {
    unsigned long long pc; asm volatile("s_getpc_b64 %0" : "=s"(pc));
    touch(mksrd((const void*)(pc & ~127ull), CODEW), (unsigned)(j * 16 + (lane & 15)) * 128u, 0u, lds0 + KM_SCR + 256u);
  }
  u64 stv;
  u64 tag;
  if (tid < 2) *(LAS unsigned*)(size_t)(lds0 + KM_CNT + tid * 4) = 0u;
  const int type0 = ct < 13 ? 0 : 1, col00 = ct < 13 ? 96 * ct : 96 * (ct - 13);
  const int type1 = ct < 10 ? 0 : 1, col01 = ct < 10 ? 1248 + 80 * ct : 1824 + 80 * (ct - 10);
  const bool perm = type0 == 0;
  K1Issue c;
  c.ra = mksrd(x + (size_t)pm * 256 * D, 0x7fffffffu);
  c.leadmask = 1 << (((j & 3) + (j >> 2)) & 3);
  const unsigned i = (unsigned)lane;
#pragma unroll
  for (int par = 0; par < 2; ++par) c.voffA[par] = (perm ? 8u * (i >> 5) + ((i >> 3) & 3u) : (i >> 3)) * (D * 2u) + (((i & 7u) ^ (i >> 4) ^ ((unsigned)par << 2)) << 4);
#pragma unroll
  for (int q = 0; q < 4; ++q) { const unsigned p = (unsigned)(g * 16 + wi * 4 + q); c.sA[q] = (perm ? 16u * (p & 1u) + 4u * ((p >> 1) & 1u) + 32u * (p >> 2) : 8u * p) * (D * 2u) - q * 1024u; }
  c.mA = lds0 + (unsigned)(g * 16 + wi * 4) * 1024u;
  { const unsigned ka = k1_phys(c, 0) * 128u;
    dma_m0(c.ra, c.voffA[0], c.sA[0] + ka, c.mA); dma_off<1024>(c.ra, c.voffA[1], c.sA[1] + ka); dma_off<2048>(c.ra, c.voffA[0], c.sA[2] + ka); dma_off<3072>(c.ra, c.voffA[1], c.sA[3] + ka); }
  SCHED();
  const int otype = g == 0 ? type1 : type0, ocol = g == 0 ? col01 : col00;
  const unsigned cpr = g == 0 ? 10u : 12u, oring = g == 0 ? K1_B1 : K1_B0;
  c.bstep = otype == 0 ? 128u : 64u * F * 2u;
  c.mB = lds0 + oring + (unsigned)wi * 3072u;
#pragma unroll
  for (int q = 0; q < 3; ++q) {
    const unsigned p = (unsigned)(3 * wi + q), nr = p < cpr ? 0x7fffffffu : 0u;
    if (otype == 0) {
      int e0 = ocol + 8 * (int)p; if (e0 > 2040) e0 = 2040;
      c.rb[q] = mksrd((const unsigned char*)(e0 < 1024 ? Dr + (size_t)e0 * D : Di + (size_t)(e0 - 1024) * D) - q * 1024, nr);
      c.voffB[q] = (i >> 3) * (D * 2u) + (((i & 7u) ^ (i >> 4) ^ ((p & 1u) << 2)) << 4);
    } else {
      const unsigned ci = 64u * p + i, rho = ci / cpr, pos = ci - rho * cpr, sp = pos >> 1;
      const unsigned seg = cpr == 12u ? sp ^ ((rho >> 2) & 1u) : sp;
      const unsigned k = (rho & 3u) + 8u * ((rho >> 2) & 1u) + 4u * ((rho >> 3) & 1u) + (rho & 48u);
      c.rb[q] = mksrd((const unsigned char*)(w1 + ocol) - q * 1024, nr);
      c.voffB[q] = k * (F * 2u) + seg * 32u + (pos & 1u) * 16u;
    }
  }
  const bool tRole = role < 2;
  const K2Args a2 = {Cr, Ci, abT, h, w2};
  const u32x4 rpt = mksrd(tRole ? (role ? Ci : Cr) + (size_t)pm * 256 * S : w2 + (size_t)(role & 1) * (F / 2) * D + pn * 128, 0x7fffffffu);
  const unsigned vpt = tRole ? (unsigned)(pn * 32 + wi * 8 + (lane & 7)) * (S * 2u) + (unsigned)(lane >> 3) * 128u
                             : (unsigned)((j & 3) * 16 + wi * 4 + ((lane & 7) >> 1)) * (D * 2u) + (unsigned)(lane & 1) * 128u + (unsigned)(lane >> 3) * (64u * D * 2u);
  {
    const K1Step s0 = k1_prep(c, 0, 0u, 0);
    if (g == 1) { k1_dma<4>(c, s0); k1_dma<5>(c, s0); k1_dma<6>(c, s0); }
    asm volatile("s_load_dwordx2 %0, %1, 0x0\n\ts_waitcnt lgkmcnt(0)" : "=s"(tag) : "s"(sync + SY_EPOCH) : "memory");
    tag += 1ull;
    if (g == 1) WAIT_V(7); else WAIT_V(4);
    const u32x4 v0 = *(const LAS u32x4*)(size_t)(xl + lane * 16u), v1 = *(const LAS u32x4*)(size_t)(xl + 1024u + lane * 16u);
    float f[16];
#pragma unroll
    for (int q = 0; q < 4; ++q) { f[2 * q] = bflo(v0[q]); f[2 * q + 1] = bfhi(v0[q]); f[8 + 2 * q] = bflo(v1[q]); f[9 + 2 * q] = bfhi(v1[q]); }
    float sm = 0.f;
#pragma unroll
    for (int q = 0; q < 16; ++q) sm += f[q];
    asm volatile("" : "+v"(sm)); STL(0);
    sm = wave_sum(sm);
    const float mean = sm * (1.0f / D);
    float sq = 0.f;
#pragma unroll
    for (int q = 0; q < 16; ++q) { f[q] -= mean; sq += f[q] * f[q]; }
    sq = wave_sum(sq);
    const float rs = 1.0f / sqrtf(sq * (1.0f / D) + 1e-5f);
    stv = (tag << 32) | (u64)__float_as_uint(lane ? rs : mean);
    STL(1);
  }
  if (g == 0) {
    if (type0 == 0) k1_group<0, 6>(lds0, c, a2, tRole, perm, wi, lane, pm, col00, abT, h, b1, sy, tag, ct, stv, rpt, vpt ST_PASS);
    else k1_group<1, 6>(lds0, c, a2, tRole, perm, wi, lane, pm, col00, abT, h, b1, sy, tag, ct, stv, rpt, vpt ST_PASS);
  } else {
    if (type1 == 0) k1_group<0, 5>(lds0, c, a2, tRole, perm, wi, lane, pm, col01, abT, h, b1, sy, tag, ct, stv, rpt, vpt ST_PASS);
    else k1_group<1, 5>(lds0, c, a2, tRole, perm, wi, lane, pm, col01, abT, h, b1, sy, tag, ct, stv, rpt, vpt ST_PASS);
  }
  if (bid == 0 && wid == 1) {
    gu64* pq = sy + SY_LN + lane;
#pragma unroll 1
    for (unsigned spins = 0; spins < (1u << 20); ++spins) {
      const u64 a0 = ld_sc1(pq), a1 = ld_sc1(pq + 64), a2_ = ld_sc1(pq + 128), a3 = ld_sc1(pq + 192);
      if (__builtin_amdgcn_ballot_w64(a0 != tag || a1 != tag || a2_ != tag || a3 != tag) == 0ull) break;
      __builtin_amdgcn_s_sleep(2);
    }
    if (lane == 0) st_sc1(sy + SY_EPOCH, tag);
  }
  const K2Issue c2 = k2_make(a2, lds0);
  auto wait_deps = [&]() {
    if (tRole) {
      const int eb = role * 8 + pn, lo = eb * 128, hi = lo + 127;
      const int hlo = lo < 1248 ? lo / 96 : 13 + (lo - 1248) / 80, hhi = hi < 1248 ? hi / 96 : 13 + (hi - 1248) / 80;
      wait_words(sy + SY_D + hlo * 8, (hhi - hlo + 1) * 8, tag, lane);
    } else wait_words(sy + SY_M + pm * 48 + (role == 2 ? 0 : 18), role == 2 ? 19 : 23, tag, lane);
  };
  const bool early = EARLYT && tRole && type1 == 1;
  if (early && wid == 0) wait_deps();
  BAR();
  asm volatile("" ::: "memory");
  if (!early) {
    k2_pre(c2, g, tRole, 0); k2_pre(c2, g, tRole, 1);
    if (tRole) { if (g == 0) { if (!PRET) k2_touch<LEADT>(c2); } else k2_sm(c2, 2, K2_R2); }
    else if (g == 1 && !PRET) k2_touch(c2);
    if (wid == 0) wait_deps();
    BAR();
    asm volatile("" ::: "memory");
  }
  ST(4);
  f32x4 acc[4][4];
#pragma unroll
  for (int m = 0; m < 4; ++m)
#pragma unroll
    for (int n = 0; n < 4; ++n) acc[m][n] = (f32x4){0.f, 0.f, 0.f, 0.f};
  const u32x4 rxx = mksrd(x + (size_t)(pm * 256 + g * 128 + (wi >> 1) * 64 + role * 16) * D + pn * 128 + (wi & 1) * 64, 16u * D * 2u);
  const u32x4 rb2 = mksrd(b2 + pn * 128 + (wi & 1) * 64, 128u);
  unsigned xaddr;
  if (tRole) k2_loop<false>(lds0, c2, g, wi, lane, acc, early, rxx, rb2, xaddr ST_PASS);
  else k2_loop<true>(lds0, c2, g, wi, lane, acc, false, rxx, rb2, xaddr ST_PASS);
  ST(6); STC(3);
  const __amdgpu_buffer_rsrc_t xr = mkrsrc(xbuf + (size_t)tile * (4 * 8 * 8 * 1024), 4u * 8u * 8u * 1024u);
  const __amdgpu_buffer_rsrc_t xw = mkrsrc(xbuf + (size_t)tile * (4 * 8 * 8 * 1024) + (role * 8 + wid) * 8192, 8192u);
  const int fr = lane & 15, fq = lane >> 4, wmi = wi >> 1, wn = wi & 1;
  const int trow = pm * 256 + g * 128 + wmi * 64 + role * 16 + fr, ecol = pn * 128 + wn * 64 + 4 * fq;
#pragma unroll
  for (int m = 0; m < 4; ++m)
    if (m != role) {
#pragma unroll
      for (int pr = 0; pr < 2; ++pr) {
        const f32x4 a = acc[m][2 * pr], b = acc[m][2 * pr + 1];
        u32x4 w; w[0] = cvtpk(a[0], a[1]); w[1] = cvtpk(a[2], a[3]); w[2] = cvtpk(b[0], b[1]); w[3] = cvtpk(b[2], b[3]);
        __builtin_amdgcn_raw_buffer_store_b128(w, xw, lane * 16 + (m * 2 + pr) * 1024, 0, 16);
      }
    }
  gu64* fl = sy + SY_X + (tile * 8 + wid) * 8;
  u64 f0 = ld_sc1(fl + (lane & 3));
  f32x4 own[4];
#pragma unroll
  for (int n = 0; n < 4; ++n) own[n] = role == 0 ? acc[0][n] : role == 1 ? acc[1][n] : role == 2 ? acc[2][n] : acc[3][n];
  STX(0);
  WAIT_V(0);
  STX(1);
  if (lane == 0) st_sc1(fl + role, tag);
  asm volatile("" ::: "memory");
  u32x2 xv[4], bv[4];
#pragma unroll
  for (int n = 0; n < 4; ++n) { xv[n] = *(const LAS u32x2*)(size_t)(xaddr + (unsigned)(fr * 128 + fq * 8 + n * 32)); bv[n] = *(const LAS u32x2*)(size_t)(c2.mScr + (unsigned)(fq * 8 + n * 32)); }
  u32x4 pk[4][2];
#pragma unroll
  for (int r = 0; r < 4; ++r) { pk[r][0] = (u32x4){0u, 0u, 0u, 0u}; pk[r][1] = (u32x4){0u, 0u, 0u, 0u}; }
  unsigned have = 1u << role;
#pragma unroll 1
  for (unsigned spins = 0;;) {
    const unsigned rdy = (unsigned)__builtin_amdgcn_ballot_w64(f0 == tag) & 15u;
    const unsigned nw = rdy & ~have;
    asm volatile("" ::: "memory");
    have |= nw;
    if (have != 15u) f0 = ld_sc1(fl + (lane & 3));
#pragma unroll
    for (int r = 0; r < 4; ++r)
      if (nw & (1u << r)) {
#pragma unroll
        for (int pr = 0; pr < 2; ++pr) pk[r][pr] = __builtin_bit_cast(u32x4, __builtin_amdgcn_raw_buffer_load_b128(xr, lane * 16, ((r * 8 + wid) * 8 + role * 2 + pr) * 1024, 16));
      }
    if (have == 15u || ++spins > (1u << 20)) break;
    if (!nw) __builtin_amdgcn_s_sleep(XSLEEP);
  }
  STX(2);
  WAIT_V(0);
  STX(3);
#pragma unroll
  for (int n = 0; n < 4; ++n) {
    const float xb[4] = {bflo(xv[n][0]), bfhi(xv[n][0]), bflo(xv[n][1]), bfhi(xv[n][1])};
    const float bb[4] = {bflo(bv[n][0]), bfhi(bv[n][0]), bflo(bv[n][1]), bfhi(bv[n][1])};
    float pv[4][4];
#pragma unroll
    for (int r = 0; r < 4; ++r) {
      const unsigned w0 = pk[r][n >> 1][(n & 1) * 2], w1_ = pk[r][n >> 1][(n & 1) * 2 + 1];
      pv[r][0] = r == role ? own[n][0] : bflo(w0); pv[r][1] = r == role ? own[n][1] : bfhi(w0);
      pv[r][2] = r == role ? own[n][2] : bflo(w1_); pv[r][3] = r == role ? own[n][3] : bfhi(w1_);
    }
    float o[4];
#pragma unroll
    for (int e = 0; e < 4; ++e) {
      const float y = pv[0][e] - pv[1][e], z = pv[2][e] + pv[3][e] + bb[e];
      const float gz = z * __builtin_amdgcn_rcpf(1.0f + __builtin_amdgcn_exp2f(-1.4426950408889634f * z));
      o[e] = xb[e] + y * gz;
    }
    u32x2 w; w[0] = cvtpk(o[0], o[1]); w[1] = cvtpk(o[2], o[3]);
    __builtin_amdgcn_raw_buffer_store_b64(w, mkrsrc(out, (unsigned)(S * D * 2)), (unsigned)(trow * D + ecol + 16 * n) * 2u, 0, OUTAUX);
  }
}

extern "C" void kernel_launch(void* const* d_in, const int* in_sizes, int n_in, void* d_out, int out_size, void* d_ws, size_t ws_size, hipStream_t stream) {
  const u16* x = (const u16*)d_in[0]; const u16* Cr = (const u16*)d_in[1]; const u16* Ci = (const u16*)d_in[2];
  const u16* Dr = (const u16*)d_in[3]; const u16* Di = (const u16*)d_in[4];
  const u16* w1 = (const u16*)d_in[6]; const u16* b1 = (const u16*)d_in[7]; const u16* w2 = (const u16*)d_in[8]; const u16* b2 = (const u16*)d_in[9];
#define WSOFF 0
  char* ws = (char*)d_ws + (WSOFF == 1 ? ((ws_size - (56u << 20)) & ~(size_t)0xfffff) : WSOFF == 2 ? ((ws_size / 2) & ~(size_t)0xfffff) + (8u << 20) : 0);
  unsigned long long* sync = (unsigned long long*)ws;
  u16* xn = (u16*)(ws + (1 << 20));
  u16* abT = (u16*)(ws + (6 << 20));
  u16* h = (u16*)(ws + (18 << 20));
  unsigned char* xbuf = (unsigned char*)(ws + (40 << 20));
  (void)hipFuncSetAttribute((const void*)k_main, hipFuncAttributeMaxDynamicSharedMemorySize, KM_LDS);
  k_main<<<256, 512, KM_LDS, stream>>>(xn, Dr, Di, w1, b1, abT, h, Cr, Ci, w2, x, b2, (u16*)d_out, xbuf, sync DBG_PASS);
}
```

```cpp
#include <hip/hip_runtime.h>
#include <hip/hip_bf16.h>

typedef unsigned short u16;
typedef __bf16 bf16x8 __attribute__((ext_vector_type(8)));
typedef short s16x4 __attribute__((ext_vector_type(4)));
typedef short s16x8 __attribute__((ext_vector_type(8)));
typedef float f32x4 __attribute__((ext_vector_type(4)));
typedef float f32x2 __attribute__((ext_vector_type(2)));
typedef __bf16 bf16x2 __attribute__((ext_vector_type(2)));
typedef unsigned u32x4 __attribute__((ext_vector_type(4)));
typedef unsigned u32x2 __attribute__((ext_vector_type(2)));
#define LAS __attribute__((address_space(3)))
#define GAS __attribute__((address_space(1)))
#define DI __device__ __forceinline__

constexpr int S = 2048, D = 1024, F = 3584;

#define LEAD1 4
#define LEAD2 4
#define LEADT 4
#define LNKT 6
#define PREIN 0
#define XSLEEP 1
#define WARMC 0
#define SWARM 0
#define OUTAUX 0
#define XNAUX 0
#define CODEW 39936
#define PRET 1
#define XMAP 1
#define XCDL(b) (int)(((XMAP == 1 ? 0x73625140u : XMAP == 2 ? 0x76325410u : 0x76543210u) >> (((unsigned)(b) & 7u) * 4u)) & 7u)
#define XWARM 6
#define EARLYT 1
#define PREBAR 1
#define DBAL 0

#define CTMAP(xcd, j) (DBAL == 2 ? ((xcd) >> 1) + 4 * ((j) >> 2) : (DBAL && (xcd) < 4) ? 2 * ((j) >> 2) + ((xcd) >> 1) : ((xcd) >> 1) * 8 + ((j) >> 2))
#define NOACQ 1
#define SC1S " sc1"
#define WAIT_V(nn) asm volatile("s_waitcnt vmcnt(%0)" ::"n"(nn) : "memory")
#define WAIT_L(n) do { asm volatile("" ::: "memory"); __builtin_amdgcn_s_waitcnt(0xC07F); asm volatile("" ::: "memory"); } while (0)
#define SETP(n) __builtin_amdgcn_s_setprio(n)
#define WSLEEP 2
#define BAR() __builtin_amdgcn_s_barrier()
#define SCHED() __builtin_amdgcn_sched_barrier(0)

DI unsigned rfl(unsigned x) { return __builtin_amdgcn_readfirstlane(x); }
DI float bflo(unsigned v) { return __uint_as_float(v << 16); }
DI float bfhi(unsigned v) { return __uint_as_float(v & 0xffff0000u); }
DI float fadd1(float a, float b) { float r; asm("v_add_f32_e32 %0, %1, %2" : "=v"(r) : "v"(a), "v"(b)); return r; }
DI unsigned cvtpk(float lo, float hi) { f32x2 v = {lo, hi}; bf16x2 b = __builtin_convertvector(v, bf16x2); return __builtin_bit_cast(unsigned, b); }
DI unsigned cvtpk2(f32x2 v) { bf16x2 b = __builtin_convertvector(v, bf16x2); return __builtin_bit_cast(unsigned, b); }
DI __amdgpu_buffer_rsrc_t mkrsrc(const void* p, unsigned n) { return __builtin_amdgcn_make_buffer_rsrc((void*)p, (short)0, (int)n, 0x00020000); }
DI u32x4 mksrd(const void* p, unsigned n) { const unsigned long long a = (unsigned long long)p; u32x4 r; r[0] = (unsigned)a; r[1] = (unsigned)(a >> 32) & 0xffffu; r[2] = n; r[3] = 0x00020000u; return r; }
DI void dma_m0(const u32x4& r, unsigned voff, unsigned soff, unsigned m0v) {
  asm volatile("s_mov_b32 m0, %0\n\ts_nop 0\n\tbuffer_load_dwordx4 %1, %2, %3 offen" SC1S " lds" ::"s"(m0v), "v"(voff), "s"(r), "s"(soff) : "memory");
}
template <int OFF> DI void dma_off(const u32x4& r, unsigned voff, unsigned soff) {
  asm volatile("buffer_load_dwordx4 %0, %1, %2 offen offset:%3" SC1S " lds" ::"v"(voff), "s"(r), "s"(soff), "n"(OFF) : "memory");
}
DI void touch(const u32x4& r, unsigned voff, unsigned soff, unsigned m0v) {
  asm volatile("s_mov_b32 m0, %0\n\ts_nop 0\n\tbuffer_load_dword %1, %2, %3 offen" SC1S " lds" ::"s"(m0v), "v"(voff), "s"(r), "s"(soff) : "memory");
}
DI bf16x8 ldsd128(unsigned a) { return *(const LAS bf16x8*)(size_t)a; }
DI bf16x8 ldstr(unsigned a0, unsigned a1) {
  s16x4 a = __builtin_amdgcn_ds_read_tr16_b64_v4i16((LAS s16x4*)(size_t)a0);
  s16x4 b = __builtin_amdgcn_ds_read_tr16_b64_v4i16((LAS s16x4*)(size_t)a1);
  s16x8 c = __builtin_shufflevector(a, b, 0, 1, 2, 3, 4, 5, 6, 7);
  return __builtin_bit_cast(bf16x8, c);
}
DI float wave_sum(float v) {
#define DPPADD(ctrl, rm) v += __int_as_float(__builtin_amdgcn_update_dpp(0, __float_as_int(v), ctrl, rm, 0xf, false))
  DPPADD(0x111, 0xf); DPPADD(0x112, 0xf); DPPADD(0x114, 0xf); DPPADD(0x118, 0xf); DPPADD(0x142, 0xa); DPPADD(0x143, 0xc);
#undef DPPADD
  return __int_as_float(__builtin_amdgcn_readlane(__float_as_int(v), 63));
}
#define MFMA(a, b, c) __builtin_amdgcn_mfma_f32_16x16x32_bf16(a, b, c, 0, 0, 0)
template <int V> struct IC { static constexpr int value = V; };
template <int N> DI void mfma_shadow() {
#pragma unroll
  for (int i = 0; i < N; ++i) { __builtin_amdgcn_sched_group_barrier(0x8, 1, 0); __builtin_amdgcn_sched_group_barrier(0x6, 2, 0); }
}
template <int N> DI void mfma_shadow3() {
#pragma unroll
  for (int i = 0; i < N / 2; ++i) {
    __builtin_amdgcn_sched_group_barrier(0x8, 1, 0); __builtin_amdgcn_sched_group_barrier(0x2, 2, 0);
    __builtin_amdgcn_sched_group_barrier(0x8, 1, 0); __builtin_amdgcn_sched_group_barrier(0x2, 1, 0); __builtin_amdgcn_sched_group_barrier(0x4, 2, 0);
  }
}

#define ST_DECL
#define ST_ARG
#define ST_PASS
#define DBG_ARG
#define DBG_PASS
#define ST(i)
#define STC(i)
#define STL(i)
#define STX(i)
#define ST_OUT(dbg, kid, w)

constexpr unsigned K1_B0 = 98304, K1_B1 = 122880, K1_BSLOT = 12288, KM_SCR = 147456  , KM_CNT = KM_SCR + 2048  ,
                   KM_ST = KM_CNT + 64  , KM_CS = KM_ST + 8192  , KM_LDS = KM_CS + 1536;
constexpr unsigned K2_R0 = 32768, K2_R1 = 65536, K2_R2 = 0, K2_Q0 = 98304, K2_Q1 = 114688, K2_Q2 = 131072;
#define KBSV 32768
constexpr unsigned KBS = KBSV;
constexpr int K1_NT = D / 64;
constexpr int NPT = LEAD2 > 2 ? LEAD2 - 2 : 0;
typedef unsigned long long u64;
typedef GAS u64 gu64;
constexpr int SY_EPOCH = 0, SY_LN = 16  , SY_D = SY_LN + 256  , SY_M = SY_D + 192  , SY_X = SY_M + 384  , SY_ST = 8192  ;
DI u64 ld_sc1(gu64* p) { return __hip_atomic_load(p, __ATOMIC_RELAXED, __HIP_MEMORY_SCOPE_AGENT); }
DI void st_sc1(gu64* p, u64 v) { __hip_atomic_store(p, v, __ATOMIC_RELAXED, __HIP_MEMORY_SCOPE_AGENT); }
DI void wait_words(gu64* p, int n, u64 tag, int lane) {
  gu64* q = p + (lane < n ? lane : 0);
#pragma unroll 1
  for (unsigned spins = 0; __builtin_amdgcn_ballot_w64(ld_sc1(q) != tag) != 0ull && spins < (1u << 20); ++spins) __builtin_amdgcn_s_sleep(WSLEEP);
  if (!NOACQ) { __builtin_amdgcn_fence(__ATOMIC_ACQUIRE, "agent"); WAIT_V(0); }
  asm volatile("" ::: "memory");
}

struct K1Issue {
  u32x4 ra, rb[3];
  unsigned voffA[2], voffB[3];
  unsigned sA[4];
  unsigned mA, mB, bstep;
  int leadmask;
};
struct K1Step { unsigned sa[4], kb, ma, mb; };
DI unsigned k1_phys(const K1Issue& c, int t) { return (unsigned)(t + (((c.leadmask >> (t & 3)) & 1) * LEAD1)) & (unsigned)(K1_NT - 1); }
DI K1Step k1_prep(const K1Issue& c, int tA, unsigned slotA, int tB) {
  K1Step s; const unsigned ka = k1_phys(c, tA) * 128u;
#pragma unroll
  for (int q = 0; q < 4; ++q) s.sa[q] = c.sA[q] + ka;
  s.kb = k1_phys(c, tB) * c.bstep; s.ma = c.mA + slotA; s.mb = c.mB + ((tB & 1) ? K1_BSLOT : 0u);
  return s;
}
template <int Q> DI void k1_dma(const K1Issue& c, const K1Step& s) {
  if (Q == 0) dma_m0(c.ra, c.voffA[0], s.sa[0], s.ma);
  else if (Q < 4) dma_off<(Q & 3) * 1024>(c.ra, c.voffA[Q & 1], s.sa[Q & 3]);
  else if (Q == 4) dma_m0(c.rb[0], c.voffB[0], s.kb, s.mb);
  else if (Q == 5) dma_off<1024>(c.rb[1], c.voffB[1], s.kb);
  else dma_off<2048>(c.rb[2], c.voffB[2], s.kb);
}

struct K2Issue {
  const unsigned char* Abase; const unsigned char* bigbase; const unsigned char* tchbase;
  unsigned voffA[2], voffBig[2], sBig[4], sSm[2], bigstep, astep, mBig, mSm, mScr, tchvoff, tchstep;
  int NT, leadmask, lead; bool bigIsQ;
};
DI unsigned k2_phys(const K2Issue& c, int t) { int p = t + (((c.leadmask >> (t & 7)) & 1) * c.lead); p = p >= c.NT ? p - c.NT : p; return (unsigned)p; }
DI K2Issue k2_setup(unsigned lds0, bool TR, const unsigned char* Abase, const unsigned char* Bbase, int NT, int g, int wi, int lane, int pm4, int pn8, int role) {
  K2Issue c; const unsigned i = (unsigned)lane;
  c.Abase = Abase; c.NT = NT;
  if (!TR && LEADT == 8) { c.leadmask = 1 << ((2 * pm4 + pn8) & 7); c.lead = 8; }
  else { c.leadmask = 0x11 << ((pm4 + pn8) & 3); c.lead = 4; }
  c.astep = TR ? 8u * KBS : 128u;
#pragma unroll
  for (int par = 0; par < 2; ++par) c.voffA[par] = TR ? ((i + 16u * (unsigned)(role + 1)) & 63u) * 16u : (i >> 3) * (S * 2u) + (((i & 7u) ^ (i >> 4) ^ ((unsigned)par << 2)) << 4);
  auto sApiece = [&](unsigned p, unsigned half) -> unsigned { return TR ? (p >> 1) * KBS + (p & 1u) * 1024u + half * 2048u : (half * 128u + (p >> 3) * 64u + (((p >> 1) + (unsigned)role + 1u) & 3u) * 16u + (p & 1u) * 8u) * (S * 2u); };
  const unsigned idx = (unsigned)(wi * 8 + (lane & 7));
  if (g == 0) {
    c.bigbase = Bbase;
    if (TR) {
      c.bigstep = 64u * D * 2u;
#pragma unroll
      for (int par = 0; par < 2; ++par) c.voffBig[par] = (i >> 4) * (D * 2u) + ((((i & 15u) >> 1) ^ ((i >> 4) + 4u * par)) << 5) + (i & 1u) * 16u;
#pragma unroll
      for (int q = 0; q < 4; ++q) { const unsigned p = (unsigned)(4 * wi + q); c.sBig[q] = (8u * (p & 1u) + 4u * ((p >> 1) & 1u) + 16u * (p >> 2)) * (D * 2u); }
    } else {
      c.bigstep = 8u * KBS;
#pragma unroll
      for (int par = 0; par < 2; ++par) c.voffBig[par] = i * 16u;
#pragma unroll
      for (int q = 0; q < 4; ++q) { const unsigned p = (unsigned)(4 * wi + q); c.sBig[q] = (p >> 1) * KBS + (p & 1u) * 1024u; }
    }
    c.tchbase = Abase; c.tchstep = c.astep;
    c.tchvoff = TR ? (unsigned)pn8 * KBS + idx * 128u : ((unsigned)pn8 * 32u + idx) * (S * 2u);
  } else {
    c.bigbase = Abase; c.bigstep = c.astep;
#pragma unroll
    for (int par = 0; par < 2; ++par) c.voffBig[par] = c.voffA[par];
#pragma unroll
    for (int q = 0; q < 4; ++q) c.sBig[q] = sApiece((unsigned)(4 * wi + q), 1u);
    c.tchbase = Bbase;
    if (!TR) { c.tchvoff = (2u * pm4 + (idx >> 4)) * KBS + (idx & 15u) * 128u; c.tchstep = 8u * KBS; }
    else { c.tchvoff = (unsigned)(pm4 * 16 + wi * 4 + ((lane & 7) >> 1)) * (D * 2u) + (lane & 1) * 128u; c.tchstep = 64u * D * 2u; }
  }
  const unsigned psm = (unsigned)((g == 0 ? 8 : 0) + 2 * wi);
#pragma unroll
  for (int q = 0; q < 2; ++q) c.sSm[q] = sApiece(psm + q, 0u);
#pragma unroll
  for (int q = 0; q < 4; ++q) c.sBig[q] -= q * 1024u;
  c.sSm[1] -= 1024u;
  c.bigIsQ = (g == 0) != TR; c.mScr = lds0 + KM_SCR + (unsigned)(g * 4 + wi) * 256u;
  c.mBig = lds0 + ((g == 1 && !TR) ? 16384u : 0u) + (unsigned)wi * 4096u; c.mSm = lds0 + (TR ? 16384u : 0u) + psm * 1024u;
  return c;
}
DI void k2_big(const K2Issue& c, int tile, unsigned sR, unsigned sQ) {
  const u32x4 r = mksrd(c.bigbase + k2_phys(c, tile) * c.bigstep, 0x7fffffffu);
  dma_m0(r, c.voffBig[0], c.sBig[0], c.mBig + (c.bigIsQ ? sQ : sR));
  dma_off<1024>(r, c.voffBig[1], c.sBig[1]); dma_off<2048>(r, c.voffBig[0], c.sBig[2]); dma_off<3072>(r, c.voffBig[1], c.sBig[3]);
}
DI void k2_sm(const K2Issue& c, int tile, unsigned sR) {
  const u32x4 r = mksrd(c.Abase + k2_phys(c, tile) * c.astep, 0x7fffffffu);
  dma_m0(r, c.voffA[0], c.sSm[0], c.mSm + sR);
  dma_off<1024>(r, c.voffA[1], c.sSm[1]);
}
template <int TEND = LEAD2> DI void k2_touch(const K2Issue& c) {
#pragma unroll
  for (int t = 2; t < TEND; ++t) touch(mksrd(c.tchbase, 0x7fffffffu), c.tchvoff, (unsigned)t * c.tchstep, c.mScr);
}
DI void k2_pre(const K2Issue& c, int g, bool tRole, int tile) {
  const unsigned sR = tile ? K2_R1 : K2_R0, sQ = tile ? K2_Q1 : K2_Q0;
  if (tRole) { k2_sm(c, tile, sR); if (g == 1) k2_big(c, tile, sR, sQ); }
  else if (g == 0) k2_big(c, tile, sR, sQ);
}
struct K2Args { const u16* Cr; const u16* Ci; const u16* abT; const u16* h; const u16* w2; };
DI K2Issue k2_make(const K2Args& a, unsigned lds0) {
  int bid = blockIdx.x, tid = threadIdx.x; asm volatile("" : "+s"(bid), "+v"(tid));
  int xcd = XCDL(bid); asm volatile("" : "+s"(xcd));
  const int j = bid >> 3, pm = (xcd & 1) * 4 + (j & 3), role = xcd >> 1, pn = j >> 2;
  const int wid = (int)rfl(tid >> 6), lane = tid & 63, g = wid >> 2, wi = wid & 3;
  const bool tRole = role < 2; const int k0 = (role & 1) * (F / 2);
  const u16* A2 = tRole ? (role ? a.Ci : a.Cr) + (size_t)pm * 256 * S : a.h + (size_t)(k0 / 8) * (KBS / 2) + pm * 256 * 8;
  const u16* B2 = tRole ? a.abT + (size_t)(role * 1024 + pn * 128) * 8 : a.w2 + (size_t)k0 * D + pn * 128;
  K2Issue c = k2_setup(lds0, !tRole, (const unsigned char*)A2, (const unsigned char*)B2, tRole ? S / 64 : F / 128, g, wi, lane, j & 3, pn, role);
#pragma unroll
  for (int q = 0; q < 4; ++q) c.sBig[q] = rfl(c.sBig[q]);
#pragma unroll
  for (int q = 0; q < 2; ++q) c.sSm[q] = rfl(c.sSm[q]);
  c.bigstep = rfl(c.bigstep); c.astep = rfl(c.astep); c.mBig = rfl(c.mBig); c.mSm = rfl(c.mSm); c.mScr = rfl(c.mScr); c.tchstep = rfl(c.tchstep);
  asm volatile("s_nop 4" : "+s"(c.sBig[0]), "+s"(c.sBig[1]), "+s"(c.sBig[2]), "+s"(c.sBig[3]), "+s"(c.sSm[0]), "+s"(c.sSm[1]), "+s"(c.tchstep) :: "memory");
  return c;
}

#define CSM(acc, frag) "v_mfma_f32_16x16x32_bf16 %" #acc ", %" #frag ", %2, %" #acc "\n\t"
template <int NB> DI void cs_mma_bar(f32x4 (&csa)[2], const bf16x8& ones, int wi, const bf16x8 (&bfr)[NB][2]) {
  asm volatile("s_cmp_lt_u32 %3, 2\n\ts_cbranch_scc0 .Lcb2_%=\n\ts_cmp_eq_u32 %3, 0\n\ts_cbranch_scc0 .Lcb1_%=\n\t"
               "s_barrier\n\t" CSM(0, 4) CSM(1, 6) CSM(0, 5) "s_branch .Lcb4_%=\n.Lcb1_%=:\n\t"
               "s_barrier\n\t" CSM(1, 8) CSM(0, 7) CSM(1, 9) "s_branch .Lcb4_%=\n.Lcb2_%=:\n\ts_cmp_eq_u32 %3, 2\n\ts_cbranch_scc0 .Lcb3_%=\n\t"
               "s_barrier\n\t" CSM(0, 10) CSM(1, 12) CSM(0, 11) "s_branch .Lcb4_%=\n.Lcb3_%=:\n\t"
               "s_barrier\n\t" CSM(1, 14) CSM(0, 13) CSM(1, 15) ".Lcb4_%=:"
               : "+v"(csa[0]), "+v"(csa[1])
               : "v"(ones), "s"(wi), "v"(bfr[0][0]), "v"(bfr[0][1]), "v"(bfr[1][0]), "v"(bfr[1][1]), "v"(bfr[2][0]), "v"(bfr[2][1]), "v"(bfr[3][0]), "v"(bfr[3][1]),
                 "v"(bfr[4][0]), "v"(bfr[4][1]), "v"(bfr[NB - 1][0]), "v"(bfr[NB - 1][1])
               : "scc", "memory");
}
template <int NB> DI void cs_mma(f32x4 (&csa)[2], const bf16x8& ones, int wi, const bf16x8 (&bfr)[NB][2]) {
  asm volatile("s_cmp_lt_u32 %3, 2\n\ts_cbranch_scc0 .Lcs2_%=\n\ts_cmp_eq_u32 %3, 0\n\ts_cbranch_scc0 .Lcs1_%=\n\t"
               CSM(0, 4) CSM(1, 6) CSM(0, 5) "s_branch .Lcs4_%=\n.Lcs1_%=:\n\t"
               CSM(1, 8) CSM(0, 7) CSM(1, 9) "s_branch .Lcs4_%=\n.Lcs2_%=:\n\ts_cmp_eq_u32 %3, 2\n\ts_cbranch_scc0 .Lcs3_%=\n\t"
               CSM(0, 10) CSM(1, 12) CSM(0, 11) "s_branch .Lcs4_%=\n.Lcs3_%=:\n\t"
               CSM(1, 14) CSM(0, 13) CSM(1, 15) ".Lcs4_%=:"
               : "+v"(csa[0]), "+v"(csa[1])
               : "v"(ones), "s"(wi), "v"(bfr[0][0]), "v"(bfr[0][1]), "v"(bfr[1][0]), "v"(bfr[1][1]), "v"(bfr[2][0]), "v"(bfr[2][1]), "v"(bfr[3][0]), "v"(bfr[3][1]),
                 "v"(bfr[4][0]), "v"(bfr[4][1]), "v"(bfr[NB - 1][0]), "v"(bfr[NB - 1][1])
               : "scc");
}
#undef CSM

template <int TYPE  , int NB>
DI void k1_group(unsigned lds0, const K1Issue& c, const K2Args& a2, bool tRole, bool perm, int wi, int lane, int pm, int col0, u16* abT, u16* h, const u16* __restrict__ b1,
                 gu64* sy, u64 tag, int ct, u64 stv, const u32x4& rpt, unsigned vpt ST_ARG) {
  constexpr int g = NB == 6 ? 0 : 1;
  const int fr = lane & 15, fq = lane >> 4, srow0 = pm * 256;
  constexpr unsigned BRING = NB == 6 ? K1_B0 : K1_B1, PITCH = NB * 32u;
  const unsigned sw = (unsigned)((fr >> 1) & 7);
  const unsigned aoff0 = lds0 + (unsigned)(wi * 64 + fr) * 128u + (((unsigned)fq ^ sw) << 4), aoff1 = aoff0 ^ 64u;
  unsigned boff0, boff1;
  if (TYPE == 0) { boff0 = lds0 + BRING + (unsigned)fr * 128u + (((unsigned)fq ^ sw) << 4); boff1 = boff0 ^ 64u; }
  else {
    const unsigned q = (lane & 15) >> 2, p = lane & 3, g4 = lane >> 4;
    const unsigned base = lds0 + BRING + (q + 4u * (g4 & 1u) + 16u * (g4 >> 1)) * PITCH + p * 8u;
    if (NB == 6) { boff0 = base + (g4 & 1u) * 32u; boff1 = base - (g4 & 1u) * 32u; } else { boff0 = base; boff1 = base; }
  }
  f32x4 acc[4][NB];
  const unsigned scr = lds0 + KM_SCR + (unsigned)(g * 4 + wi) * 256u;
  f32x4 csa[2] = {(f32x4){0.f, 0.f, 0.f, 0.f}, (f32x4){0.f, 0.f, 0.f, 0.f}};
  bf16x8 ones = __builtin_bit_cast(bf16x8, (u32x4){0x3f803f80u, 0x3f803f80u, 0x3f803f80u, 0x3f803f80u});
  asm volatile("" : "+v"(ones), "+v"(csa[0]), "+v"(csa[1]));
  {
    const K1Step s1 = k1_prep(c, 1, 32768u, g);
    k1_dma<4>(c, s1); k1_dma<5>(c, s1); k1_dma<6>(c, s1);
    k1_dma<0>(c, s1); k1_dma<1>(c, s1); k1_dma<2>(c, s1); k1_dma<3>(c, s1);
  }
  if (lane < 2) st_sc1(sy + SY_ST + (srow0 + ct * 8 + g * 4 + wi) * 2 + lane, stv);
  asm volatile("" ::: "memory");
  if (TYPE == 1) touch(mksrd(b1 + col0, NB * 32u), (unsigned)lane * 4u, 0u, scr);
  unsigned cur = 0;
  K1Step st = k1_prep(c, 2, 65536u, 1 + g);
  unsigned pa0 = aoff0, pa1 = aoff1, pb0 = boff0, pb1 = boff1;
#pragma unroll
  for (int m = 0; m < 4; ++m)
#pragma unroll
    for (int n = 0; n < NB; ++n) { acc[m][n] = (f32x4){0.f, 0.f, 0.f, 0.f}; asm volatile("" : "+v"(acc[m][n])); }
  WAIT_V(8 + TYPE);
  BAR();
  if (g == 1) {
    if (wi == 0 && lane == 0) st_sc1(sy + SY_LN + pm * 32 + ct, tag);
    BAR();
  }
  const u32x4 rst = mksrd((const void*)(size_t)(sy + SY_ST + (srow0 + 64 * wi) * 2), 1024u);
  const unsigned stl = lds0 + KM_ST + (unsigned)(g * 4 + wi) * 1024u, csl = lds0 + KM_CS + g * 768u;
  SCHED();
  ST(1); STC(0);
  auto iter = [&](auto modec, int t) {
    constexpr int MODE = decltype(modec)::value;
    bf16x8 af[4][2], bfr[NB][2];
    constexpr bool va = MODE == 0, vb = MODE == 0 || (MODE == 1 && g == 0);
    auto rdB = [&](int n) {
      if (TYPE == 0) { bfr[n][0] = ldsd128(pb0 + n * 2048); bfr[n][1] = ldsd128(pb1 + n * 2048); }
      else {
        const unsigned pb = (n & 1) ? pb1 : pb0;
#pragma unroll
        for (int ks = 0; ks < 2; ++ks) bfr[n][ks] = ldstr(pb + n * 32 + (32 * ks) * PITCH, pb + n * 32 + (32 * ks + 8) * PITCH);
      }
    };
    auto rdA = [&](int m) { af[m][0] = ldsd128(pa0 + m * 2048); af[m][1] = ldsd128(pa1 + m * 2048); };
    if (vb) k1_dma<4>(c, st);
    rdA(0); rdA(1);
    if (vb) k1_dma<5>(c, st);
    rdA(2); rdA(3);
    if (vb) k1_dma<6>(c, st);
    rdB(0);
    if (va) k1_dma<0>(c, st);
    rdB(1);
    if (va) k1_dma<1>(c, st);
    rdB(2);
    if (va) k1_dma<2>(c, st);
    rdB(3);
    if (va) k1_dma<3>(c, st);
    rdB(4);
    if (NB == 6) rdB(5);
    auto cs_all = [&]() { cs_mma<NB>(csa, ones, wi, bfr); };
    if (MODE == 0) WAIT_V(7);
    else if (MODE == 1) {
      dma_m0(rst, (unsigned)lane * 16u, 0u, stl);
      if (PRET && g == 1) { touch(rpt, vpt, 0u, csl + 512u); WAIT_V(2); }
      else if (vb) WAIT_V(4); else WAIT_V(1);
    } else {
      if (PRET && g == 1) WAIT_V(1); else WAIT_V(0);
      WAIT_L(0);
      cs_all();
      asm volatile("s_nop 7\n\ts_nop 3" : "+v"(csa[0]), "+v"(csa[1]));
      if (fr == 0) { *(LAS f32x4*)(size_t)(csl + (unsigned)(2 * wi) * 64u + (unsigned)fq * 16u) = csa[0]; *(LAS f32x4*)(size_t)(csl + (unsigned)(2 * wi + 1) * 64u + (unsigned)fq * 16u) = csa[1]; }
    }
    WAIT_L(0);
    SETP(1);
    SCHED();
    if (MODE != 2) cs_mma_bar<NB>(csa, ones, wi, bfr); else BAR();
    SCHED();
#pragma unroll
    for (int ks = 0; ks < 2; ++ks)
#pragma unroll
      for (int n = 0; n < NB; ++n)
#pragma unroll
        for (int m = 0; m < 4; ++m) {
          acc[m][n] = TYPE == 0 ? MFMA(af[m][ks], bfr[n][ks], acc[m][n]) : MFMA(bfr[n][ks], af[m][ks], acc[m][n]);
        }
    if (MODE != 2) {
      st = k1_prep(c, t + 3, cur, t + 2 + g);
      cur = cur == 65536u ? 0u : cur + 32768u;
      const unsigned bs = (t & 1) ? 0u : K1_BSLOT;
      pa0 = aoff0 + cur; pa1 = aoff1 + cur; pb0 = boff0 + bs; pb1 = boff1 + bs;
      mfma_shadow<8 * NB>();
      asm volatile("" : "+s"(st.sa[0]), "+s"(st.sa[1]), "+s"(st.sa[2]), "+s"(st.sa[3]), "+s"(st.kb), "+s"(st.ma), "+s"(st.mb), "+s"(cur), "+v"(pa0), "+v"(pa1), "+v"(pb0), "+v"(pb1));
    }
    SETP(0);
    SCHED();
    if (!(g == 1 && MODE == 2)) BAR();
    SCHED();
  };
  for (int t = 0; t < K1_NT - 2; ++t) iter(IC<0>{}, t);
  iter(IC<1>{}, K1_NT - 2);
  iter(IC<2>{}, K1_NT - 1);
  ST(2); STC(1);
#pragma unroll 1
  for (unsigned spins = 0; spins < (1u << 20); ++spins) {
    const u32x4 gq = *(const LAS u32x4*)(size_t)(stl + (unsigned)lane * 16u);
    if (__builtin_amdgcn_ballot_w64(gq[1] != (unsigned)tag || gq[3] != (unsigned)tag) == 0ull) break;
    __builtin_amdgcn_s_sleep(2);
    WAIT_L(0);
    dma_m0(rst, (unsigned)lane * 16u, 0u, stl); WAIT_V(0);
  }
  if (TYPE == 0) {
    const __amdgpu_buffer_rsrc_t ro = mkrsrc(abT + (size_t)(srow0 / 8 + 8 * wi) * (KBS / 2) + col0 * 8, 0x7fffffffu);
    const unsigned vo = (unsigned)fq * KBS + (unsigned)fr * 16u;
    float cs[NB];
#pragma unroll
    for (int n = 0; n < NB; ++n) { constexpr int sl[6] = {0, 1, 3, 4, 5, 7}; cs[n] = *(const LAS float*)(size_t)(csl + sl[n] * 64 + fr * 4); if (n == 1 || n == 4) cs[n] += *(const LAS float*)(size_t)(csl + (sl[n] + 1) * 64 + fr * 4); }
#pragma unroll
    for (int j = 0; j < 2; ++j) {
      float mu[8], rs[8];
#pragma unroll
      for (int r = 0; r < 8; ++r) { const u32x4 gq = *(const LAS u32x4*)(size_t)(stl + (unsigned)(32 * j + 8 * fq + r) * 16u); mu[r] = __uint_as_float(gq[0]); rs[r] = __uint_as_float(gq[2]); }
#pragma unroll
      for (int n = 0; n < NB; ++n) {
        float v[8];
#pragma unroll
        for (int r = 0; r < 8; ++r) v[r] = __builtin_fmaf(-mu[r], cs[n], acc[2 * j + (r >> 2)][n][r & 3]) * rs[r];
        u32x4 w; w[0] = cvtpk(v[0], v[1]); w[1] = cvtpk(v[2], v[3]); w[2] = cvtpk(v[4], v[5]); w[3] = cvtpk(v[6], v[7]);
        __builtin_amdgcn_raw_buffer_store_b128(w, ro, vo + j * 4 * KBS + n * 256, 0, 16);
      }
    }
  } else {
    const __amdgpu_buffer_rsrc_t ro = mkrsrc(h + (size_t)(col0 / 8) * (KBS / 2) + (srow0 + 64 * wi) * 8, 0x7fffffffu);
    const unsigned rowl = perm ? (unsigned)(8 * (fr >> 2) + 4 * (fq & 1) + (fr & 3)) : (unsigned)(16 * (fq & 1) + fr);
    const unsigned vo = (unsigned)(fq >> 1) * KBS + rowl * 16u;
    float rs[4], rm[4];
#pragma unroll
    for (int m = 0; m < 4; ++m) {
      const unsigned row = perm ? (unsigned)(32 * (m >> 1) + 8 * (fr >> 2) + 4 * (m & 1) + (fr & 3)) : (unsigned)(16 * m + fr);
      const u32x4 gq = *(const LAS u32x4*)(size_t)(stl + row * 16u); rs[m] = __uint_as_float(gq[2]); rm[m] = -__uint_as_float(gq[0]) * rs[m];
    }
    auto gelu2 = [&](f32x2 a, f32x2 cs, f32x2 bv, float r_, float m_) -> unsigned {
      constexpr float c1 = -2.0f * 0.7978845608028654f * 1.4426950408889634f, c3 = c1 * 0.044715f;
      const f32x2 v = __builtin_elementwise_fma(a, (f32x2){r_, r_}, __builtin_elementwise_fma(cs, (f32x2){m_, m_}, bv));
      const f32x2 u = v * __builtin_elementwise_fma(v * v, (f32x2){c3, c3}, (f32x2){c1, c1});
      f32x2 e; e[0] = __builtin_amdgcn_exp2f(u[0]); e[1] = __builtin_amdgcn_exp2f(u[1]);
      const f32x2 d = e + 1.0f;
      f32x2 r; r[0] = __builtin_amdgcn_rcpf(d[0]); r[1] = __builtin_amdgcn_rcpf(d[1]);
      return cvtpk2(v * r);
    };
#pragma unroll
    for (int n = 0; n < NB; ++n) {
      constexpr int sl[6] = {0, 1, 3, 4, 5, 7};
      f32x4 cs = *(const LAS f32x4*)(size_t)(csl + sl[n] * 64 + fq * 16); if (n == 1 || n == 4) cs += *(const LAS f32x4*)(size_t)(csl + (sl[n] + 1) * 64 + fq * 16);
      const u32x2 bb = *(const LAS u32x2*)(size_t)(scr + (unsigned)fq * 8u + n * 32);
      const f32x2 c01 = {cs[0], cs[1]}, c23 = {cs[2], cs[3]}, b01 = {bflo(bb[0]), bfhi(bb[0])}, b23 = {bflo(bb[1]), bfhi(bb[1])};
#pragma unroll
      for (int j = 0; j < 2; ++j) {
        const f32x4 a = acc[2 * j][n], b = acc[2 * j + 1][n];
        const unsigned a0 = gelu2((f32x2){a[0], a[1]}, c01, b01, rs[2 * j], rm[2 * j]), a1 = gelu2((f32x2){a[2], a[3]}, c23, b23, rs[2 * j], rm[2 * j]);
        const unsigned b0 = gelu2((f32x2){b[0], b[1]}, c01, b01, rs[2 * j + 1], rm[2 * j + 1]), b1_ = gelu2((f32x2){b[2], b[3]}, c23, b23, rs[2 * j + 1], rm[2 * j + 1]);
        auto r0 = __builtin_amdgcn_permlane16_swap(a0, b0, false, false);
        auto r1 = __builtin_amdgcn_permlane16_swap(a1, b1_, false, false);
        u32x4 w; w[0] = r0[0]; w[1] = r1[0]; w[2] = r0[1]; w[3] = r1[1];
        __builtin_amdgcn_raw_buffer_store_b128(w, ro, vo + n * 2 * KBS + j * 512, 0, 16);
      }
    }
  }
  WAIT_V(0);
  if (lane == 0) {
    const unsigned old = __hip_atomic_fetch_add((LAS unsigned*)(size_t)(lds0 + KM_CNT + g * 4), 1u, __ATOMIC_RELAXED, __HIP_MEMORY_SCOPE_WORKGROUP);
    if (old == 3u) {
      const int hid = TYPE == 0 ? (g == 0 ? ct : 13 + ct) : (g == 0 ? ct - 13 : 19 + ct - 10);
      st_sc1(TYPE == 0 ? sy + SY_D + hid * 8 + pm : sy + SY_M + pm * 48 + hid, tag);
    }
  }
  ST(3);
}

template <bool TR>
DI void k2_loop(unsigned lds0, const K2Issue& c, int g, int wi, int lane, f32x4 (&acc)[4][4], bool early, const u32x4& rxx, const u32x4& rb2, unsigned& xaddr, const __amdgpu_buffer_rsrc_t& xw ST_ARG) {
  const int fr = lane & 15, fq = lane >> 4, wmi = wi >> 1, wn = wi & 1;
  constexpr int NT = TR ? F / 128 : S / 64;
  const unsigned sw = (unsigned)((fr >> 1) & 7);
  if (TR) {
    if (g == 0) { k2_sm(c, 0, K2_R0); k2_sm(c, 1, K2_R1); k2_touch(c); }
    else { k2_sm(c, 0, K2_R0); k2_big(c, 0, K2_R0, K2_Q0); k2_sm(c, 1, K2_R1); k2_big(c, 1, K2_R1, K2_Q1); k2_sm(c, 2, K2_R2); }
  } else {
    if (early) {
      if (g == 0) { k2_big(c, 0, K2_R0, K2_Q0); k2_sm(c, 0, K2_R0); k2_big(c, 1, K2_R1, K2_Q1); k2_sm(c, 1, K2_R1); if (!PRET) k2_touch<LEADT>(c); }
      else { k2_sm(c, 0, K2_R0); k2_big(c, 0, K2_R0, K2_Q0); k2_sm(c, 1, K2_R1); k2_big(c, 1, K2_R1, K2_Q1); k2_sm(c, 2, K2_R2); k2_touch(c); }
    } else {
      if (g == 0) { k2_big(c, 0, K2_R0, K2_Q0); k2_big(c, 1, K2_R1, K2_Q1); }
      else k2_touch(c);
    }
  }
  const unsigned abase = lds0 + (TR ? (g == 0 ? 16384u : 0u) : (g == 0 ? 0u : 16384u));
  const bool aIsQ = TR && g == 1;
  const unsigned aoff0 = TR ? abase + (unsigned)fq * 2048u + (unsigned)(wmi * 64 + fr) * 16u : abase + (unsigned)(wmi * 64 + fr) * 128u + (((unsigned)fq ^ sw) << 4);
  const unsigned aoff1 = aoff0 ^ 64u;
  constexpr int AM = TR ? 256 : 2048;
  unsigned boff[4];
  if (TR) {
    const unsigned q = (lane & 15) >> 2, p = lane & 3, g4 = lane >> 4, s8 = q + 4u * (g4 & 1u);
#pragma unroll
    for (int n = 0; n < 4; ++n) boff[n] = lds0 + (s8 + 16u * (g4 >> 1)) * 256u + ((((unsigned)(wn * 4 + n)) ^ s8) << 5) + p * 8u;
  } else {
    boff[0] = lds0 + (unsigned)fq * 2048u + (unsigned)(wn * 64 + fr) * 16u; boff[1] = boff[2] = boff[3] = 0;
  }
  unsigned curR = K2_R0, curQ = K2_Q0;
  u32x4 rb, rs; unsigned mb, ms, pa0, pa1, pb[4];
  auto prep = [&](int t) {
    const unsigned prvR = curR == 0u ? 65536u : curR - 32768u, prvQ = curQ == K2_Q0 ? K2_Q2 : curQ - 16384u;
    const int tsm = t + 2 + g;
    rb = mksrd(c.bigbase + k2_phys(c, t + 2) * c.bigstep, t + 2 < NT ? 0x7fffffffu : 0u);
    rs = mksrd(c.Abase + k2_phys(c, tsm) * c.astep, tsm < NT ? 0x7fffffffu : 0u);
    mb = c.mBig + (c.bigIsQ ? prvQ : prvR); ms = c.mSm + (g == 0 ? prvR : curR);
    const unsigned sa = aIsQ ? curQ : curR, sb = TR ? curR : curQ;
    pa0 = aoff0 + sa; pa1 = aoff1 + sa;
#pragma unroll
    for (int n = 0; n < (TR ? 4 : 1); ++n) pb[n] = boff[n] + sb;
  };
  prep(0);
  if (TR) {
    if (g == 0) { WAIT_V(2 + NPT); BAR(); } else { WAIT_V(12); BAR(); WAIT_V(6); BAR(); }
  } else {
    if (g == 0) { if (early) WAIT_V(PRET ? 6 : 6 + LEADT - 2); else WAIT_V(4); BAR(); } else { WAIT_V(12 + NPT); BAR(); WAIT_V(6 + NPT); BAR(); }
  }
  SCHED();
  ST(5); STC(2);
  auto rdA = [&](int m, int ks) -> bf16x8 { return TR ? ldsd128(pa0 + m * AM + ks * 8192) : ldsd128((ks ? pa1 : pa0) + m * AM); };
  auto rdB = [&](int n, int ks) -> bf16x8 { return TR ? ldstr(pb[n] + ks * 8192, pb[n] + ks * 8192 + 2048) : ldsd128(pb[0] + n * 256 + ks * 8192); };
  auto body = [&](auto lastc, int t) {
    constexpr bool LAST = decltype(lastc)::value;
    bf16x8 af[4][2], bfr[4][2];
    if (!LAST) dma_m0(rb, c.voffBig[0], c.sBig[0], mb);
    af[0][0] = rdA(0, 0); af[0][1] = rdA(0, 1); bfr[0][0] = rdB(0, 0);
    if (!LAST) dma_off<1024>(rb, c.voffBig[1], c.sBig[1]);
    af[1][0] = rdA(1, 0); af[1][1] = rdA(1, 1); bfr[1][0] = rdB(1, 0);
    if (!LAST) dma_off<2048>(rb, c.voffBig[0], c.sBig[2]);
    af[2][0] = rdA(2, 0); af[2][1] = rdA(2, 1); bfr[2][0] = rdB(2, 0);
    if (!LAST) dma_off<3072>(rb, c.voffBig[1], c.sBig[3]);
    af[3][0] = rdA(3, 0); af[3][1] = rdA(3, 1);
    if (!LAST) dma_m0(rs, c.voffA[0], c.sSm[0], ms);
    bfr[3][0] = rdB(3, 0); bfr[0][1] = rdB(0, 1); bfr[1][1] = rdB(1, 1);
    if (!LAST) dma_off<1024>(rs, c.voffA[1], c.sSm[1]);
    bfr[2][1] = rdB(2, 1); bfr[3][1] = rdB(3, 1);
    WAIT_L(0); BAR(); SCHED();
    SETP(1);
    if (!LAST) {
#pragma unroll
      for (int ks = 0; ks < 2; ++ks)
#pragma unroll
        for (int n = 0; n < 4; ++n)
#pragma unroll
          for (int m = 0; m < 4; ++m) acc[m][n] = MFMA(bfr[n][ks], af[m][ks], acc[m][n]);
    } else {
#pragma unroll
      for (int m = 0; m < 4; ++m) {
#pragma unroll
        for (int ks = 0; ks < 2; ++ks)
#pragma unroll
          for (int n = 0; n < 4; ++n) acc[m][n] = MFMA(bfr[n][ks], af[m][ks], acc[m][n]);
        if (m > 0) {
#pragma unroll
          for (int pr = 0; pr < 2; ++pr) {
            const f32x4 a = acc[m - 1][2 * pr], b = acc[m - 1][2 * pr + 1];
            u32x4 w; w[0] = cvtpk(a[0], a[1]); w[1] = cvtpk(a[2], a[3]); w[2] = cvtpk(b[0], b[1]); w[3] = cvtpk(b[2], b[3]);
            __builtin_amdgcn_raw_buffer_store_b128(w, xw, lane * 16 + ((m - 1) * 2 + pr) * 1024, 0, 16);
          }
        }
        SCHED();
      }
    }
    if (!LAST) {
    curR = curR == 65536u ? 0u : curR + 32768u; curQ = curQ == K2_Q2 ? K2_Q0 : curQ + 16384u;
    prep(t + 1);
    mfma_shadow<32>();
    if (TR) asm volatile("" : "+s"(rb), "+s"(rs), "+s"(mb), "+s"(ms), "+s"(curR), "+s"(curQ), "+v"(pa0), "+v"(pb[0]), "+v"(pb[1]), "+v"(pb[2]), "+v"(pb[3]));
    else asm volatile("" : "+s"(rb), "+s"(rs), "+s"(mb), "+s"(ms), "+s"(curR), "+s"(curQ), "+v"(pa0), "+v"(pa1), "+v"(pb[0]));
    }
    SETP(0);
    SCHED();
    if (!LAST) { WAIT_V(6); BAR(); } else if (g == 0) BAR();
    SCHED();
  };
  for (int t = 0; t < NT - 1; ++t) body(IC<0>{}, t);
  xaddr = c.mBig + (NT % 3 == 1 ? (c.bigIsQ ? K2_Q1 : K2_R1) : (c.bigIsQ ? K2_Q2 : K2_R2));
  static_assert(NT % 3 != 0, "slot of K-tile NT");
  dma_m0(rxx, (unsigned)(lane >> 3) * (D * 2u) + (unsigned)(lane & 7) * 16u, 0u, xaddr);
  dma_off<1024>(rxx, (unsigned)(lane >> 3) * (D * 2u) + (unsigned)(lane & 7) * 16u, 8u * D * 2u - 1024u);
  touch(rb2, (unsigned)lane * 4u, 0u, c.mScr);
  body(IC<1>{}, NT - 1);
}

__global__ __launch_bounds__(512, 2) void k_main(u16* xn, const u16* __restrict__ Dr, const u16* __restrict__ Di, const u16* __restrict__ w1,
                                                 const u16* __restrict__ b1, u16* abT, u16* h, const u16* __restrict__ Cr, const u16* __restrict__ Ci,
                                                 const u16* __restrict__ w2, const u16* __restrict__ x, const u16* __restrict__ b2, u16* __restrict__ out,
                                                 unsigned char* xbuf, unsigned long long* sync DBG_ARG) {
  ST_DECL; ST(0);
  extern __shared__ __attribute__((aligned(16))) unsigned char smem[];
  const unsigned lds0 = (unsigned)(size_t)(LAS unsigned char*)smem;
  asm volatile("" : "+s"(xn), "+s"(Dr), "+s"(Di), "+s"(w1), "+s"(b1), "+s"(x), "+s"(sync));
  const int bid = blockIdx.x, j = bid >> 3;
  int xcd = XCDL(bid); asm volatile("" : "+s"(xcd));
  const int pm = (xcd & 1) * 4 + (j & 3), ct = CTMAP(xcd, j);
  const int role = xcd >> 1, pn = j >> 2, tile = pm * 8 + pn;
  const int tid = threadIdx.x, wid = (int)rfl(tid >> 6), lane = tid & 63, g = wid >> 2, wi = wid & 3;
  gu64* sy = (gu64*)sync;
  const int lnrow = pm * 256 + ct * 8 + wid;
  const unsigned xl = lds0 + 65536u + (unsigned)wid * 2048u;
  { const u32x4 rx = mksrd(x + (size_t)lnrow * D, D * 2u); dma_m0(rx, (unsigned)lane * 16u, 0u, xl); dma_off<1024>(rx, (unsigned)lane * 16u, 0u); }
  if (CODEW && wid == 1) {
    unsigned long long pc; asm volatile("s_getpc_b64 %0" : "=s"(pc));
    touch(mksrd((const void*)(pc & ~127ull), CODEW), (unsigned)(j * 16 + (lane & 15)) * 128u, 0u, lds0 + KM_SCR + 256u);
  }
  u64 stv;
  u64 tag;
  if (tid < 2) *(LAS unsigned*)(size_t)(lds0 + KM_CNT + tid * 4) = 0u;
  const int type0 = ct < 13 ? 0 : 1, col00 = ct < 13 ? 96 * ct : 96 * (ct - 13);
  const int type1 = ct < 10 ? 0 : 1, col01 = ct < 10 ? 1248 + 80 * ct : 1824 + 80 * (ct - 10);
  const bool perm = type0 == 0;
  K1Issue c;
  c.ra = mksrd(x + (size_t)pm * 256 * D, 0x7fffffffu);
  c.leadmask = 1 << (((j & 3) + (j >> 2)) & 3);
  const unsigned i = (unsigned)lane;
#pragma unroll
  for (int par = 0; par < 2; ++par) c.voffA[par] = (perm ? 8u * (i >> 5) + ((i >> 3) & 3u) : (i >> 3)) * (D * 2u) + (((i & 7u) ^ (i >> 4) ^ ((unsigned)par << 2)) << 4);
#pragma unroll
  for (int q = 0; q < 4; ++q) { const unsigned p = (unsigned)(g * 16 + wi * 4 + q); c.sA[q] = (perm ? 16u * (p & 1u) + 4u * ((p >> 1) & 1u) + 32u * (p >> 2) : 8u * p) * (D * 2u) - q * 1024u; }
  c.mA = lds0 + (unsigned)(g * 16 + wi * 4) * 1024u;
  { const unsigned ka = k1_phys(c, 0) * 128u;
    dma_m0(c.ra, c.voffA[0], c.sA[0] + ka, c.mA); dma_off<1024>(c.ra, c.voffA[1], c.sA[1] + ka); dma_off<2048>(c.ra, c.voffA[0], c.sA[2] + ka); dma_off<3072>(c.ra, c.voffA[1], c.sA[3] + ka); }
  SCHED();
  const int otype = g == 0 ? type1 : type0, ocol = g == 0 ? col01 : col00;
  const unsigned cpr = g == 0 ? 10u : 12u, oring = g == 0 ? K1_B1 : K1_B0;
  c.bstep = otype == 0 ? 128u : 64u * F * 2u;
  c.mB = lds0 + oring + (unsigned)wi * 3072u;
#pragma unroll
  for (int q = 0; q < 3; ++q) {
    const unsigned p = (unsigned)(3 * wi + q), nr = p < cpr ? 0x7fffffffu : 0u;
    if (otype == 0) {
      int e0 = ocol + 8 * (int)p; if (e0 > 2040) e0 = 2040;
      c.rb[q] = mksrd((const unsigned char*)(e0 < 1024 ? Dr + (size_t)e0 * D : Di + (size_t)(e0 - 1024) * D) - q * 1024, nr);
      c.voffB[q] = (i >> 3) * (D * 2u) + (((i & 7u) ^ (i >> 4) ^ ((p & 1u) << 2)) << 4);
    } else {
      const unsigned ci = 64u * p + i, rho = ci / cpr, pos = ci - rho * cpr, sp = pos >> 1;
      const unsigned seg = cpr == 12u ? sp ^ ((rho >> 2) & 1u) : sp;
      const unsigned k = (rho & 3u) + 8u * ((rho >> 2) & 1u) + 4u * ((rho >> 3) & 1u) + (rho & 48u);
      c.rb[q] = mksrd((const unsigned char*)(w1 + ocol) - q * 1024, nr);
      c.voffB[q] = k * (F * 2u) + seg * 32u + (pos & 1u) * 16u;
    }
  }
  const bool tRole = role < 2;
  const K2Args a2 = {Cr, Ci, abT, h, w2};
  const u32x4 rpt = mksrd(tRole ? (role ? Ci : Cr) + (size_t)pm * 256 * S : w2 + (size_t)(role & 1) * (F / 2) * D + pn * 128, 0x7fffffffu);
  const unsigned vpt = tRole ? (unsigned)(pn * 32 + wi * 8 + (lane & 7)) * (S * 2u) + (unsigned)(lane >> 3) * 128u
                             : (unsigned)((j & 3) * 16 + wi * 4 + ((lane & 7) >> 1)) * (D * 2u) + (unsigned)(lane & 1) * 128u + (unsigned)(lane >> 3) * (64u * D * 2u);
  {
    const K1Step s0 = k1_prep(c, 0, 0u, 0);
    if (g == 1) { k1_dma<4>(c, s0); k1_dma<5>(c, s0); k1_dma<6>(c, s0); }
    asm volatile("s_load_dwordx2 %0, %1, 0x0\n\ts_waitcnt lgkmcnt(0)" : "=s"(tag) : "s"(sync + SY_EPOCH) : "memory");
    tag += 1ull;
    if (g == 1) WAIT_V(7); else WAIT_V(4);
    const u32x4 v0 = *(const LAS u32x4*)(size_t)(xl + lane * 16u), v1 = *(const LAS u32x4*)(size_t)(xl + 1024u + lane * 16u);
    float f[16];
#pragma unroll
    for (int q = 0; q < 4; ++q) { f[2 * q] = bflo(v0[q]); f[2 * q + 1] = bfhi(v0[q]); f[8 + 2 * q] = bflo(v1[q]); f[9 + 2 * q] = bfhi(v1[q]); }
    float sm = 0.f;
#pragma unroll
    for (int q = 0; q < 16; ++q) sm += f[q];
    asm volatile("" : "+v"(sm)); STL(0);
    sm = wave_sum(sm);
    const float mean = sm * (1.0f / D);
    float sq = 0.f;
#pragma unroll
    for (int q = 0; q < 16; ++q) { f[q] -= mean; sq += f[q] * f[q]; }
    sq = wave_sum(sq);
    const float rs = 1.0f / sqrtf(sq * (1.0f / D) + 1e-5f);
    stv = (tag << 32) | (u64)__float_as_uint(lane ? rs : mean);
    STL(1);
  }
  if (g == 0) {
    if (type0 == 0) k1_group<0, 6>(lds0, c, a2, tRole, perm, wi, lane, pm, col00, abT, h, b1, sy, tag, ct, stv, rpt, vpt ST_PASS);
    else k1_group<1, 6>(lds0, c, a2, tRole, perm, wi, lane, pm, col00, abT, h, b1, sy, tag, ct, stv, rpt, vpt ST_PASS);
  } else {
    if (type1 == 0) k1_group<0, 5>(lds0, c, a2, tRole, perm, wi, lane, pm, col01, abT, h, b1, sy, tag, ct, stv, rpt, vpt ST_PASS);
    else k1_group<1, 5>(lds0, c, a2, tRole, perm, wi, lane, pm, col01, abT, h, b1, sy, tag, ct, stv, rpt, vpt ST_PASS);
  }
  if (bid == 0 && wid == 1) {
    gu64* pq = sy + SY_LN + lane;
#pragma unroll 1
    for (unsigned spins = 0; spins < (1u << 20); ++spins) {
      const u64 a0 = ld_sc1(pq), a1 = ld_sc1(pq + 64), a2_ = ld_sc1(pq + 128), a3 = ld_sc1(pq + 192);
      if (__builtin_amdgcn_ballot_w64(a0 != tag || a1 != tag || a2_ != tag || a3 != tag) == 0ull) break;
      __builtin_amdgcn_s_sleep(2);
    }
    if (lane == 0) st_sc1(sy + SY_EPOCH, tag);
  }
  const K2Issue c2 = k2_make(a2, lds0);
  auto wait_deps = [&]() {
    if (tRole) {
      const int eb = role * 8 + pn, lo = eb * 128, hi = lo + 127;
      const int hlo = lo < 1248 ? lo / 96 : 13 + (lo - 1248) / 80, hhi = hi < 1248 ? hi / 96 : 13 + (hi - 1248) / 80;
      wait_words(sy + SY_D + hlo * 8, (hhi - hlo + 1) * 8, tag, lane);
    } else wait_words(sy + SY_M + pm * 48 + (role == 2 ? 0 : 18), role == 2 ? 19 : 23, tag, lane);
  };
  const bool early = EARLYT && tRole && type1 == 1;
  if (early && wid == 0) wait_deps();
  BAR();
  asm volatile("" ::: "memory");
  if (!early) {
    k2_pre(c2, g, tRole, 0); k2_pre(c2, g, tRole, 1);
    if (tRole) { if (g == 0) { if (!PRET) k2_touch<LEADT>(c2); } else k2_sm(c2, 2, K2_R2); }
    else if (g == 1 && !PRET) k2_touch(c2);
    if (wid == 0) wait_deps();
    BAR();
    asm volatile("" ::: "memory");
  }
  ST(4);
  f32x4 acc[4][4];
#pragma unroll
  for (int m = 0; m < 4; ++m)
#pragma unroll
    for (int n = 0; n < 4; ++n) acc[m][n] = (f32x4){0.f, 0.f, 0.f, 0.f};
  const u32x4 rxx = mksrd(x + (size_t)(pm * 256 + g * 128 + (wi >> 1) * 64 + role * 16) * D + pn * 128 + (wi & 1) * 64, 16u * D * 2u);
  const u32x4 rb2 = mksrd(b2 + pn * 128 + (wi & 1) * 64, 128u);
  unsigned xaddr;
  const __amdgpu_buffer_rsrc_t xw = mkrsrc(xbuf + (size_t)tile * (4 * 8 * 8 * 1024) + (role * 8 + wid) * 8192, 8192u);
  if (tRole) k2_loop<false>(lds0, c2, g, wi, lane, acc, early, rxx, rb2, xaddr, xw ST_PASS);
  else k2_loop<true>(lds0, c2, g, wi, lane, acc, false, rxx, rb2, xaddr, xw ST_PASS);
  ST(6); STC(3);
  const __amdgpu_buffer_rsrc_t xr = mkrsrc(xbuf + (size_t)tile * (4 * 8 * 8 * 1024), 4u * 8u * 8u * 1024u);
  const int fr = lane & 15, fq = lane >> 4, wmi = wi >> 1, wn = wi & 1;
  const int trow = pm * 256 + g * 128 + wmi * 64 + role * 16 + fr, ecol = pn * 128 + wn * 64 + 4 * fq;
  gu64* fl = sy + SY_X + (tile * 8 + wid) * 8;
  u64 f0 = ld_sc1(fl + (lane & 3));
  f32x4 own[4];
#pragma unroll
  for (int n = 0; n < 4; ++n) own[n] = acc[3][n];
  STX(0);
  WAIT_V(0);
  STX(1);
  if (lane == 0) st_sc1(fl + role, tag);
  asm volatile("" ::: "memory");
  u32x2 xv[4], bv[4];
#pragma unroll
  for (int n = 0; n < 4; ++n) { xv[n] = *(const LAS u32x2*)(size_t)(xaddr + (unsigned)(fr * 128 + fq * 8 + n * 32)); bv[n] = *(const LAS u32x2*)(size_t)(c2.mScr + (unsigned)(fq * 8 + n * 32)); }
  u32x4 pk[4][2];
#pragma unroll
  for (int r = 0; r < 4; ++r) { pk[r][0] = (u32x4){0u, 0u, 0u, 0u}; pk[r][1] = (u32x4){0u, 0u, 0u, 0u}; }
  unsigned have = 1u << role;
#pragma unroll 1
  for (unsigned spins = 0;;) {
    const unsigned rdy = (unsigned)__builtin_amdgcn_ballot_w64(f0 == tag) & 15u;
    const unsigned nw = rdy & ~have;
    asm volatile("" ::: "memory");
    have |= nw;
    if (have != 15u) f0 = ld_sc1(fl + (lane & 3));
#pragma unroll
    for (int r = 0; r < 4; ++r)
      if (nw & (1u << r)) {
#pragma unroll
        for (int pr = 0; pr < 2; ++pr) pk[r][pr] = __builtin_bit_cast(u32x4, __builtin_amdgcn_raw_buffer_load_b128(xr, lane * 16, ((r * 8 + wid) * 8 + ((role - r - 1) & 3) * 2 + pr) * 1024, 16));
      }
    if (have == 15u || ++spins > (1u << 20)) break;
    if (!nw) __builtin_amdgcn_s_sleep(XSLEEP);
  }
  STX(2);
  WAIT_V(0);
  STX(3);
#pragma unroll
  for (int n = 0; n < 4; ++n) {
    const float xb[4] = {bflo(xv[n][0]), bfhi(xv[n][0]), bflo(xv[n][1]), bfhi(xv[n][1])};
    const float bb[4] = {bflo(bv[n][0]), bfhi(bv[n][0]), bflo(bv[n][1]), bfhi(bv[n][1])};
    float pv[4][4];
#pragma unroll
    for (int r = 0; r < 4; ++r) {
      const unsigned w0 = pk[r][n >> 1][(n & 1) * 2], w1_ = pk[r][n >> 1][(n & 1) * 2 + 1];
      pv[r][0] = r == role ? own[n][0] : bflo(w0); pv[r][1] = r == role ? own[n][1] : bfhi(w0);
      pv[r][2] = r == role ? own[n][2] : bflo(w1_); pv[r][3] = r == role ? own[n][3] : bfhi(w1_);
    }
    float o[4];
#pragma unroll
    for (int e = 0; e < 4; ++e) {
      const float y = pv[0][e] - pv[1][e], z = pv[2][e] + pv[3][e] + bb[e];
      const float gz = z * __builtin_amdgcn_rcpf(1.0f + __builtin_amdgcn_exp2f(-1.4426950408889634f * z));
      o[e] = xb[e] + y * gz;
    }
    u32x2 w; w[0] = cvtpk(o[0], o[1]); w[1] = cvtpk(o[2], o[3]);
    __builtin_amdgcn_raw_buffer_store_b64(w, mkrsrc(out, (unsigned)(S * D * 2)), (unsigned)(trow * D + ecol + 16 * n) * 2u, 0, OUTAUX);
  }
}

extern "C" void kernel_launch(void* const* d_in, const int* in_sizes, int n_in, void* d_out, int out_size, void* d_ws, size_t ws_size, hipStream_t stream) {
  const u16* x = (const u16*)d_in[0]; const u16* Cr = (const u16*)d_in[1]; const u16* Ci = (const u16*)d_in[2];
  const u16* Dr = (const u16*)d_in[3]; const u16* Di = (const u16*)d_in[4];
  const u16* w1 = (const u16*)d_in[6]; const u16* b1 = (const u16*)d_in[7]; const u16* w2 = (const u16*)d_in[8]; const u16* b2 = (const u16*)d_in[9];
#define WSOFF 1
  char* ws = (char*)d_ws + (WSOFF == 1 ? ((ws_size - (56u << 20)) & ~(size_t)0xfffff) : WSOFF == 2 ? ((ws_size / 2) & ~(size_t)0xfffff) + (8u << 20) : 0);
  unsigned long long* sync = (unsigned long long*)ws;
  u16* xn = (u16*)(ws + (1 << 20));
  u16* abT = (u16*)(ws + (6 << 20));
  u16* h = (u16*)(ws + (18 << 20));
  unsigned char* xbuf = (unsigned char*)(ws + (40 << 20));
  (void)hipFuncSetAttribute((const void*)k_main, hipFuncAttributeMaxDynamicSharedMemorySize, KM_LDS);
  k_main<<<256, 512, KM_LDS, stream>>>(xn, Dr, Di, w1, b1, abT, h, Cr, Ci, w2, x, b2, (u16*)d_out, xbuf, sync DBG_PASS);
}
```

```cpp
#include <hip/hip_runtime.h>
#include <hip/hip_bf16.h>

typedef unsigned short u16;
typedef __bf16 bf16x8 __attribute__((ext_vector_type(8)));
typedef short s16x4 __attribute__((ext_vector_type(4)));
typedef short s16x8 __attribute__((ext_vector_type(8)));
typedef float f32x4 __attribute__((ext_vector_type(4)));
typedef float f32x2 __attribute__((ext_vector_type(2)));
typedef __bf16 bf16x2 __attribute__((ext_vector_type(2)));
typedef unsigned u32x4 __attribute__((ext_vector_type(4)));
typedef unsigned u32x2 __attribute__((ext_vector_type(2)));
#define LAS __attribute__((address_space(3)))
#define GAS __attribute__((address_space(1)))
#define DI __device__ __forceinline__

constexpr int S = 2048, D = 1024, F = 3584;
constexpr int LEAD = 4;
constexpr unsigned CODEW = 40448;
#define XCDL(b) (int)((0x73625140u >> (((unsigned)(b) & 7u) * 4u)) & 7u)

#define WAIT_V(nn) asm volatile("s_waitcnt vmcnt(%0)" ::"n"(nn) : "memory")
#define WAIT_L(n) do { asm volatile("" ::: "memory"); __builtin_amdgcn_s_waitcnt(0xC07F); asm volatile("" ::: "memory"); } while (0)
#define SETP(n) __builtin_amdgcn_s_setprio(n)
#define BAR() __builtin_amdgcn_s_barrier()
#define SCHED() __builtin_amdgcn_sched_barrier(0)

DI unsigned rfl(unsigned x) { return __builtin_amdgcn_readfirstlane(x); }
DI float bflo(unsigned v) { return __uint_as_float(v << 16); }
DI float bfhi(unsigned v) { return __uint_as_float(v & 0xffff0000u); }
DI unsigned cvtpk(float lo, float hi) { f32x2 v = {lo, hi}; bf16x2 b = __builtin_convertvector(v, bf16x2); return __builtin_bit_cast(unsigned, b); }
DI unsigned cvtpk2(f32x2 v) { bf16x2 b = __builtin_convertvector(v, bf16x2); return __builtin_bit_cast(unsigned, b); }
DI __amdgpu_buffer_rsrc_t mkrsrc(const void* p, unsigned n) { return __builtin_amdgcn_make_buffer_rsrc((void*)p, (short)0, (int)n, 0x00020000); }
DI u32x4 mksrd(const void* p, unsigned n) { const unsigned long long a = (unsigned long long)p; u32x4 r; r[0] = (unsigned)a; r[1] = (unsigned)(a >> 32) & 0xffffu; r[2] = n; r[3] = 0x00020000u; return r; }
DI void dma_m0(const u32x4& r, unsigned voff, unsigned soff, unsigned m0v) {
  asm volatile("s_mov_b32 m0, %0\n\ts_nop 0\n\tbuffer_load_dwordx4 %1, %2, %3 offen sc1 lds" ::"s"(m0v), "v"(voff), "s"(r), "s"(soff) : "memory");
}
template <int OFF> DI void dma_off(const u32x4& r, unsigned voff, unsigned soff) {
  asm volatile("buffer_load_dwordx4 %0, %1, %2 offen offset:%3 sc1 lds" ::"v"(voff), "s"(r), "s"(soff), "n"(OFF) : "memory");
}
DI void touch(const u32x4& r, unsigned voff, unsigned soff, unsigned m0v) {
  asm volatile("s_mov_b32 m0, %0\n\ts_nop 0\n\tbuffer_load_dword %1, %2, %3 offen sc1 lds" ::"s"(m0v), "v"(voff), "s"(r), "s"(soff) : "memory");
}
DI void gtouch(const void* p, unsigned m0v) {
  asm volatile("s_mov_b32 m0, %0\n\ts_nop 0\n\tglobal_load_lds_dword %1, off sc1" ::"s"(m0v), "v"(p) : "memory");
}
DI bf16x8 ldsd128(unsigned a) { return *(const LAS bf16x8*)(size_t)a; }
DI bf16x8 ldstr(unsigned a0, unsigned a1) {
  s16x4 a = __builtin_amdgcn_ds_read_tr16_b64_v4i16((LAS s16x4*)(size_t)a0);
  s16x4 b = __builtin_amdgcn_ds_read_tr16_b64_v4i16((LAS s16x4*)(size_t)a1);
  s16x8 c = __builtin_shufflevector(a, b, 0, 1, 2, 3, 4, 5, 6, 7);
  return __builtin_bit_cast(bf16x8, c);
}
DI float wave_sum(float v) {
#define DPPADD(ctrl, rm) v += __int_as_float(__builtin_amdgcn_update_dpp(0, __float_as_int(v), ctrl, rm, 0xf, false))
  DPPADD(0x111, 0xf); DPPADD(0x112, 0xf); DPPADD(0x114, 0xf); DPPADD(0x118, 0xf); DPPADD(0x142, 0xa); DPPADD(0x143, 0xc);
#undef DPPADD
  return __int_as_float(__builtin_amdgcn_readlane(__float_as_int(v), 63));
}
#define MFMA(a, b, c) __builtin_amdgcn_mfma_f32_16x16x32_bf16(a, b, c, 0, 0, 0)
template <int V> struct IC { static constexpr int value = V; };
template <int N> DI void mfma_shadow() {
#pragma unroll
  for (int i = 0; i < N; ++i) { __builtin_amdgcn_sched_group_barrier(0x8, 1, 0); __builtin_amdgcn_sched_group_barrier(0x6, 2, 0); }
}

constexpr unsigned K1_B0 = 98304, K1_B1 = 122880, K1_BSLOT = 12288, KM_SCR = 147456  , KM_CNT = KM_SCR + 2048  ,
                   KM_ST = KM_CNT + 64  , KM_CS = KM_ST + 8192  , KM_LDS = KM_CS + 1536;
constexpr unsigned K2_R0 = 32768, K2_R1 = 65536, K2_R2 = 0, K2_Q0 = 98304, K2_Q1 = 114688, K2_Q2 = 131072;
constexpr unsigned KBS = 32768;
constexpr int K1_NT = D / 64;
constexpr int NPT = LEAD - 2;
typedef unsigned long long u64;
typedef GAS u64 gu64;
constexpr int SY_EPOCH = 0, SY_LN = 16  , SY_D = SY_LN + 256  , SY_M = SY_D + 192  , SY_X = SY_M + 384  , SY_ST = 8192  ;
DI u64 ld_sc1(gu64* p) { return __hip_atomic_load(p, __ATOMIC_RELAXED, __HIP_MEMORY_SCOPE_AGENT); }
DI void st_sc1(gu64* p, u64 v) { __hip_atomic_store(p, v, __ATOMIC_RELAXED, __HIP_MEMORY_SCOPE_AGENT); }
DI void wait_words(gu64* p, int n, u64 tag, int lane) {
  gu64* q = p + (lane < n ? lane : 0);
#pragma unroll 1
  for (unsigned spins = 0; __builtin_amdgcn_ballot_w64(ld_sc1(q) != tag) != 0ull && spins < (1u << 20); ++spins) __builtin_amdgcn_s_sleep(2);
  asm volatile("" ::: "memory");
}

struct K1Issue {
  u32x4 ra, rb[3];
  unsigned voffA[2], voffB[3];
  unsigned sA[4];
  unsigned mA, mB, bstep;
  int leadmask;
};
struct K1Step { unsigned sa[4], kb, ma, mb; };
DI unsigned k1_phys(const K1Issue& c, int t) { return (unsigned)(t + (((c.leadmask >> (t & 3)) & 1) * LEAD)) & (unsigned)(K1_NT - 1); }
DI K1Step k1_prep(const K1Issue& c, int tA, unsigned slotA, int tB) {
  K1Step s; const unsigned ka = k1_phys(c, tA) * 128u;
#pragma unroll
  for (int q = 0; q < 4; ++q) s.sa[q] = c.sA[q] + ka;
  s.kb = k1_phys(c, tB) * c.bstep; s.ma = c.mA + slotA; s.mb = c.mB + ((tB & 1) ? K1_BSLOT : 0u);
  return s;
}
template <int Q> DI void k1_dma(const K1Issue& c, const K1Step& s) {
  if (Q == 0) dma_m0(c.ra, c.voffA[0], s.sa[0], s.ma);
  else if (Q < 4) dma_off<(Q & 3) * 1024>(c.ra, c.voffA[Q & 1], s.sa[Q & 3]);
  else if (Q == 4) dma_m0(c.rb[0], c.voffB[0], s.kb, s.mb);
  else if (Q == 5) dma_off<1024>(c.rb[1], c.voffB[1], s.kb);
  else dma_off<2048>(c.rb[2], c.voffB[2], s.kb);
}

struct K2Issue {
  const unsigned char* Abase; const unsigned char* bigbase; const unsigned char* tchbase;
  unsigned voffA[2], voffBig[2], sBig[4], sSm[2], bigstep, astep, mBig, mSm, mScr, tchvoff, tchstep;
  int NT, leadmask; bool bigIsQ;
};
DI unsigned k2_phys(const K2Issue& c, int t) { int p = t + (((c.leadmask >> (t & 3)) & 1) * LEAD); p = p >= c.NT ? p - c.NT : p; return (unsigned)p; }
DI K2Issue k2_setup(unsigned lds0, bool TR, const unsigned char* Abase, const unsigned char* Bbase, int NT, int g, int wi, int lane, int pm4, int pn8, int role) {
  K2Issue c; const unsigned i = (unsigned)lane;
  c.Abase = Abase; c.NT = NT;
  c.leadmask = 1 << ((pm4 + pn8) & 3);
  c.astep = TR ? 8u * KBS : 128u;
#pragma unroll
  for (int par = 0; par < 2; ++par) c.voffA[par] = TR ? ((i + 16u * (unsigned)(role + 1)) & 63u) * 16u : (i >> 3) * (S * 2u) + (((i & 7u) ^ (i >> 4) ^ ((unsigned)par << 2)) << 4);
  auto sApiece = [&](unsigned p, unsigned half) -> unsigned { return TR ? (p >> 1) * KBS + (p & 1u) * 1024u + half * 2048u : (half * 128u + (p >> 3) * 64u + (((p >> 1) + (unsigned)role + 1u) & 3u) * 16u + (p & 1u) * 8u) * (S * 2u); };
  const unsigned idx = (unsigned)(wi * 8 + (lane & 7));
  if (g == 0) {
    c.bigbase = Bbase;
    if (TR) {
      c.bigstep = 64u * D * 2u;
#pragma unroll
      for (int par = 0; par < 2; ++par) c.voffBig[par] = (i >> 4) * (D * 2u) + ((((i & 15u) >> 1) ^ ((i >> 4) + 4u * par)) << 5) + (i & 1u) * 16u;
#pragma unroll
      for (int q = 0; q < 4; ++q) { const unsigned p = (unsigned)(4 * wi + q); c.sBig[q] = (8u * (p & 1u) + 4u * ((p >> 1) & 1u) + 16u * (p >> 2)) * (D * 2u); }
    } else {
      c.bigstep = 8u * KBS;
#pragma unroll
      for (int par = 0; par < 2; ++par) c.voffBig[par] = i * 16u;
#pragma unroll
      for (int q = 0; q < 4; ++q) { const unsigned p = (unsigned)(4 * wi + q); c.sBig[q] = (p >> 1) * KBS + (p & 1u) * 1024u; }
    }
    c.tchbase = Abase; c.tchstep = c.astep;
    c.tchvoff = TR ? (unsigned)pn8 * KBS + idx * 128u : ((unsigned)pn8 * 32u + idx) * (S * 2u);
  } else {
    c.bigbase = Abase; c.bigstep = c.astep;
#pragma unroll
    for (int par = 0; par < 2; ++par) c.voffBig[par] = c.voffA[par];
#pragma unroll
    for (int q = 0; q < 4; ++q) c.sBig[q] = sApiece((unsigned)(4 * wi + q), 1u);
    c.tchbase = Bbase;
    if (!TR) { c.tchvoff = (2u * pm4 + (idx >> 4)) * KBS + (idx & 15u) * 128u; c.tchstep = 8u * KBS; }
    else { c.tchvoff = (unsigned)(pm4 * 16 + wi * 4 + ((lane & 7) >> 1)) * (D * 2u) + (lane & 1) * 128u; c.tchstep = 64u * D * 2u; }
  }
  const unsigned psm = (unsigned)((g == 0 ? 8 : 0) + 2 * wi);
#pragma unroll
  for (int q = 0; q < 2; ++q) c.sSm[q] = sApiece(psm + q, 0u);
#pragma unroll
  for (int q = 0; q < 4; ++q) c.sBig[q] -= q * 1024u;
  c.sSm[1] -= 1024u;
  c.bigIsQ = (g == 0) != TR; c.mScr = lds0 + KM_SCR + (unsigned)(g * 4 + wi) * 256u;
  c.mBig = lds0 + ((g == 1 && !TR) ? 16384u : 0u) + (unsigned)wi * 4096u; c.mSm = lds0 + (TR ? 16384u : 0u) + psm * 1024u;
  return c;
}
DI void k2_big(const K2Issue& c, int tile, unsigned sR, unsigned sQ) {
  const u32x4 r = mksrd(c.bigbase + k2_phys(c, tile) * c.bigstep, 0x7fffffffu);
  dma_m0(r, c.voffBig[0], c.sBig[0], c.mBig + (c.bigIsQ ? sQ : sR));
  dma_off<1024>(r, c.voffBig[1], c.sBig[1]); dma_off<2048>(r, c.voffBig[0], c.sBig[2]); dma_off<3072>(r, c.voffBig[1], c.sBig[3]);
}
DI void k2_sm(const K2Issue& c, int tile, unsigned sR) {
  const u32x4 r = mksrd(c.Abase + k2_phys(c, tile) * c.astep, 0x7fffffffu);
  dma_m0(r, c.voffA[0], c.sSm[0], c.mSm + sR);
  dma_off<1024>(r, c.voffA[1], c.sSm[1]);
}
DI void k2_touch(const K2Issue& c) {
#pragma unroll
  for (int t = 2; t < LEAD; ++t) touch(mksrd(c.tchbase, 0x7fffffffu), c.tchvoff, (unsigned)t * c.tchstep, c.mScr);
}
DI void k2_pre(const K2Issue& c, int g, bool tRole, int tile) {
  const unsigned sR = tile ? K2_R1 : K2_R0, sQ = tile ? K2_Q1 : K2_Q0;
  if (tRole) { k2_sm(c, tile, sR); if (g == 1) k2_big(c, tile, sR, sQ); }
  else if (g == 0) k2_big(c, tile, sR, sQ);
}
struct K2Args { const u16* Cr; const u16* Ci; const u16* abT; const u16* h; const u16* w2; };
DI K2Issue k2_make(const K2Args& a, unsigned lds0) {
  int bid = blockIdx.x, tid = threadIdx.x; asm volatile("" : "+s"(bid), "+v"(tid));
  int xcd = XCDL(bid); asm volatile("" : "+s"(xcd));
  const int j = bid >> 3, pm = (xcd & 1) * 4 + (j & 3), role = xcd >> 1, pn = j >> 2;
  const int wid = (int)rfl(tid >> 6), lane = tid & 63, g = wid >> 2, wi = wid & 3;
  const bool tRole = role < 2; const int k0 = (role & 1) * (F / 2);
  const u16* A2 = tRole ? (role ? a.Ci : a.Cr) + (size_t)pm * 256 * S : a.h + (size_t)(k0 / 8) * (KBS / 2) + pm * 256 * 8;
  const u16* B2 = tRole ? a.abT + (size_t)(role * 1024 + pn * 128) * 8 : a.w2 + (size_t)k0 * D + pn * 128;
  K2Issue c = k2_setup(lds0, !tRole, (const unsigned char*)A2, (const unsigned char*)B2, tRole ? S / 64 : F / 128, g, wi, lane, j & 3, pn, role);
#pragma unroll
  for (int q = 0; q < 4; ++q) c.sBig[q] = rfl(c.sBig[q]);
#pragma unroll
  for (int q = 0; q < 2; ++q) c.sSm[q] = rfl(c.sSm[q]);
  c.bigstep = rfl(c.bigstep); c.astep = rfl(c.astep); c.mBig = rfl(c.mBig); c.mSm = rfl(c.mSm); c.mScr = rfl(c.mScr); c.tchstep = rfl(c.tchstep);
  asm volatile("s_nop 4" : "+s"(c.sBig[0]), "+s"(c.sBig[1]), "+s"(c.sBig[2]), "+s"(c.sBig[3]), "+s"(c.sSm[0]), "+s"(c.sSm[1]), "+s"(c.tchstep) :: "memory");
  return c;
}

#define CSM(acc, frag) "v_mfma_f32_16x16x32_bf16 %" #acc ", %" #frag ", %2, %" #acc "\n\t"
template <int NB> DI void cs_mma_bar(f32x4 (&csa)[2], const bf16x8& ones, int wi, const bf16x8 (&bfr)[NB][2]) {
  asm volatile("s_cmp_lt_u32 %3, 2\n\ts_cbranch_scc0 .Lcb2_%=\n\ts_cmp_eq_u32 %3, 0\n\ts_cbranch_scc0 .Lcb1_%=\n\t"
               "s_barrier\n\t" CSM(0, 4) CSM(1, 6) CSM(0, 5) "s_branch .Lcb4_%=\n.Lcb1_%=:\n\t"
               "s_barrier\n\t" CSM(1, 8) CSM(0, 7) CSM(1, 9) "s_branch .Lcb4_%=\n.Lcb2_%=:\n\ts_cmp_eq_u32 %3, 2\n\ts_cbranch_scc0 .Lcb3_%=\n\t"
               "s_barrier\n\t" CSM(0, 10) CSM(1, 12) CSM(0, 11) "s_branch .Lcb4_%=\n.Lcb3_%=:\n\t"
               "s_barrier\n\t" CSM(1, 14) CSM(0, 13) CSM(1, 15) ".Lcb4_%=:"
               : "+v"(csa[0]), "+v"(csa[1])
               : "v"(ones), "s"(wi), "v"(bfr[0][0]), "v"(bfr[0][1]), "v"(bfr[1][0]), "v"(bfr[1][1]), "v"(bfr[2][0]), "v"(bfr[2][1]), "v"(bfr[3][0]), "v"(bfr[3][1]),
                 "v"(bfr[4][0]), "v"(bfr[4][1]), "v"(bfr[NB - 1][0]), "v"(bfr[NB - 1][1])
               : "scc", "memory");
}
template <int NB> DI void cs_mma(f32x4 (&csa)[2], const bf16x8& ones, int wi, const bf16x8 (&bfr)[NB][2]) {
  asm volatile("s_cmp_lt_u32 %3, 2\n\ts_cbranch_scc0 .Lcs2_%=\n\ts_cmp_eq_u32 %3, 0\n\ts_cbranch_scc0 .Lcs1_%=\n\t"
               CSM(0, 4) CSM(1, 6) CSM(0, 5) "s_branch .Lcs4_%=\n.Lcs1_%=:\n\t"
               CSM(1, 8) CSM(0, 7) CSM(1, 9) "s_branch .Lcs4_%=\n.Lcs2_%=:\n\ts_cmp_eq_u32 %3, 2\n\ts_cbranch_scc0 .Lcs3_%=\n\t"
               CSM(0, 10) CSM(1, 12) CSM(0, 11) "s_branch .Lcs4_%=\n.Lcs3_%=:\n\t"
               CSM(1, 14) CSM(0, 13) CSM(1, 15) ".Lcs4_%=:"
               : "+v"(csa[0]), "+v"(csa[1])
               : "v"(ones), "s"(wi), "v"(bfr[0][0]), "v"(bfr[0][1]), "v"(bfr[1][0]), "v"(bfr[1][1]), "v"(bfr[2][0]), "v"(bfr[2][1]), "v"(bfr[3][0]), "v"(bfr[3][1]),
                 "v"(bfr[4][0]), "v"(bfr[4][1]), "v"(bfr[NB - 1][0]), "v"(bfr[NB - 1][1])
               : "scc");
}
#undef CSM

template <int TYPE  , int NB>
DI void k1_group(unsigned lds0, const K1Issue& c, bool perm, int wi, int lane, int pm, int col0, u16* abT, u16* h, const u16* __restrict__ b1,
                 gu64* sy, u64 tag, int ct, u64 stv, const unsigned char* ppt) {
  constexpr int g = NB == 6 ? 0 : 1;
  const int fr = lane & 15, fq = lane >> 4, srow0 = pm * 256;
  constexpr unsigned BRING = NB == 6 ? K1_B0 : K1_B1, PITCH = NB * 32u;
  const unsigned sw = (unsigned)((fr >> 1) & 7);
  const unsigned aoff0 = lds0 + (unsigned)(wi * 64 + fr) * 128u + (((unsigned)fq ^ sw) << 4), aoff1 = aoff0 ^ 64u;
  unsigned boff0, boff1;
  if (TYPE == 0) { boff0 = lds0 + BRING + (unsigned)fr * 128u + (((unsigned)fq ^ sw) << 4); boff1 = boff0 ^ 64u; }
  else {
    const unsigned q = (lane & 15) >> 2, p = lane & 3, g4 = lane >> 4;
    const unsigned base = lds0 + BRING + (q + 4u * (g4 & 1u) + 16u * (g4 >> 1)) * PITCH + p * 8u;
    if (NB == 6) { boff0 = base + (g4 & 1u) * 32u; boff1 = base - (g4 & 1u) * 32u; } else { boff0 = base; boff1 = base; }
  }
  f32x4 acc[4][NB];
  const unsigned scr = lds0 + KM_SCR + (unsigned)(g * 4 + wi) * 256u;
  f32x4 csa[2] = {(f32x4){0.f, 0.f, 0.f, 0.f}, (f32x4){0.f, 0.f, 0.f, 0.f}};
  bf16x8 ones = __builtin_bit_cast(bf16x8, (u32x4){0x3f803f80u, 0x3f803f80u, 0x3f803f80u, 0x3f803f80u});
  asm volatile("" : "+v"(ones), "+v"(csa[0]), "+v"(csa[1]));
  {
    const K1Step s1 = k1_prep(c, 1, 32768u, g);
    k1_dma<4>(c, s1); k1_dma<5>(c, s1); k1_dma<6>(c, s1);
    k1_dma<0>(c, s1); k1_dma<1>(c, s1); k1_dma<2>(c, s1); k1_dma<3>(c, s1);
  }
  if (lane < 2) st_sc1(sy + SY_ST + (srow0 + ct * 8 + g * 4 + wi) * 2 + lane, stv);
  asm volatile("" ::: "memory");
  if (TYPE == 1) touch(mksrd(b1 + col0, NB * 32u), (unsigned)lane * 4u, 0u, scr);
  unsigned cur = 0;
  K1Step st = k1_prep(c, 2, 65536u, 1 + g);
  unsigned pa0 = aoff0, pa1 = aoff1, pb0 = boff0, pb1 = boff1;
#pragma unroll
  for (int m = 0; m < 4; ++m)
#pragma unroll
    for (int n = 0; n < NB; ++n) { acc[m][n] = (f32x4){0.f, 0.f, 0.f, 0.f}; asm volatile("" : "+v"(acc[m][n])); }
  WAIT_V(8 + TYPE);
  BAR();
  if (g == 1) {
    if (wi == 0 && lane == 0) st_sc1(sy + SY_LN + pm * 32 + ct, tag);
    BAR();
  }
  const u32x4 rst = mksrd((const void*)(size_t)(sy + SY_ST + (srow0 + 64 * wi) * 2), 1024u);
  const unsigned stl = lds0 + KM_ST + (unsigned)(g * 4 + wi) * 1024u, csl = lds0 + KM_CS + g * 768u;
  SCHED();
  auto iter = [&](auto modec, int t) {
    constexpr int MODE = decltype(modec)::value;
    bf16x8 af[4][2], bfr[NB][2];
    constexpr bool va = MODE == 0, vb = MODE == 0 || (MODE == 1 && g == 0);
    auto rdB = [&](int n) {
      if (TYPE == 0) { bfr[n][0] = ldsd128(pb0 + n * 2048); bfr[n][1] = ldsd128(pb1 + n * 2048); }
      else {
        const unsigned pb = (n & 1) ? pb1 : pb0;
#pragma unroll
        for (int ks = 0; ks < 2; ++ks) bfr[n][ks] = ldstr(pb + n * 32 + (32 * ks) * PITCH, pb + n * 32 + (32 * ks + 8) * PITCH);
      }
    };
    auto rdA = [&](int m) { af[m][0] = ldsd128(pa0 + m * 2048); af[m][1] = ldsd128(pa1 + m * 2048); };
    if (vb) k1_dma<4>(c, st);
    rdA(0); rdA(1);
    if (vb) k1_dma<5>(c, st);
    rdA(2); rdA(3);
    if (vb) k1_dma<6>(c, st);
    rdB(0);
    if (va) k1_dma<0>(c, st);
    rdB(1);
    if (va) k1_dma<1>(c, st);
    rdB(2);
    if (va) k1_dma<2>(c, st);
    rdB(3);
    if (va) k1_dma<3>(c, st);
    rdB(4);
    if (NB == 6) rdB(5);
    if (MODE == 0) WAIT_V(7);
    else if (MODE == 1) {
      dma_m0(rst, (unsigned)lane * 16u, 0u, stl);
      if (g == 1) { gtouch(ppt, csl + 512u); WAIT_V(2); }
      else WAIT_V(4);
    } else {
      if (g == 1) WAIT_V(1); else WAIT_V(0);
#pragma unroll 1
      for (unsigned spins = 0; spins < (1u << 20); ++spins) {
        const u32x4 gq = *(const LAS u32x4*)(size_t)(stl + (unsigned)lane * 16u);
        if (__builtin_amdgcn_ballot_w64(gq[1] != (unsigned)tag || gq[3] != (unsigned)tag) == 0ull) break;
        __builtin_amdgcn_s_sleep(2);
        WAIT_L(0);
        dma_m0(rst, (unsigned)lane * 16u, 0u, stl); WAIT_V(0);
      }
      WAIT_L(0);
      cs_mma<NB>(csa, ones, wi, bfr);
      asm volatile("s_nop 7\n\ts_nop 3" : "+v"(csa[0]), "+v"(csa[1]));
      if (fr == 0) { *(LAS f32x4*)(size_t)(csl + (unsigned)(2 * wi) * 64u + (unsigned)fq * 16u) = csa[0]; *(LAS f32x4*)(size_t)(csl + (unsigned)(2 * wi + 1) * 64u + (unsigned)fq * 16u) = csa[1]; }
    }
    WAIT_L(0);
    SETP(1);
    SCHED();
    if (MODE != 2) cs_mma_bar<NB>(csa, ones, wi, bfr); else BAR();
    SCHED();
#pragma unroll
    for (int ks = 0; ks < 2; ++ks)
#pragma unroll
      for (int n = 0; n < NB; ++n)
#pragma unroll
        for (int m = 0; m < 4; ++m) {
          acc[m][n] = TYPE == 0 ? MFMA(af[m][ks], bfr[n][ks], acc[m][n]) : MFMA(bfr[n][ks], af[m][ks], acc[m][n]);
        }
    if (MODE != 2) {
      st = k1_prep(c, t + 3, cur, t + 2 + g);
      cur = cur == 65536u ? 0u : cur + 32768u;
      const unsigned bs = (t & 1) ? 0u : K1_BSLOT;
      pa0 = aoff0 + cur; pa1 = aoff1 + cur; pb0 = boff0 + bs; pb1 = boff1 + bs;
      mfma_shadow<8 * NB>();
      asm volatile("" : "+s"(st.sa[0]), "+s"(st.sa[1]), "+s"(st.sa[2]), "+s"(st.sa[3]), "+s"(st.kb), "+s"(st.ma), "+s"(st.mb), "+s"(cur), "+v"(pa0), "+v"(pa1), "+v"(pb0), "+v"(pb1));
    }
    SETP(0);
    SCHED();
    if (!(g == 1 && MODE == 2)) BAR();
    SCHED();
  };
  for (int t = 0; t < K1_NT - 2; ++t) iter(IC<0>{}, t);
  iter(IC<1>{}, K1_NT - 2);
  iter(IC<2>{}, K1_NT - 1);
  if (TYPE == 0) {
    const __amdgpu_buffer_rsrc_t ro = mkrsrc(abT + (size_t)(srow0 / 8 + 8 * wi) * (KBS / 2) + col0 * 8, 0x7fffffffu);
    const unsigned vo = (unsigned)fq * KBS + (unsigned)fr * 16u;
    float cs[NB];
#pragma unroll
    for (int n = 0; n < NB; ++n) { constexpr int sl[6] = {0, 1, 3, 4, 5, 7}; cs[n] = *(const LAS float*)(size_t)(csl + sl[n] * 64 + fr * 4); if (n == 1 || n == 4) cs[n] += *(const LAS float*)(size_t)(csl + (sl[n] + 1) * 64 + fr * 4); }
#pragma unroll
    for (int j = 0; j < 2; ++j) {
      float mu[8], rs[8];
#pragma unroll
      for (int r = 0; r < 8; ++r) { const u32x4 gq = *(const LAS u32x4*)(size_t)(stl + (unsigned)(32 * j + 8 * fq + r) * 16u); mu[r] = __uint_as_float(gq[0]); rs[r] = __uint_as_float(gq[2]); }
#pragma unroll
      for (int n = 0; n < NB; ++n) {
        float v[8];
#pragma unroll
        for (int r = 0; r < 8; ++r) v[r] = __builtin_fmaf(-mu[r], cs[n], acc[2 * j + (r >> 2)][n][r & 3]) * rs[r];
        u32x4 w; w[0] = cvtpk(v[0], v[1]); w[1] = cvtpk(v[2], v[3]); w[2] = cvtpk(v[4], v[5]); w[3] = cvtpk(v[6], v[7]);
        __builtin_amdgcn_raw_buffer_store_b128(w, ro, vo + j * 4 * KBS + n * 256, 0, 16);
      }
    }
  } else {
    const __amdgpu_buffer_rsrc_t ro = mkrsrc(h + (size_t)(col0 / 8) * (KBS / 2) + (srow0 + 64 * wi) * 8, 0x7fffffffu);
    const unsigned rowl = perm ? (unsigned)(8 * (fr >> 2) + 4 * (fq & 1) + (fr & 3)) : (unsigned)(16 * (fq & 1) + fr);
    const unsigned vo = (unsigned)(fq >> 1) * KBS + rowl * 16u;
    float rs[4], rm[4];
#pragma unroll
    for (int m = 0; m < 4; ++m) {
      const unsigned row = perm ? (unsigned)(32 * (m >> 1) + 8 * (fr >> 2) + 4 * (m & 1) + (fr & 3)) : (unsigned)(16 * m + fr);
      const u32x4 gq = *(const LAS u32x4*)(size_t)(stl + row * 16u); rs[m] = __uint_as_float(gq[2]); rm[m] = -__uint_as_float(gq[0]) * rs[m];
    }
    auto gelu2 = [&](f32x2 a, f32x2 cs, f32x2 bv, float r_, float m_) -> unsigned {
      constexpr float c1 = -2.0f * 0.7978845608028654f * 1.4426950408889634f, c3 = c1 * 0.044715f;
      const f32x2 v = __builtin_elementwise_fma(a, (f32x2){r_, r_}, __builtin_elementwise_fma(cs, (f32x2){m_, m_}, bv));
      const f32x2 u = v * __builtin_elementwise_fma(v * v, (f32x2){c3, c3}, (f32x2){c1, c1});
      f32x2 e; e[0] = __builtin_amdgcn_exp2f(u[0]); e[1] = __builtin_amdgcn_exp2f(u[1]);
      const f32x2 d = e + 1.0f;
      f32x2 r; r[0] = __builtin_amdgcn_rcpf(d[0]); r[1] = __builtin_amdgcn_rcpf(d[1]);
      return cvtpk2(v * r);
    };
#pragma unroll
    for (int n = 0; n < NB; ++n) {
      constexpr int sl[6] = {0, 1, 3, 4, 5, 7};
      f32x4 cs = *(const LAS f32x4*)(size_t)(csl + sl[n] * 64 + fq * 16); if (n == 1 || n == 4) cs += *(const LAS f32x4*)(size_t)(csl + (sl[n] + 1) * 64 + fq * 16);
      const u32x2 bb = *(const LAS u32x2*)(size_t)(scr + (unsigned)fq * 8u + n * 32);
      const f32x2 c01 = {cs[0], cs[1]}, c23 = {cs[2], cs[3]}, b01 = {bflo(bb[0]), bfhi(bb[0])}, b23 = {bflo(bb[1]), bfhi(bb[1])};
#pragma unroll
      for (int j = 0; j < 2; ++j) {
        const f32x4 a = acc[2 * j][n], b = acc[2 * j + 1][n];
        const unsigned a0 = gelu2((f32x2){a[0], a[1]}, c01, b01, rs[2 * j], rm[2 * j]), a1 = gelu2((f32x2){a[2], a[3]}, c23, b23, rs[2 * j], rm[2 * j]);
        const unsigned b0 = gelu2((f32x2){b[0], b[1]}, c01, b01, rs[2 * j + 1], rm[2 * j + 1]), b1_ = gelu2((f32x2){b[2], b[3]}, c23, b23, rs[2 * j + 1], rm[2 * j + 1]);
        auto r0 = __builtin_amdgcn_permlane16_swap(a0, b0, false, false);
        auto r1 = __builtin_amdgcn_permlane16_swap(a1, b1_, false, false);
        u32x4 w; w[0] = r0[0]; w[1] = r1[0]; w[2] = r0[1]; w[3] = r1[1];
        __builtin_amdgcn_raw_buffer_store_b128(w, ro, vo + n * 2 * KBS + j * 512, 0, 16);
      }
    }
  }
}

template <bool TR>
DI void k2_loop(unsigned lds0, const K2Issue& c, int g, int wi, int lane, f32x4 (&acc)[4][4], bool early, const u32x4& rxx, const u32x4& rb2, unsigned& xaddr, const __amdgpu_buffer_rsrc_t& xw) {
  const int fr = lane & 15, fq = lane >> 4, wmi = wi >> 1, wn = wi & 1;
  constexpr int NT = TR ? F / 128 : S / 64;
  const unsigned sw = (unsigned)((fr >> 1) & 7);
  if (TR) {
    if (g == 0) { k2_sm(c, 0, K2_R0); k2_sm(c, 1, K2_R1); k2_touch(c); }
    else { k2_sm(c, 0, K2_R0); k2_big(c, 0, K2_R0, K2_Q0); k2_sm(c, 1, K2_R1); k2_big(c, 1, K2_R1, K2_Q1); k2_sm(c, 2, K2_R2); }
  } else {
    if (early) {
      if (g == 0) { k2_big(c, 0, K2_R0, K2_Q0); k2_sm(c, 0, K2_R0); k2_big(c, 1, K2_R1, K2_Q1); k2_sm(c, 1, K2_R1); }
      else { k2_sm(c, 0, K2_R0); k2_big(c, 0, K2_R0, K2_Q0); k2_sm(c, 1, K2_R1); k2_big(c, 1, K2_R1, K2_Q1); k2_sm(c, 2, K2_R2); }
    } else {
      if (g == 0) { k2_big(c, 0, K2_R0, K2_Q0); k2_big(c, 1, K2_R1, K2_Q1); }
      else k2_touch(c);
    }
  }
  const unsigned abase = lds0 + (TR ? (g == 0 ? 16384u : 0u) : (g == 0 ? 0u : 16384u));
  const bool aIsQ = TR && g == 1;
  const unsigned aoff0 = TR ? abase + (unsigned)fq * 2048u + (unsigned)(wmi * 64 + fr) * 16u : abase + (unsigned)(wmi * 64 + fr) * 128u + (((unsigned)fq ^ sw) << 4);
  const unsigned aoff1 = aoff0 ^ 64u;
  constexpr int AM = TR ? 256 : 2048;
  unsigned boff[4];
  if (TR) {
    const unsigned q = (lane & 15) >> 2, p = lane & 3, g4 = lane >> 4, s8 = q + 4u * (g4 & 1u);
#pragma unroll
    for (int n = 0; n < 4; ++n) boff[n] = lds0 + (s8 + 16u * (g4 >> 1)) * 256u + ((((unsigned)(wn * 4 + n)) ^ s8) << 5) + p * 8u;
  } else {
    boff[0] = lds0 + (unsigned)fq * 2048u + (unsigned)(wn * 64 + fr) * 16u; boff[1] = boff[2] = boff[3] = 0;
  }
  unsigned curR = K2_R0, curQ = K2_Q0;
  u32x4 rb, rs; unsigned mb, ms, pa0, pa1, pb[4];
  auto prep = [&](int t) {
    const unsigned prvR = curR == 0u ? 65536u : curR - 32768u, prvQ = curQ == K2_Q0 ? K2_Q2 : curQ - 16384u;
    const int tsm = t + 2 + g;
    rb = mksrd(c.bigbase + k2_phys(c, t + 2) * c.bigstep, t + 2 < NT ? 0x7fffffffu : 0u);
    rs = mksrd(c.Abase + k2_phys(c, tsm) * c.astep, tsm < NT ? 0x7fffffffu : 0u);
    mb = c.mBig + (c.bigIsQ ? prvQ : prvR); ms = c.mSm + (g == 0 ? prvR : curR);
    const unsigned sa = aIsQ ? curQ : curR, sb = TR ? curR : curQ;
    pa0 = aoff0 + sa; pa1 = aoff1 + sa;
#pragma unroll
    for (int n = 0; n < (TR ? 4 : 1); ++n) pb[n] = boff[n] + sb;
  };
  prep(0);
#pragma unroll
  for (int m = 0; m < 4; ++m)
#pragma unroll
    for (int n = 0; n < 4; ++n) { acc[m][n] = (f32x4){0.f, 0.f, 0.f, 0.f}; asm volatile("" : "+v"(acc[m][n])); }
  if (TR) {
    if (g == 0) { WAIT_V(2 + NPT); BAR(); } else { WAIT_V(12); BAR(); WAIT_V(6); BAR(); }
  } else {
    if (g == 0) { if (early) WAIT_V(6); else WAIT_V(4); BAR(); }
    else if (early) { WAIT_V(12); BAR(); k2_touch(c); WAIT_V(6 + NPT); BAR(); }
    else { WAIT_V(12 + NPT); BAR(); WAIT_V(6 + NPT); BAR(); }
  }
  SCHED();
  auto rdA = [&](int m, int ks) -> bf16x8 { return TR ? ldsd128(pa0 + m * AM + ks * 8192) : ldsd128((ks ? pa1 : pa0) + m * AM); };
  auto rdB = [&](int n, int ks) -> bf16x8 { return TR ? ldstr(pb[n] + ks * 8192, pb[n] + ks * 8192 + 2048) : ldsd128(pb[0] + n * 256 + ks * 8192); };
  auto body = [&](auto lastc, int t) {
    constexpr bool LAST = decltype(lastc)::value;
    bf16x8 af[4][2], bfr[4][2];
    if (!LAST) dma_m0(rb, c.voffBig[0], c.sBig[0], mb);
    af[0][0] = rdA(0, 0); af[0][1] = rdA(0, 1); bfr[0][0] = rdB(0, 0);
    if (!LAST) dma_off<1024>(rb, c.voffBig[1], c.sBig[1]);
    af[1][0] = rdA(1, 0); af[1][1] = rdA(1, 1); bfr[1][0] = rdB(1, 0);
    if (!LAST) dma_off<2048>(rb, c.voffBig[0], c.sBig[2]);
    af[2][0] = rdA(2, 0); af[2][1] = rdA(2, 1); bfr[2][0] = rdB(2, 0);
    if (!LAST) dma_off<3072>(rb, c.voffBig[1], c.sBig[3]);
    af[3][0] = rdA(3, 0); af[3][1] = rdA(3, 1);
    if (!LAST) dma_m0(rs, c.voffA[0], c.sSm[0], ms);
    bfr[3][0] = rdB(3, 0); bfr[0][1] = rdB(0, 1); bfr[1][1] = rdB(1, 1);
    if (!LAST) dma_off<1024>(rs, c.voffA[1], c.sSm[1]);
    bfr[2][1] = rdB(2, 1); bfr[3][1] = rdB(3, 1);
    WAIT_L(0); BAR(); SCHED();
    SETP(1);
    if (!LAST) {
#pragma unroll
      for (int ks = 0; ks < 2; ++ks)
#pragma unroll
        for (int n = 0; n < 4; ++n)
#pragma unroll
          for (int m = 0; m < 4; ++m) acc[m][n] = MFMA(bfr[n][ks], af[m][ks], acc[m][n]);
    } else {
#pragma unroll
      for (int m = 0; m < 4; ++m) {
#pragma unroll
        for (int ks = 0; ks < 2; ++ks)
#pragma unroll
          for (int n = 0; n < 4; ++n) acc[m][n] = MFMA(bfr[n][ks], af[m][ks], acc[m][n]);
        if (m > 0) {
#pragma unroll
          for (int pr = 0; pr < 2; ++pr) {
            const f32x4 a = acc[m - 1][2 * pr], b = acc[m - 1][2 * pr + 1];
            u32x4 w; w[0] = cvtpk(a[0], a[1]); w[1] = cvtpk(a[2], a[3]); w[2] = cvtpk(b[0], b[1]); w[3] = cvtpk(b[2], b[3]);
            __builtin_amdgcn_raw_buffer_store_b128(w, xw, lane * 16 + ((m - 1) * 2 + pr) * 1024, 0, 16);
          }
        }
        SCHED();
      }
    }
    if (!LAST) {
    curR = curR == 65536u ? 0u : curR + 32768u; curQ = curQ == K2_Q2 ? K2_Q0 : curQ + 16384u;
    prep(t + 1);
    mfma_shadow<32>();
    if (TR) asm volatile("" : "+s"(rb), "+s"(rs), "+s"(mb), "+s"(ms), "+s"(curR), "+s"(curQ), "+v"(pa0), "+v"(pb[0]), "+v"(pb[1]), "+v"(pb[2]), "+v"(pb[3]));
    else asm volatile("" : "+s"(rb), "+s"(rs), "+s"(mb), "+s"(ms), "+s"(curR), "+s"(curQ), "+v"(pa0), "+v"(pa1), "+v"(pb[0]));
    }
    SETP(0);
    SCHED();
    if (!LAST) { WAIT_V(6); BAR(); } else if (g == 0) BAR();
    SCHED();
  };
  for (int t = 0; t < NT - 1; ++t) body(IC<0>{}, t);
  xaddr = c.mBig + (NT % 3 == 1 ? (c.bigIsQ ? K2_Q1 : K2_R1) : (c.bigIsQ ? K2_Q2 : K2_R2));
  static_assert(NT % 3 != 0, "slot of K-tile NT");
  dma_m0(rxx, (unsigned)(lane >> 3) * (D * 2u) + (unsigned)(lane & 7) * 16u, 0u, xaddr);
  dma_off<1024>(rxx, (unsigned)(lane >> 3) * (D * 2u) + (unsigned)(lane & 7) * 16u, 8u * D * 2u - 1024u);
  touch(rb2, (unsigned)lane * 4u, 0u, c.mScr);
  body(IC<1>{}, NT - 1);
}

__global__ __launch_bounds__(512, 2) void k_main(const u16* __restrict__ Dr, const u16* __restrict__ Di, const u16* __restrict__ w1,
                                                 const u16* __restrict__ b1, u16* abT, u16* h, const u16* __restrict__ Cr, const u16* __restrict__ Ci,
                                                 const u16* __restrict__ w2, const u16* __restrict__ x, const u16* __restrict__ b2, u16* __restrict__ out,
                                                 unsigned char* xbuf, unsigned long long* sync) {
  extern __shared__ __attribute__((aligned(16))) unsigned char smem[];
  const unsigned lds0 = (unsigned)(size_t)(LAS unsigned char*)smem;
  asm volatile("" : "+s"(Dr), "+s"(Di), "+s"(w1), "+s"(b1), "+s"(x), "+s"(sync));
  const int bid = blockIdx.x, j = bid >> 3;
  int xcd = XCDL(bid); asm volatile("" : "+s"(xcd));
  const int pm = (xcd & 1) * 4 + (j & 3), ct = (xcd >> 1) * 8 + (j >> 2);
  const int role = xcd >> 1, pn = j >> 2, tile = pm * 8 + pn;
  const int tid = threadIdx.x, wid = (int)rfl(tid >> 6), lane = tid & 63, g = wid >> 2, wi = wid & 3;
  gu64* sy = (gu64*)sync;
  const int lnrow = pm * 256 + ct * 8 + wid;
  const unsigned xl = lds0 + 65536u + (unsigned)wid * 2048u;
  { const u32x4 rx = mksrd(x + (size_t)lnrow * D, D * 2u); dma_m0(rx, (unsigned)lane * 16u, 0u, xl); dma_off<1024>(rx, (unsigned)lane * 16u, 0u); }
  if (wid == 1) {
    unsigned long long pc; asm volatile("s_getpc_b64 %0" : "=s"(pc));
    touch(mksrd((const void*)(pc & ~127ull), CODEW), (unsigned)(j * 16 + (lane & 15)) * 128u, 0u, lds0 + KM_SCR + 256u);
  }
  u64 stv;
  u64 tag;
  if (tid < 4) *(LAS unsigned*)(size_t)(lds0 + KM_CNT + tid * 4) = 0u;
  const int type0 = ct < 13 ? 0 : 1, col00 = ct < 13 ? 96 * ct : 96 * (ct - 13);
  const int type1 = ct < 10 ? 0 : 1, col01 = ct < 10 ? 1248 + 80 * ct : 1824 + 80 * (ct - 10);
  const bool perm = type0 == 0;
  K1Issue c;
  c.ra = mksrd(x + (size_t)pm * 256 * D, 0x7fffffffu);
  c.leadmask = 1 << (((j & 3) + (j >> 2)) & 3);
  const unsigned i = (unsigned)lane;
#pragma unroll
  for (int par = 0; par < 2; ++par) c.voffA[par] = (perm ? 8u * (i >> 5) + ((i >> 3) & 3u) : (i >> 3)) * (D * 2u) + (((i & 7u) ^ (i >> 4) ^ ((unsigned)par << 2)) << 4);
#pragma unroll
  for (int q = 0; q < 4; ++q) { const unsigned p = (unsigned)(g * 16 + wi * 4 + q); c.sA[q] = (perm ? 16u * (p & 1u) + 4u * ((p >> 1) & 1u) + 32u * (p >> 2) : 8u * p) * (D * 2u) - q * 1024u; }
  c.mA = lds0 + (unsigned)(g * 16 + wi * 4) * 1024u;
  { const unsigned ka = k1_phys(c, 0) * 128u;
    dma_m0(c.ra, c.voffA[0], c.sA[0] + ka, c.mA); dma_off<1024>(c.ra, c.voffA[1], c.sA[1] + ka); dma_off<2048>(c.ra, c.voffA[0], c.sA[2] + ka); dma_off<3072>(c.ra, c.voffA[1], c.sA[3] + ka); }
  SCHED();
  const int otype = g == 0 ? type1 : type0, ocol = g == 0 ? col01 : col00;
  const unsigned cpr = g == 0 ? 10u : 12u, oring = g == 0 ? K1_B1 : K1_B0;
  c.bstep = otype == 0 ? 128u : 64u * F * 2u;
  c.mB = lds0 + oring + (unsigned)wi * 3072u;
#pragma unroll
  for (int q = 0; q < 3; ++q) {
    const unsigned p = (unsigned)(3 * wi + q), nr = p < cpr ? 0x7fffffffu : 0u;
    if (otype == 0) {
      int e0 = ocol + 8 * (int)p; if (e0 > 2040) e0 = 2040;
      c.rb[q] = mksrd((const unsigned char*)(e0 < 1024 ? Dr + (size_t)e0 * D : Di + (size_t)(e0 - 1024) * D) - q * 1024, nr);
      c.voffB[q] = (i >> 3) * (D * 2u) + (((i & 7u) ^ (i >> 4) ^ ((p & 1u) << 2)) << 4);
    } else {
      const unsigned ci = 64u * p + i, rho = ci / cpr, pos = ci - rho * cpr, sp = pos >> 1;
      const unsigned seg = cpr == 12u ? sp ^ ((rho >> 2) & 1u) : sp;
      const unsigned k = (rho & 3u) + 8u * ((rho >> 2) & 1u) + 4u * ((rho >> 3) & 1u) + (rho & 48u);
      c.rb[q] = mksrd((const unsigned char*)(w1 + ocol) - q * 1024, nr);
      c.voffB[q] = k * (F * 2u) + seg * 32u + (pos & 1u) * 16u;
    }
  }
  const bool tRole = role < 2;
  const K2Args a2 = {Cr, Ci, abT, h, w2};
  const unsigned char* ppt = tRole ? (const unsigned char*)((role ? Ci : Cr) + (size_t)(pm * 256 + pn * 32 + wi * 8 + (lane & 7)) * S + (lane >> 3) * 64)
                                   : (const unsigned char*)(w2 + (size_t)((role & 1) * (F / 2) + (lane >> 3) * 64 + (j & 3) * 16 + wi * 4 + ((lane & 7) >> 1)) * D + pn * 128 + (lane & 1) * 64);
  asm volatile("" : "+v"(ppt));
  {
    const K1Step s0 = k1_prep(c, 0, 0u, 0);
    if (g == 1) { k1_dma<4>(c, s0); k1_dma<5>(c, s0); k1_dma<6>(c, s0); }
    asm volatile("s_load_dwordx2 %0, %1, 0x0\n\ts_waitcnt lgkmcnt(0)" : "=s"(tag) : "s"(sync + SY_EPOCH) : "memory");
    tag += 1ull;
    if (g == 1) WAIT_V(7); else WAIT_V(4);
    const u32x4 v0 = *(const LAS u32x4*)(size_t)(xl + lane * 16u), v1 = *(const LAS u32x4*)(size_t)(xl + 1024u + lane * 16u);
    float f[16];
#pragma unroll
    for (int q = 0; q < 4; ++q) { f[2 * q] = bflo(v0[q]); f[2 * q + 1] = bfhi(v0[q]); f[8 + 2 * q] = bflo(v1[q]); f[9 + 2 * q] = bfhi(v1[q]); }
    float sm = 0.f;
#pragma unroll
    for (int q = 0; q < 16; ++q) sm += f[q];
        sm = wave_sum(sm);
    const float mean = sm * (1.0f / D);
    float sq = 0.f;
#pragma unroll
    for (int q = 0; q < 16; ++q) { f[q] -= mean; sq += f[q] * f[q]; }
    sq = wave_sum(sq);
    const float rs = 1.0f / sqrtf(sq * (1.0f / D) + 1e-5f);
    stv = (tag << 32) | (u64)__float_as_uint(lane ? rs : mean);
  }
  if (g == 0) {
    if (type0 == 0) k1_group<0, 6>(lds0, c, perm, wi, lane, pm, col00, abT, h, b1, sy, tag, ct, stv, ppt);
    else k1_group<1, 6>(lds0, c, perm, wi, lane, pm, col00, abT, h, b1, sy, tag, ct, stv, ppt);
  } else {
    if (type1 == 0) k1_group<0, 5>(lds0, c, perm, wi, lane, pm, col01, abT, h, b1, sy, tag, ct, stv, ppt);
    else k1_group<1, 5>(lds0, c, perm, wi, lane, pm, col01, abT, h, b1, sy, tag, ct, stv, ppt);
  }
  const K2Issue c2 = k2_make(a2, lds0);
  WAIT_V(0);
  if (lane == 0) {
    const unsigned old = __hip_atomic_fetch_add((LAS unsigned*)(size_t)(lds0 + KM_CNT + g * 4), 1u, __ATOMIC_RELAXED, __HIP_MEMORY_SCOPE_WORKGROUP);
    if (old == 3u) {
      const int mytype = g == 0 ? type0 : type1;
      st_sc1(mytype == 0 ? sy + SY_D + (g == 0 ? ct : 13 + ct) * 8 + pm : sy + SY_M + pm * 48 + (g == 0 ? ct - 13 : 9 + ct), tag);
    }
  }
  if (bid == 0 && wid == 1) {
    gu64* pq = sy + SY_LN + lane;
#pragma unroll 1
    for (unsigned spins = 0; spins < (1u << 20); ++spins) {
      const u64 a0 = ld_sc1(pq), a1 = ld_sc1(pq + 64), a2_ = ld_sc1(pq + 128), a3 = ld_sc1(pq + 192);
      if (__builtin_amdgcn_ballot_w64(a0 != tag || a1 != tag || a2_ != tag || a3 != tag) == 0ull) break;
      __builtin_amdgcn_s_sleep(2);
    }
    if (lane == 0) st_sc1(sy + SY_EPOCH, tag);
  }
  auto wait_deps = [&]() {
    if (tRole) {
      const int eb = role * 8 + pn, lo = eb * 128, hi = lo + 127;
      const int hlo = lo < 1248 ? lo / 96 : 13 + (lo - 1248) / 80, hhi = hi < 1248 ? hi / 96 : 13 + (hi - 1248) / 80;
      wait_words(sy + SY_D + hlo * 8, (hhi - hlo + 1) * 8, tag, lane);
    } else wait_words(sy + SY_M + pm * 48 + (role == 2 ? 0 : 18), role == 2 ? 19 : 23, tag, lane);
  };
  const bool early = tRole && type1 == 1;
  if (early) {
    volatile LAS unsigned* go = (volatile LAS unsigned*)(size_t)(lds0 + KM_CNT + 8);
    if (wid == 0) { wait_deps(); *go = 1u; }
    else if (g == 0) {
#pragma unroll 1
      for (unsigned spins = 0; rfl(*go) == 0u && spins < (1u << 24); ++spins) __builtin_amdgcn_s_sleep(1);
    }
    asm volatile("" ::: "memory");
  } else {
    BAR();
    asm volatile("" ::: "memory");
    k2_pre(c2, g, tRole, 0); k2_pre(c2, g, tRole, 1);
    if (tRole && g == 1) k2_sm(c2, 2, K2_R2);
    if (wid == 0) wait_deps();
    BAR();
    asm volatile("" ::: "memory");
  }
  f32x4 acc[4][4];
  const u32x4 rxx = mksrd(x + (size_t)(pm * 256 + g * 128 + (wi >> 1) * 64 + role * 16) * D + pn * 128 + (wi & 1) * 64, 16u * D * 2u);
  const u32x4 rb2 = mksrd(b2 + pn * 128 + (wi & 1) * 64, 128u);
  unsigned xaddr;
  const __amdgpu_buffer_rsrc_t xw = mkrsrc(xbuf + (size_t)tile * (4 * 8 * 8 * 1024) + (role * 8 + wid) * 8192, 8192u);
  if (tRole) k2_loop<false>(lds0, c2, g, wi, lane, acc, early, rxx, rb2, xaddr, xw);
  else k2_loop<true>(lds0, c2, g, wi, lane, acc, false, rxx, rb2, xaddr, xw);
  const __amdgpu_buffer_rsrc_t xr = mkrsrc(xbuf + (size_t)tile * (4 * 8 * 8 * 1024), 4u * 8u * 8u * 1024u);
  const int fr = lane & 15, fq = lane >> 4, wmi = wi >> 1, wn = wi & 1;
  gu64* fl = sy + SY_X + (tile * 8 + wid) * 8;
  u64 f0 = ld_sc1(fl + (lane & 3));
  WAIT_V(0);
  if (lane == 0) st_sc1(fl + role, tag);
  asm volatile("" ::: "memory");
  u32x2 xv[4], bv[4];
#pragma unroll
  for (int n = 0; n < 4; ++n) { xv[n] = *(const LAS u32x2*)(size_t)(xaddr + (unsigned)(fr * 128 + fq * 8 + n * 32)); bv[n] = *(const LAS u32x2*)(size_t)(c2.mScr + (unsigned)(fq * 8 + n * 32)); }
  f32x4 yo[4], zo[4];
#pragma unroll
  for (int n = 0; n < 4; ++n) {
    const f32x4 bb = {bflo(bv[n][0]), bfhi(bv[n][0]), bflo(bv[n][1]), bfhi(bv[n][1])}, zero = {0.f, 0.f, 0.f, 0.f};
    yo[n] = role == 0 ? acc[3][n] : role == 1 ? -acc[3][n] : zero;
    zo[n] = tRole ? bb : bb + acc[3][n];
    asm volatile("" : "+v"(yo[n]), "+v"(zo[n]));
  }
  u32x4 pk[4][2];
#pragma unroll
  for (int r = 0; r < 4; ++r) { pk[r][0] = (u32x4){0u, 0u, 0u, 0u}; pk[r][1] = (u32x4){0u, 0u, 0u, 0u}; }
  unsigned have = 1u << role;
#pragma unroll 1
  for (unsigned spins = 0;;) {
    const unsigned rdy = (unsigned)__builtin_amdgcn_ballot_w64(f0 == tag) & 15u;
    const unsigned nw = rdy & ~have;
    asm volatile("" ::: "memory");
    have |= nw;
    if (have != 15u) f0 = ld_sc1(fl + (lane & 3));
#pragma unroll
    for (int r = 0; r < 4; ++r)
      if (nw & (1u << r)) {
#pragma unroll
        for (int pr = 0; pr < 2; ++pr) pk[r][pr] = __builtin_bit_cast(u32x4, __builtin_amdgcn_raw_buffer_load_b128(xr, lane * 16, ((r * 8 + wid) * 8 + ((role - r - 1) & 3) * 2 + pr) * 1024, 16));
      }
    if (have == 15u || ++spins > (1u << 20)) break;
    if (!nw) __builtin_amdgcn_s_sleep(1);
  }
  const __amdgpu_buffer_rsrc_t ro = mkrsrc(out + (size_t)(pm * 256 + g * 128 + wmi * 64 + role * 16) * D + pn * 128 + wn * 64, 16u * D * 2u);
  const unsigned vo = (unsigned)(fr * D + 16 * (fq & 1) + 8 * (fq >> 1)) * 2u;
#pragma unroll
  for (int pr = 0; pr < 2; ++pr) {
    unsigned w[2][2];
#pragma unroll
    for (int q = 0; q < 2; ++q) {
      const int n = 2 * pr + q;
      const float xb[4] = {bflo(xv[n][0]), bfhi(xv[n][0]), bflo(xv[n][1]), bfhi(xv[n][1])};
      float o[4];
#pragma unroll
      for (int e = 0; e < 4; ++e) {
        float p[4];
#pragma unroll
        for (int r = 0; r < 4; ++r) { const unsigned u = pk[r][pr][2 * q + (e >> 1)]; p[r] = (e & 1) ? bfhi(u) : bflo(u); }
        const float y = (p[0] - p[1]) + yo[n][e], z = (p[2] + p[3]) + zo[n][e];
        o[e] = xb[e] + y * (z * __builtin_amdgcn_rcpf(1.0f + __builtin_amdgcn_exp2f(-1.4426950408889634f * z)));
      }
      w[q][0] = cvtpk(o[0], o[1]); w[q][1] = cvtpk(o[2], o[3]);
    }
    auto r0 = __builtin_amdgcn_permlane16_swap(w[0][0], w[1][0], false, false);
    auto r1 = __builtin_amdgcn_permlane16_swap(w[0][1], w[1][1], false, false);
    u32x4 v; v[0] = r0[0]; v[1] = r1[0]; v[2] = r0[1]; v[3] = r1[1];
    __builtin_amdgcn_raw_buffer_store_b128(v, ro, vo + pr * 64, 0, 0);
  }
}

extern "C" void kernel_launch(void* const* d_in, const int* in_sizes, int n_in, void* d_out, int out_size, void* d_ws, size_t ws_size, hipStream_t stream) {
  const u16* x = (const u16*)d_in[0]; const u16* Cr = (const u16*)d_in[1]; const u16* Ci = (const u16*)d_in[2];
  const u16* Dr = (const u16*)d_in[3]; const u16* Di = (const u16*)d_in[4];
  const u16* w1 = (const u16*)d_in[6]; const u16* b1 = (const u16*)d_in[7]; const u16* w2 = (const u16*)d_in[8]; const u16* b2 = (const u16*)d_in[9];
  char* ws = (char*)d_ws;
  unsigned long long* sync = (unsigned long long*)ws;
  u16* abT = (u16*)(ws + (6 << 20));
  u16* h = (u16*)(ws + (18 << 20));
  unsigned char* xbuf = (unsigned char*)(ws + (40 << 20));
  (void)hipFuncSetAttribute((const void*)k_main, hipFuncAttributeMaxDynamicSharedMemorySize, KM_LDS);
  k_main<<<256, 512, KM_LDS, stream>>>(Dr, Di, w1, b1, abT, h, Cr, Ci, w2, x, b2, (u16*)d_out, xbuf, sync);
}
```
